```python
import torch

def _softmax(x, axis=-1):
    return torch.softmax(x, dim=axis)
def _gelu(x):
    return torch.nn.functional.gelu(x, approximate="tanh")
def _silu(x):
    return torch.nn.functional.silu(x)
def _sigmoid(x):
    return torch.sigmoid(x)
def _layernorm(x, axis=-1, eps=1e-5):
    m = x.mean(dim=axis, keepdim=True)
    v = x.var(dim=axis, keepdim=True, unbiased=False)
    return (x - m) / torch.sqrt(v + eps)
def _scatter_add(tgt, idx, src, axis=0):
    return tgt.clone().index_add_(axis, idx, src)
def _segment_sum(src, seg, n):
    out = torch.zeros((n,) + src.shape[1:], dtype=src.dtype)
    return out.index_add_(0, seg, src)
def _pad(x, pw):
    flat = tuple(v for pair in reversed(pw) for v in pair)
    return torch.nn.functional.pad(x, flat)

kernel_name = 'sequential_preLN__linear_attn_block__dense_activation_silu__prompt'

def setup_inputs():

    _w = (lambda a: (lambda t: t.to(torch.bfloat16) if t.is_floating_point() else t)(torch.as_tensor(a)))
    def _A_setup():
        B, S, D = 1, 2048, 640
        return {
            "x": _w((lambda *s: torch.randn(*s, dtype=torch.bfloat16) * 0.05)(B, S, D)),
            "wqkv": _w((lambda *s: torch.randn(*s, dtype=torch.bfloat16) * 0.05)(D, 3 * D)),
            "wo": _w((lambda *s: torch.randn(*s, dtype=torch.bfloat16) * 0.05)(D, D)),
        }
    _A_in = _A_setup()

    def _B_setup():
        B, S, D, F = 1, 2048, 640, 1728
        return {
            "x": _w((lambda *s: torch.randn(*s, dtype=torch.bfloat16) * 0.05)(B, S, D)),
            "w1": _w((lambda *s: torch.randn(*s, dtype=torch.bfloat16) * 0.8)(D, F) * (D ** -0.5)),
            "w2": _w((lambda *s: torch.randn(*s, dtype=torch.bfloat16) * 0.1)(F, D) * (F ** -0.5)),
        }
    _B_in = _B_setup()

    return {
        "A_x": _A_in["x"],
        "A_wqkv": _A_in["wqkv"],
        "A_wo": _A_in["wo"],
        "B_x": _B_in["x"],
        "B_w1": _B_in["w1"],
        "B_w2": _B_in["w2"],
    }

def reference(A_x, A_wqkv, A_wo, B_x, B_w1, B_w2):
    def _A_ref(x, wqkv, wo):
        B, S, D = x.shape
        H = 32; Dh = D // H
        qkv = (lambda x, *d: x.permute(*d))((x @ wqkv).reshape(B, S, 3, H, Dh), 2, 0, 3, 1, 4)
        q, k, v = qkv[0], qkv[1], qkv[2]
        phi = lambda t: torch.where(t > 0, t + 1, torch.exp(t)) + 1e-6
        pq, pk = phi(q), phi(k)
        kv = torch.einsum("bhsd,bhse->bhde", pk, v)
        z = torch.einsum("bhsd->bhd", pk)
        num = torch.einsum("bhqd,bhde->bhqe", pq, kv)
        den = torch.einsum("bhqd,bhd->bhq", pq, z)[..., None] + 1e-6
        o = num / den
        return (lambda x, *d: x.permute(*d))(o, 0, 2, 1, 3).reshape(B, S, D) @ wo

    def _B_ref(x, w1, w2):
        h = torch.einsum("bsd,df->bsf", x, w1)
        h = _silu(h)
        out = torch.einsum("bsf,fd->bsd", h, w2)
        return out

    x = A_x
    h = x + _A_ref(_layernorm(x, axis=-1), A_wqkv, A_wo)
    h2 = h + _B_ref(_layernorm(h, axis=-1), B_w1, B_w2)
    return h2
```

```cpp
#include <hip/hip_runtime.h>
#include <hip/hip_bf16.h>
typedef unsigned short u16;
__device__ __forceinline__ float bf2f(u16 b){ return __builtin_bit_cast(float, (unsigned)b<<16); }
__device__ __forceinline__ u16 f2bf(float f){ __hip_bfloat16 h = __float2bfloat16(f); return __builtin_bit_cast(u16,h);}
constexpr int S=2048, D=640, F=1728, H=32, DH=20;

__global__ void ln_k(const u16* x, u16* y){
  int row=blockIdx.x; int t=threadIdx.x;
  float v[10]; float s=0;
  for(int i=0;i<10;i++){ v[i]=bf2f(x[row*D+t+64*i]); s+=v[i]; }
  for(int o=32;o;o>>=1) s+=__shfl_xor(s,o);
  float m=s/D; float q=0;
  for(int i=0;i<10;i++){ float d=v[i]-m; q+=d*d; }
  for(int o=32;o;o>>=1) q+=__shfl_xor(q,o);
  float r=rsqrtf(q/D+1e-5f);
  for(int i=0;i<10;i++) y[row*D+t+64*i]=f2bf((v[i]-m)*r);
}
template<int MODE>
__global__ void gemm_k(const u16* A, const u16* B, const u16* R, u16* C, int M,int N,int K){
  __shared__ float sa[16][17], sb[16][17];
  int tx=threadIdx.x, ty=threadIdx.y; int row=blockIdx.y*16+ty, col=blockIdx.x*16+tx;
  float acc=0;
  for(int k0=0;k0<K;k0+=16){
    sa[ty][tx]=bf2f(A[(size_t)row*K+k0+tx]); sb[ty][tx]=bf2f(B[(size_t)(k0+ty)*N+col]);
    __syncthreads();
    for(int k=0;k<16;k++) acc+=sa[ty][k]*sb[k][tx];
    __syncthreads();
  }
  if(MODE==1) acc=bf2f(f2bf(acc))+bf2f(R[(size_t)row*N+col]);
  if(MODE==2) { acc=bf2f(f2bf(acc)); acc=acc/(1.f+__expf(-acc)); }
  C[(size_t)row*N+col]=f2bf(acc);
}
__device__ __forceinline__ float phi(float t){ return (t>0.f? t+1.f: __expf(t))+1e-6f; }
__global__ void kv_k(const u16* qkv, float* kv){
  int h=blockIdx.x, t=threadIdx.x; if(t>=420) return; int d=t/21,e=t%21;
  float acc=0;
  for(int s=0;s<S;s++){ float pk=phi(bf2f(qkv[(size_t)s*1920+640+h*20+d])); float v= e<20? bf2f(qkv[(size_t)s*1920+1280+h*20+e]):1.f; acc+=pk*v; }
  kv[h*420+t]=acc;
}
__global__ void o_k(const u16* qkv, const float* kv, u16* o){
  int s=blockIdx.x, t=threadIdx.x, h=t/20, e=t%20;
  float num=0, den=0;
  for(int d=0;d<20;d++){ float pq=phi(bf2f(qkv[(size_t)s*1920+h*20+d])); num+=pq*kv[h*420+d*21+e]; den+=pq*kv[h*420+d*21+20]; }
  o[(size_t)s*D+t]=f2bf(num/(den+1e-6f));
}
extern "C" void kernel_launch(void* const* d_in, const int* in_sizes, int n_in, void* d_out, int out_size, void* d_ws, size_t ws_size, hipStream_t stream){
  const u16* x=(const u16*)d_in[0]; const u16* wqkv=(const u16*)d_in[1]; const u16* wo=(const u16*)d_in[2];
  const u16* w1=(const u16*)d_in[4]; const u16* w2=(const u16*)d_in[5];
  char* ws=(char*)d_ws;
  u16* ln=(u16*)ws; u16* qkv=(u16*)(ws+(4<<20)); float* kv=(float*)(ws+(16<<20)); u16* o=(u16*)(ws+(20<<20)); u16* h=(u16*)(ws+(24<<20)); u16* f=(u16*)(ws+(32<<20));
  ln_k<<<S,64,0,stream>>>(x,ln);
  gemm_k<0><<<dim3(1920/16,S/16),dim3(16,16),0,stream>>>(ln,wqkv,nullptr,qkv,S,1920,D);
  kv_k<<<H,448,0,stream>>>(qkv,kv);
  o_k<<<S,640,0,stream>>>(qkv,kv,o);
  gemm_k<1><<<dim3(D/16,S/16),dim3(16,16),0,stream>>>(o,wo,x,h,S,D,D);
  ln_k<<<S,64,0,stream>>>(h,ln);
  gemm_k<2><<<dim3(F/16,S/16),dim3(16,16),0,stream>>>(ln,w1,nullptr,f,S,F,D);
  gemm_k<1><<<dim3(D/16,S/16),dim3(16,16),0,stream>>>(f,w2,h,(u16*)d_out,S,D,F);
}
```

```cpp
#include <hip/hip_runtime.h>
#include <stdint.h>

typedef unsigned v4u __attribute__((ext_vector_type(4)));
typedef unsigned v2u __attribute__((ext_vector_type(2)));
typedef short v4s __attribute__((ext_vector_type(4)));
typedef float f32x4 __attribute__((ext_vector_type(4)));
typedef float f32x2 __attribute__((ext_vector_type(2)));
typedef __bf16 bf16x2 __attribute__((ext_vector_type(2)));
typedef __bf16 bf16x8 __attribute__((ext_vector_type(8)));
typedef unsigned long long u64;
#define LAS __attribute__((address_space(3)))
#define GAS __attribute__((address_space(1)))
#define DI __device__ __forceinline__
#define RLX_AGENT __ATOMIC_RELAXED, __HIP_MEMORY_SCOPE_AGENT
#define RLX_WG __ATOMIC_RELAXED, __HIP_MEMORY_SCOPE_WORKGROUP
#define SCHED() __builtin_amdgcn_sched_barrier(0)
#define CFENCE() asm volatile("" ::: "memory")
#define DRAIN() asm volatile("s_waitcnt vmcnt(0)" ::: "memory")
#define LGKM0() asm volatile("s_waitcnt lgkmcnt(0)" ::: "memory")
constexpr int SC1 = 16;

constexpr int OFF_EPOCH = 0;
constexpr int OFF_FLAGS = 4096;
constexpr int OFF_CS = 262144;
constexpr int OFF_HS = 327680;
constexpr int OFF_PROF = 1536 * 1024;
constexpr int OFF_KVP = 524288;
constexpr int KVP_STR = 3456;
constexpr int OFF_XN = 2 << 20;
constexpr int OFF_WA = OFF_XN + 2621440;
constexpr int OFF_WB = OFF_WA + 2621440;
constexpr int OFF_WC = OFF_WB + 819200;
constexpr int OFF_WD = OFF_WC + 2293760;
constexpr int OFF_O = OFF_WD + 2211840;
constexpr int OFF_H = OFF_O + 2621440;
constexpr int OFF_F = OFF_H + 2621440;
constexpr int WS_BYTES = OFF_F + 7077888;
enum { FL0 = 0, FLR = 1, FL1 = 2, FL2 = 3, FL3 = 4, FL4 = 5 };
constexpr u64 EPOCH_STEP = 0x9E3779B97F4A7C15ull;

constexpr int RING = 130;
constexpr int L_SCR = RING * 1024;
constexpr int L_KVS = L_SCR + 20480;
constexpr int L_CSUM = L_KVS + 3456;
constexpr int L_DONE = L_CSUM + 512;
constexpr int L_CTL = L_DONE + 1024;
constexpr int LDS_BYTES = L_CTL + 256;
static_assert(LDS_BYTES <= 163840, "lds");
constexpr int L_STG2 = 100 * 1024;
enum { C_LANDED = 0, C_ARR0, C_ARRR, C_ARR1, C_GO1, C_BAR1, C_ARR2, C_GO2, C_XB0, C_XB1, C_ARR3, C_GO3, C_ARR4, C_GO4, C_XD0, C_XD1, C_CS, C_GO0 };

constexpr int V_A1 = 0, V_A2 = 100, V_B = 160, V_C = 260, V_D = 400, V_END = 670;

struct Ctx {
  __amdgpu_buffer_rsrc_t rs;
  GAS char* ws;
  LAS char* L;
  u64 ep;
  int lane;
};

DI f32x4 mfma(v4u a, v4u b, f32x4 c) {
  return __builtin_amdgcn_mfma_f32_16x16x32_bf16(__builtin_bit_cast(bf16x8, a), __builtin_bit_cast(bf16x8, b), c, 0, 0, 0);
}
DI unsigned pk2(float a, float b) { f32x2 f = {a, b}; return __builtin_bit_cast(unsigned, __builtin_convertvector(f, bf16x2)); }
DI float bflo(unsigned u) { return __builtin_bit_cast(float, u << 16); }
DI float bfhi(unsigned u) { return __builtin_bit_cast(float, u & 0xffff0000u); }
DI v4u ld128(const Ctx& c, int voff, int soff) { return __builtin_amdgcn_raw_buffer_load_b128(c.rs, voff, soff, SC1); }
DI void st128(const Ctx& c, v4u v, int voff, int soff) { __builtin_amdgcn_raw_buffer_store_b128(v, c.rs, voff + soff, 0, SC1); }
DI void st64(const Ctx& c, v2u v, int voff, int soff) { __builtin_amdgcn_raw_buffer_store_b64(v, c.rs, voff, soff, SC1); }
DI void st32(const Ctx& c, unsigned v, int voff, int soff) { __builtin_amdgcn_raw_buffer_store_b32(v, c.rs, voff, soff, SC1); }
DI u64 now() { return __builtin_amdgcn_s_memrealtime(); }
DI volatile LAS unsigned* ctl(const Ctx& c, int i) { return (volatile LAS unsigned*)(c.L + L_CTL) + i; }
DI unsigned ctl_read(const Ctx& c, int i) { return __builtin_amdgcn_readfirstlane(*ctl(c, i)); }
DI void ctl_wait(const Ctx& c, int i, unsigned v) {
  unsigned n = 0;
  while (ctl_read(c, i) < v && ++n < (1u << 20)) {}
  CFENCE();
}
DI unsigned ctl_arrive(const Ctx& c, int i) {
  LGKM0();
  unsigned old = 0;
  if (c.lane == 0) old = __hip_atomic_fetch_add((LAS unsigned*)(c.L + L_CTL) + i, 1u, RLX_WG);
  old = __builtin_amdgcn_readfirstlane(old);
  CFENCE();
  return old;
}
DI void flag_set(const Ctx& c, int kind, int idx) {
  if (c.lane == 0) __hip_atomic_store((GAS u64*)(c.ws + OFF_FLAGS + (kind * 256 + idx) * 128), c.ep, RLX_AGENT);
}
DI void flag_wait(const Ctx& c, int kind, int idx) {
  GAS u64* p = (GAS u64*)(c.ws + OFF_FLAGS + (kind * 256 + (idx < 0 ? 0 : idx)) * 128);
  unsigned n = 0;
  for (;;) {
    u64 v = c.ep;
    if (idx >= 0) v = __hip_atomic_load(p, RLX_AGENT);
    if (__all(v == c.ep)) break;
    if (++n > (1u << 17)) break;
  }
  CFENCE();
}
#define STAMP(i) do {} while (0)

DI float dpp_sum16(float v) {
  v += __builtin_bit_cast(float, __builtin_amdgcn_update_dpp(0, __builtin_bit_cast(int, v), 0x128, 0xf, 0xf, false));
  v += __builtin_bit_cast(float, __builtin_amdgcn_update_dpp(0, __builtin_bit_cast(int, v), 0x124, 0xf, 0xf, false));
  v += __builtin_bit_cast(float, __builtin_amdgcn_update_dpp(0, __builtin_bit_cast(int, v), 0x122, 0xf, 0xf, false));
  v += __builtin_bit_cast(float, __builtin_amdgcn_update_dpp(0, __builtin_bit_cast(int, v), 0x121, 0xf, 0xf, false));
  return v;
}
DI float sum_g(float v) {
  v += __shfl_xor(v, 16);
  v += __shfl_xor(v, 32);
  return v;
}

DI int gam0(int j) { return 27 * (j >> 2) + 7 * (j & 3); }
template <int KIND> DI int kmap(int s, int g, int j) {
  if (KIND == 0) return 32 * s + 8 * g + j;
  if (KIND == 1) {
    int p, t;
    if (s < 16) { p = s; t = j; } else { p = 4 * (s - 16) + (j >> 1); t = 8 + (j & 1); }
    return 40 * p + (t >= 5 ? 20 + (t - 5) : t) + 5 * g;
  }
  if (KIND == 2) {
    int u = s / 5, v = s - 5 * u;
    if (v < 2) return 160 * u + 32 * v + 8 * g + j;
    if (v == 2) return 160 * u + 64 + 4 * g + (j & 3) + (j >> 2) * 80;
    return 160 * u + 80 + 32 * (v - 3) + 8 * g + j;
  }
  int gam;
  if (s < 48) { int jj = s / 3, v = s - 3 * jj; gam = gam0(jj) + 2 * v + (j >> 2); }
  else { int l = 2 * (s - 48) + (j >> 2); int q = l / 3; gam = gam0(4 * q + (l - 3 * q)) + 6; }
  return 16 * gam + 4 * g + (j & 3);
}
template <int KIND> DI int sglob(int rb, int sp) {
  if (KIND == 1) return sp < 4 ? 4 * rb + sp : 16 + rb;
  if (KIND == 3) return 6 * rb + sp;
  return 5 * rb + sp;
}

template <int KIND> DI void repack_issue(const Ctx& c, const char* W, int rb, int cb, int ww, int stg) {
  constexpr int NS = KIND == 3 ? 6 : 5;
  constexpr int PITCH = KIND == 0 ? 3840 : KIND == 2 ? 3456 : 1280;
  const int j = c.lane >> 3, ch = c.lane & 7;
#pragma unroll
  for (int sp = 0; sp < NS; ++sp) {
    const int row = kmap<KIND>(sglob<KIND>(rb, sp), ww, j);
    __builtin_amdgcn_global_load_lds((const GAS unsigned*)(W + (size_t)row * PITCH + cb * 128 + ch * 16),
                                     (LAS unsigned*)(c.L + stg + (sp * 4 + ww) * 1024), 16, 0, 0);
  }
}
template <int KIND> DI void repack_emit(const Ctx& c, int rb, int cb, int ww, int stg) {
  constexpr int NS = KIND == 3 ? 6 : 5;
  const int l = c.lane, n = l & 15, g = l >> 4, q = (l >> 2) & 3, p = l & 3;
  const int gam = 4 * cb + ww;
  int sig = p, base, fstep;
  int cq = 0;
  if (KIND == 0) {
    const int C = 16 * gam + n, type = C / 640, cc = C - 640 * type, pp = cc / 40, u0 = cc - 40 * pp;
    const int u = (type == 1 ? 0 : type == 2 ? 40 : 80) + u0, tile = u >> 4, slot = u & 15;
    base = OFF_WA + pp * 163840 + (tile < 5 ? tile : 100 + tile - 5) * 1024 + (g * 16 + slot) * 16;
    fstep = tile < 5 ? 5 : 3;
  } else if (KIND == 2) {
    const int grp = gam / 27, r = gam - 27 * grp, jj = min(r / 7, 3), t = r - 7 * jj;
    base = OFF_WC + (4 * grp + jj) * 143360 + t * 1024 + l * 16;
    fstep = 7;
  } else {
    const int cc = gam / 5;
    cq = gam - 5 * cc;
    int tile = 4, slot = n;
    if (cq < 4) { sig = ((p & 1) << 1) | (p >> 1); tile = 2 * (cq >> 1) + (n >> 3); slot = 8 * (cq & 1) + (n & 7); }
    base = (KIND == 1 ? OFF_WB + cc * 102400 : OFF_WD + cc * 276480) + tile * 1024 + (g * 16 + slot) * 16;
    fstep = 10;
  }
  LAS char* src = c.L + stg + (8 * g + q) * 128 + 32 * ww + 8 * sig;
  float cs = 0.f;
#pragma unroll
  for (int sp = 0; sp < NS; ++sp) {
    const v4s lo = __builtin_amdgcn_ds_read_tr16_b64_v4i16((LAS v4s*)(src + sp * 4096));
    const v4s hi = __builtin_amdgcn_ds_read_tr16_b64_v4i16((LAS v4s*)(src + sp * 4096 + 512));
    const v2u a = __builtin_bit_cast(v2u, lo), b = __builtin_bit_cast(v2u, hi);
    const v4u v = {a.x, a.y, b.x, b.y};
    const int s = sglob<KIND>(rb, sp);
    int off;
    if (KIND == 1) off = (s >= 10 ? (s - 10) * 10 + 5 : s * 10) * 1024;
    else if (KIND == 3) off = (s >= 27 ? (s - 27) * 10 + 5 : s * 10) * 1024;
    else off = s * fstep * 1024;
    st128(c, v, base + off, 0);
    if (KIND == 2) {
      cs += (bflo(v.x) + bfhi(v.x)) + (bflo(v.y) + bfhi(v.y)) + (bflo(v.z) + bfhi(v.z)) + (bflo(v.w) + bfhi(v.w));
    }
  }
  if (KIND == 2) {
    cs = sum_g(cs);
    if (g == 0) st32(c, __builtin_bit_cast(unsigned, cs), OFF_CS + (rb * 1792 + 16 * gam + n) * 4, 0);
  }
}

struct Ring { unsigned landed; unsigned budget; unsigned laddr; };
template <int POS> DI void ring_need(Ring& r) {
  unsigned tmp;
  asm volatile(
      "s_cmpk_lt_u32 %0, %4\n\t"
      "s_cbranch_scc1 .Lslow%=\n"
      ".Lback%=:\n\t"
      ".subsection 1\n"
      ".Lslow%=:\n\t"
      "ds_read_b32 %2, %3\n\t"
      "s_sub_u32 %1, %1, 1\n\t"
      "s_cbranch_scc1 .Lgive%=\n\t"
      "s_waitcnt lgkmcnt(0)\n\t"
      "v_readfirstlane_b32 %0, %2\n\t"
      "s_cmpk_lt_u32 %0, %4\n\t"
      "s_cbranch_scc1 .Lslow%=\n\t"
      "s_branch .Lback%=\n"
      ".Lgive%=:\n\t"
      "s_waitcnt lgkmcnt(0)\n\t"
      "s_movk_i32 %0, 0x7fff\n\t"
      "s_branch .Lback%=\n\t"
      ".subsection 0\n"
      : "+s"(r.landed), "+s"(r.budget), "=&v"(tmp) : "v"(r.laddr), "i"(POS) : "memory", "scc");
}
DI void ring_done(const Ctx& c, int w, unsigned pos) { *(volatile LAS unsigned*)(c.L + L_DONE + w * 256 + c.lane * 4) = pos; }

template <int I> struct IC { static constexpr int v = I; };
template <int B, int E, class Fn> DI void static_for(Fn&& fn) {
  if constexpr (B < E) { fn(IC<B>{}); static_for<B + 1, E>(fn); }
}
struct WB { unsigned b0, b1, b2; };
DI WB make_wb(unsigned base) {
  WB r; r.b0 = base; r.b1 = base + 65536u; r.b2 = base + 131072u;
  asm volatile("" : "+v"(r.b0), "+v"(r.b1), "+v"(r.b2));
  return r;
}
template <int P> DI v4u wread(const WB& wb) {
  constexpr int q = P % RING;
  if constexpr (q < 64) return *(LAS v4u*)(uintptr_t)(wb.b0 + q * 1024);
  else if constexpr (q < 128) return *(LAS v4u*)(uintptr_t)(wb.b1 + (q - 64) * 1024);
  else return *(LAS v4u*)(uintptr_t)(wb.b2 + (q - 128) * 1024);
}
template <int NT, int NKS, int FSTEP, int V0, int TL, int XW, bool ZERO>
DI void gemm_pass(const Ctx& c, Ring& r, int w, const WB& wb, f32x4 (&acc)[2][NT], v4u (&x)[2][XW], int xvoff, int xsoff, int xmt) {
  constexpr int LA = 5, WBN = LA + 1, NF = NKS * NT;
  v4u wf[WBN];
  if constexpr (!ZERO) {
#pragma unroll
    for (int t = 0; t < NT; ++t) asm volatile("" : "+v"(acc[0][t]), "+v"(acc[1][t]));
  }
  SCHED();
  static_for<-LA, NF>([&](auto ic) __attribute__((always_inline)) {
    constexpr int f = decltype(ic)::v;
    constexpr int fn = f + LA;
    if constexpr (fn < NF) {
      constexpr int pos = V0 + (fn / NT) * FSTEP + fn % NT;
      constexpr int prev = fn > 0 ? V0 + ((fn - 1) / NT) * FSTEP + (fn - 1) % NT : -10;
      if constexpr (pos / 10 != prev / 10 || fn == 0) ring_need<(pos / 10 + 1) * 10>(r);
      wf[fn % WBN] = wread<pos>(wb);
    }
    if constexpr (f == -1) { SCHED(); asm volatile("s_nop 1"); }
    if constexpr (f >= 0) {
      constexpr int s = f / NT, t = f % NT;
      SCHED();
      if constexpr (ZERO && s == 0 && TL)
        asm volatile("v_mfma_f32_16x16x32_bf16 %0, %2, %3, 0\n\tv_mfma_f32_16x16x32_bf16 %1, %2, %4, 0"
                     : "=&v"(acc[0][t]), "=&v"(acc[1][t]) : "v"(wf[f % WBN]), "v"(x[0][s % XW]), "v"(x[1][s % XW]));
      else if constexpr (ZERO && s == 0)
        asm volatile("v_mfma_f32_16x16x32_bf16 %0, %3, %2, 0\n\tv_mfma_f32_16x16x32_bf16 %1, %4, %2, 0"
                     : "=&v"(acc[0][t]), "=&v"(acc[1][t]) : "v"(wf[f % WBN]), "v"(x[0][s % XW]), "v"(x[1][s % XW]));
      else if constexpr (TL)
        asm volatile("v_mfma_f32_16x16x32_bf16 %0, %2, %3, %0\n\tv_mfma_f32_16x16x32_bf16 %1, %2, %4, %1"
                     : "+v"(acc[0][t]), "+v"(acc[1][t]) : "v"(wf[f % WBN]), "v"(x[0][s % XW]), "v"(x[1][s % XW]));
      else
        asm volatile("v_mfma_f32_16x16x32_bf16 %0, %3, %2, %0\n\tv_mfma_f32_16x16x32_bf16 %1, %4, %2, %1"
                     : "+v"(acc[0][t]), "+v"(acc[1][t]) : "v"(wf[f % WBN]), "v"(x[0][s % XW]), "v"(x[1][s % XW]));
      SCHED();
      if constexpr (t == NT - 1 && s + XW < NKS) {
        x[0][s % XW] = ld128(c, xvoff, xsoff + (s + XW) * 1024);
        x[1][s % XW] = ld128(c, xvoff, xsoff + xmt + (s + XW) * 1024);
      }
      constexpr int pos = V0 + s * FSTEP + t;
      constexpr int nxt = V0 + ((f + 1) / NT) * FSTEP + (f + 1) % NT;
      if constexpr (f + 1 == NF || nxt / 10 != pos / 10) ring_done(c, w, (pos / 10 + 1) * 10);
    }
  });
  SCHED();
  asm volatile("s_nop 7");
  SCHED();
}
template <int XW> DI void xload(const Ctx& c, v4u (&x)[2][XW], int xvoff, int xsoff, int xmt) {
#pragma unroll
  for (int s = 0; s < XW; ++s) { x[0][s] = ld128(c, xvoff, xsoff + s * 1024); x[1][s] = ld128(c, xvoff, xsoff + xmt + s * 1024); }
  SCHED();
}
DI float phi(float t) { return (t > 0.f ? t + 1.f : __expf(t)) + 1e-6f; }

DI void panel_sync(const Ctx& c, int kind, int carr, int cgo, int self, int pollidx) {
  DRAIN();
  if (ctl_arrive(c, carr) == 3u) {
    flag_set(c, kind, self);
    flag_wait(c, kind, pollidx);
    *ctl(c, cgo) = 1u;
  } else {
    ctl_wait(c, cgo, 1u);
  }
}
DI void pair_reduce(const Ctx& c, int w, int cx, f32x4 (&acc)[2][5], f32x4 (&o)[5]) {
  const int b = w & 1;
  LAS f32x4* mine = (LAS f32x4*)(c.L + L_SCR + w * 5120 + c.lane * 16);
  LAS f32x4* other = (LAS f32x4*)(c.L + L_SCR + (w ^ 1) * 5120 + c.lane * 16);
#pragma unroll
  for (int t = 0; t < 5; ++t) { mine[t * 64] = b ? acc[0][t] : acc[1][t]; o[t] = b ? acc[1][t] : acc[0][t]; }
  ctl_arrive(c, cx + (w >> 1));
  ctl_wait(c, cx + (w >> 1), 2u);
#pragma unroll
  for (int t = 0; t < 5; ++t) o[t] += other[t * 64];
}

DI void dma4(const Ctx& c, int voff, int soff, int ldsoff, int n) {
  asm volatile("s_mov_b32 m0, %2\n\ts_nop 0\n\tbuffer_load_dwordx4 %0, %1, %3 offen sc1 lds\n\tbuffer_load_dwordx4 %0, %1, %3 offen offset:1024 sc1 lds"
               :: "v"(voff), "s"(c.rs), "s"(ldsoff), "s"(soff) : "memory");
  if (n > 2)
    asm volatile("s_mov_b32 m0, %2\n\ts_nop 0\n\tbuffer_load_dwordx4 %0, %1, %3 offen offset:2048 sc1 lds\n\tbuffer_load_dwordx4 %0, %1, %3 offen offset:3072 sc1 lds"
                 :: "v"(voff), "s"(c.rs), "s"(ldsoff), "s"(soff) : "memory");
}

__global__ void __launch_bounds__(512, 2)
mk_kernel(const char* __restrict__ x, const char* __restrict__ wqkv, const char* __restrict__ wo, const char* __restrict__ w1,
          const char* __restrict__ w2, char* __restrict__ out, char* ws) {
  extern __shared__ __attribute__((aligned(16))) char smem[];
  Ctx c;
  c.L = (LAS char*)smem;
  c.ws = (GAS char*)ws;
  c.rs = __builtin_amdgcn_make_buffer_rsrc((void*)ws, 0, WS_BYTES, 0x00020000);
  const int tid = threadIdx.x, lane = tid & 63, w = __builtin_amdgcn_readfirstlane(tid >> 6);
  c.lane = lane;
  const int b = blockIdx.x, xc = b & 7, sx = b >> 3;
  const int pi = 4 * (xc >> 1) + (sx >> 3), pj = (xc & 1) * 8 + (sx & 7);
  const int hp = 8 * (xc >> 1) + (sx >> 2), cc = (xc & 1) * 4 + (sx & 3);
  const int n16 = lane & 15, g = lane >> 4;
  if (w == 0) STAMP(0);
  const u64 e0 = __hip_atomic_load((GAS u64*)(c.ws + OFF_EPOCH), RLX_AGENT);

  if (w == 7) {
    if (lane < 32) *ctl(c, lane) = 0u;
#pragma unroll
    for (int i = 0; i < 4; ++i) *(LAS unsigned*)(c.L + L_DONE + i * 256 + lane * 4) = 0u;
  }
  const int ww = w & 3;
  if (w < 4) {
    if (b < 128) {
      const int row = 4 * w + g;
      const GAS v4u* xp = (const GAS v4u*)(x + (size_t)(16 * b + row) * 1280 + n16 * 16);
      v4u v[5];
#pragma unroll
      for (int t = 0; t < 5; ++t) v[t] = xp[t * 16];
      float f[40];
      float s = 0.f;
#pragma unroll
      for (int t = 0; t < 5; ++t)
#pragma unroll
        for (int i = 0; i < 4; ++i) { f[t * 8 + 2 * i] = bflo(v[t][i]); f[t * 8 + 2 * i + 1] = bfhi(v[t][i]); s += f[t * 8 + 2 * i] + f[t * 8 + 2 * i + 1]; }
      const float mean = dpp_sum16(s) * (1.f / 640.f);
      float q = 0.f;
#pragma unroll
      for (int i = 0; i < 40; ++i) { f[i] -= mean; q += f[i] * f[i]; }
      const float rstd = rsqrtf(dpp_sum16(q) * (1.f / 640.f) + 1e-5f);
#pragma unroll
      for (int t = 0; t < 5; ++t) {
        v4u o;
#pragma unroll
        for (int i = 0; i < 4; ++i) o[i] = pk2(f[t * 8 + 2 * i] * rstd, f[t * 8 + 2 * i + 1] * rstd);
        *(LAS v4u*)(c.L + (n16 + 16 * t) * 272 + row * 16) = o;
      }
    } else if (b < 248) {
      repack_issue<0>(c, wqkv, (b - 128) / 30, (b - 128) % 30, ww, 0);
    }
  } else {
    if (b < 40) repack_issue<1>(c, wo, b / 10, b % 10, ww, L_STG2);
    else if (b < 148) repack_issue<2>(c, w1, (b - 40) / 27, (b - 40) % 27, ww, L_STG2);
    else if (b < 238) repack_issue<3>(c, w2, (b - 148) / 10, (b - 148) % 10, ww, L_STG2);
  }
  {
    const unsigned lo = __builtin_amdgcn_readfirstlane((unsigned)e0), hi = __builtin_amdgcn_readfirstlane((unsigned)(e0 >> 32));
    c.ep = (((u64)hi << 32) | lo) + EPOCH_STEP;
  }
  CFENCE();
  __builtin_amdgcn_s_waitcnt(0);
  __builtin_amdgcn_s_barrier();
  CFENCE();
  if (w == 0) STAMP(1);

  if (w >= 4) {
    if (b < 40) repack_emit<1>(c, b / 10, b % 10, ww, L_STG2);
    else if (b < 148) repack_emit<2>(c, (b - 40) / 27, (b - 40) % 27, ww, L_STG2);
    else if (b < 238) repack_emit<3>(c, (b - 148) / 10, (b - 148) % 10, ww, L_STG2);
    DRAIN();
    if (ctl_arrive(c, C_ARRR) == 3u) flag_set(c, FLR, b);
    if (w >= 6) return;
    if (w == 5) {
      ctl_wait(c, C_ARR1, 4u);
#pragma unroll
      for (int i = 0; i < 4; ++i) {
        const int it = i * 64 + lane;
        if (it < 210) {
          const LAS f32x4* p = (const LAS f32x4*)(c.L + L_SCR + it * 16);
          const f32x4 v = (p[0] + p[216]) + (p[432] + p[648]);
          st128(c, __builtin_bit_cast(v4u, v), it * 16, OFF_KVP + (pj * 16 + pi) * KVP_STR);
        }
      }
      DRAIN();
      flag_set(c, FL1, pj * 16 + pi);
      flag_wait(c, FL1, lane < 16 ? pj * 16 + lane : -1);
      *ctl(c, C_GO1) = 1u;
      {
        const int g0 = gam0(pj), cb0 = g0 >> 2;
        int idx = -1;
        if (lane < 12) { const int cb = cb0 + (lane >> 2); if (cb < 27 && 4 * cb <= g0 + 6) idx = 40 + (lane & 3) * 27 + cb; }
        flag_wait(c, FLR, idx);
        if (lane < 28) {
          f32x4 s = {0.f, 0.f, 0.f, 0.f};
#pragma unroll
          for (int rb = 0; rb < 4; ++rb) s += __builtin_bit_cast(f32x4, ld128(c, lane * 16, OFF_CS + (rb * 1792 + 16 * g0) * 4));
          *(LAS f32x4*)(c.L + L_CSUM + lane * 16) = s;
        }
        LGKM0();
        *ctl(c, C_CS) = 1u;
      }
      return;
    }
    {
      ctl_wait(c, C_ARR0, 4u);
      ctl_wait(c, C_ARRR, 4u);
      {
        int idx = -1;
        if (lane < 24) {
          const int type = lane >> 3, hi = (lane >> 2) & 1, rb = lane & 3;
          const int c0 = type * 640 + 40 * pj, cb = (hi ? c0 + 39 : c0) >> 6;
          idx = 128 + rb * 30 + cb;
        }
        flag_wait(c, FL0, idx);
      }
      STAMP(16);
      const int voff = lane * 16;
      int issued = 0;
      int pub = 0;
      int ro = 0;
      for (int ch = 0; ch < 67; ++ch) {
        if (ch == 16) {
          int idx = -1;
          if (lane < 8) idx = (lane & 3) * 10 + ((80 * cc + (lane >> 2) * 79) >> 6);
          else if (lane < 20) { const int g0 = gam0(pj), cb = (g0 >> 2) + ((lane - 8) >> 2); if (cb < 27 && 4 * cb <= g0 + 6) idx = 40 + (lane & 3) * 27 + cb; }
          else if (lane < 38) { const int l = lane - 20; idx = 148 + (l >> 1) * 10 + ((80 * cc + (l & 1) * 79) >> 6); }
          DRAIN();
          pub = issued; *ctl(c, C_LANDED) = pub * 10;
          flag_wait(c, FLR, idx);
        }
        if (ch >= 13) {
          const unsigned need = (unsigned)(ch - 12) * 10u;
          unsigned n = 0;
          for (;;) {
            const unsigned d = *(volatile LAS unsigned*)(c.L + L_DONE + (lane & 3) * 256);
            if (__all(d >= need)) break;
            const int fl = issued - pub;
            if (fl >= 4) asm volatile("s_waitcnt vmcnt(30)" ::: "memory");
            else if (fl == 3) asm volatile("s_waitcnt vmcnt(20)" ::: "memory");
            else if (fl == 2) asm volatile("s_waitcnt vmcnt(10)" ::: "memory");
            else if (fl == 1) asm volatile("s_waitcnt vmcnt(0)" ::: "memory");
            if (fl > 0) { ++pub; *ctl(c, C_LANDED) = pub * 10; }
            if (++n > (1u << 20)) break;
          }
          CFENCE();
        }
        int soff;
        if (ch < 16) soff = OFF_WA + pj * 163840 + ch * 10240;
        else if (ch < 26) soff = OFF_WB + cc * 102400 + (ch - 16) * 10240;
        else if (ch < 40) soff = OFF_WC + pj * 143360 + (ch - 26) * 10240;
        else soff = OFF_WD + cc * 276480 + (ch - 40) * 10240;
        const int lo = ro * 10240;
        dma4(c, voff, soff, lo, 4);
        dma4(c, voff, soff + 4096, lo + 4096, 4);
        dma4(c, voff, soff + 8192, lo + 8192, 2);
        ++issued;
        ro = ro == 12 ? 0 : ro + 1;
        if (issued - pub > 4) { asm volatile("s_waitcnt vmcnt(40)" ::: "memory"); ++pub; *ctl(c, C_LANDED) = pub * 10; }
      }
      asm volatile("s_waitcnt vmcnt(30)" ::: "memory"); *ctl(c, C_LANDED) = max(pub, 64) * 10;
      asm volatile("s_waitcnt vmcnt(20)" ::: "memory"); *ctl(c, C_LANDED) = max(pub, 65) * 10;
      asm volatile("s_waitcnt vmcnt(10)" ::: "memory"); *ctl(c, C_LANDED) = max(pub, 66) * 10;
      asm volatile("s_waitcnt vmcnt(0)" ::: "memory"); *ctl(c, C_LANDED) = 670;
      return;
    }
  }

  if (b < 128) {
#pragma unroll
    for (int i = 0; i < 5; ++i) {
      const int s = 5 * w + i;
      const v4u v = *(LAS v4u*)(c.L + (4 * s + g) * 272 + n16 * 16);
      st128(c, v, lane * 16, OFF_XN + (b * 20 + s) * 1024);
    }
  } else if (b < 248) {
    repack_emit<0>(c, (b - 128) / 30, (b - 128) % 30, ww, 0);
  }
  DRAIN();
  if (w == 0) STAMP(2);
  if (ctl_arrive(c, C_ARR0) == 3u) {
    flag_set(c, FL0, b);
    flag_wait(c, FL0, lane < 8 ? 8 * pi + lane : -1);
    *ctl(c, C_GO0) = 1u;
  } else ctl_wait(c, C_GO0, 1u);
  if (w == 0) STAMP(3);

  Ring r; r.landed = 0; r.budget = 1u << 22; r.laddr = (unsigned)(uintptr_t)(c.L + L_CTL) + C_LANDED * 4;
  const WB wb = make_wb((unsigned)(uintptr_t)c.L + lane * 16);
  const int xvoff = lane * 16;
  const int mate = lane < 16 ? (((sx & ~7) | (lane & 7)) << 3 | (xc & ~1) | (lane >> 3)) : -1;

  unsigned oq[2][5];
  {
    v4u xa[2][20];
    xload<20>(c, xa, xvoff, OFF_XN + (8 * pi + 2 * w) * 20480, 20480);
    {
      f32x4 acc[2][5];
      gemm_pass<5, 20, 5, V_A1, 0, 20, true>(c, r, w, wb, acc, xa, 0, 0, 0);
      if (w == 0) STAMP(4);
      v4u pk[3], vv[3];
#pragma unroll
      for (int t = 0; t < 3; ++t)
        pk[t] = v4u{pk2(phi(acc[0][t][0]), phi(acc[0][t][1])), pk2(phi(acc[0][t][2]), phi(acc[0][t][3])),
                    pk2(phi(acc[1][t][0]), phi(acc[1][t][1])), pk2(phi(acc[1][t][2]), phi(acc[1][t][3]))};
#pragma unroll
      for (int t = 0; t < 3; ++t)
        vv[t] = v4u{pk2(acc[0][t + 2][0], acc[0][t + 2][1]), pk2(acc[0][t + 2][2], acc[0][t + 2][3]),
                    pk2(acc[1][t + 2][0], acc[1][t + 2][1]), pk2(acc[1][t + 2][2], acc[1][t + 2][3])};
      if (n16 < 8) vv[0] = v4u{0x3f803f80u, 0x3f803f80u, 0x3f803f80u, 0x3f803f80u};
      LAS char* kp = c.L + L_SCR + w * 3456;
      const f32x4 z4 = {0.f, 0.f, 0.f, 0.f};
#pragma unroll
      for (int ta = 0; ta < 3; ++ta)
#pragma unroll
        for (int tb = 0; tb < 3; ++tb) {
          if (ta == 0 && tb == 2) continue;
          const f32x4 d = mfma(pk[ta], vv[tb], z4);
          const int d4g = 4 * ta + g, hd = d4g >= 5 ? 1 : 0, d4 = d4g - 5 * hd;
          const int eps = 16 * tb - 8 + n16;
          int e = -1;
          if (eps < 0) { if (n16 == 0) e = 20; }
          else if ((eps >= 20 ? 1 : 0) == hd) e = eps - 20 * hd;
          if (d4g < 10 && e >= 0) *(LAS f32x4*)(kp + ((hd * 21 + e) * 5 + d4) * 16) = d;
        }
      ctl_arrive(c, C_ARR1);
    }
    f32x4 aq[2][3];
    gemm_pass<3, 20, 3, V_A2, 1, 20, true>(c, r, w, wb, aq, xa, 0, 0, 0);
    v2u pq[2][3];
#pragma unroll
    for (int m = 0; m < 2; ++m)
#pragma unroll
      for (int t = 0; t < 3; ++t) {
        pq[m][t] = v2u{pk2(phi(aq[m][t][0]), phi(aq[m][t][1])), pk2(phi(aq[m][t][2]), phi(aq[m][t][3]))};
        if (t == 2 && g >= 2) pq[m][t] = v2u{0u, 0u};
      }
    if (w == 0) STAMP(5);
    ctl_wait(c, C_GO1, 1u);
    if (w == 0) STAMP(6);
    if (tid < 210) {
      v4u p[16];
#pragma unroll
      for (int i = 0; i < 16; ++i) p[i] = ld128(c, tid * 16, OFF_KVP + (pj * 16 + i) * KVP_STR);
      f32x4 s = __builtin_bit_cast(f32x4, p[0]);
#pragma unroll
      for (int i = 1; i < 16; ++i) s += __builtin_bit_cast(f32x4, p[i]);
      *(LAS f32x4*)(c.L + L_KVS + tid * 16) = s;
    }
    ctl_arrive(c, C_BAR1);
    ctl_wait(c, C_BAR1, 4u);
    if (w == 0) STAMP(7);
    const int gi = n16 >> 2, ri = n16 & 3;
    f32x4 on[2][2][2];
#pragma unroll
    for (int hh = 0; hh < 2; ++hh)
#pragma unroll
      for (int R = 0; R < 2; ++R) {
        const int e = R == 0 ? 5 * gi + ri : (ri == 0 ? 5 * gi + 4 : 20);
        const bool rowok = R == 0 || ri < 2;
        const int dlo = hh == 0 ? g : g - 1, dhi = hh == 0 ? 4 : 3 + g;
        const bool lok = rowok && (hh == 0 || g >= 1), hok = rowok && (hh == 0 ? g == 0 : g < 2);
        const LAS f32x4* kr = (const LAS f32x4*)(c.L + L_KVS + (hh * 21 + e) * 80);
        f32x4 lo = kr[lok ? dlo : 0], hi = kr[hok ? dhi : 0];
        v4u a;
        a.x = lok ? pk2(lo[0], lo[1]) : 0u; a.y = lok ? pk2(lo[2], lo[3]) : 0u;
        a.z = hok ? pk2(hi[0], hi[1]) : 0u; a.w = hok ? pk2(hi[2], hi[3]) : 0u;
        const f32x4 z4 = {0.f, 0.f, 0.f, 0.f};
#pragma unroll
        for (int m = 0; m < 2; ++m) on[m][hh][R] = mfma(a, v4u{pq[m][hh].x, pq[m][hh].y, pq[m][hh + 1].x, pq[m][hh + 1].y}, z4);
      }
#pragma unroll
    for (int m = 0; m < 2; ++m) {
      const float r0 = __builtin_amdgcn_rcpf(on[m][0][1][1] + 1e-6f), r1 = __builtin_amdgcn_rcpf(on[m][1][1][1] + 1e-6f);
      oq[m][0] = pk2(on[m][0][0][0] * r0, on[m][0][0][1] * r0);
      oq[m][1] = pk2(on[m][0][0][2] * r0, on[m][0][0][3] * r0);
      oq[m][2] = pk2(on[m][0][1][0] * r0, on[m][1][0][0] * r1);
      oq[m][3] = pk2(on[m][1][0][1] * r1, on[m][1][0][2] * r1);
      oq[m][4] = pk2(on[m][1][0][3] * r1, on[m][1][1][0] * r1);
      const int mt = 8 * pi + 2 * w + m;
      st128(c, v4u{oq[m][0], oq[m][1], oq[m][2], oq[m][3]}, xvoff, OFF_O + (mt * 20 + pj) * 1024);
      st32(c, oq[m][4], xvoff, OFF_O + (mt * 20 + 16 + (pj >> 2)) * 1024 + (pj & 3) * 4);
    }
  }
  const int mtB = 4 * hp + w;
  const size_t rowoff = (size_t)(16 * mtB + n16) * 1280 + cc * 160;
  const v4u xr0 = *(const GAS v4u*)(x + rowoff + g * 16), xr1 = *(const GAS v4u*)(x + rowoff + 64 + g * 16);
  const v2u xr2 = *(const GAS v2u*)(x + rowoff + 128 + g * 8);
  if (w == 0) STAMP(8);
  panel_sync(c, FL2, C_ARR2, C_GO2, b, mate);
  if (b == 0 && w == 0 && lane == 0) __hip_atomic_store((GAS u64*)(c.ws + OFF_EPOCH), c.ep, RLX_AGENT);
  if (w == 0) STAMP(9);

  const int ka = w >> 1, kb = w & 1;

  unsigned hq[10];
  {
    v4u xb[2][10];
    xload<10>(c, xb, xvoff, OFF_O + (4 * hp + 2 * ka) * 20480 + kb * 10240, 20480);
    f32x4 acc[2][5];
    gemm_pass<5, 10, 10, V_B, 1, 10, true>(c, r, w, make_wb(wb.b0 + kb * 5120), acc, xb, 0, 0, 0);
    f32x4 o[5];
    pair_reduce(c, w, C_XB0, acc, o);
    const unsigned xr[10] = {xr0.x, xr0.y, xr0.z, xr0.w, xr1.x, xr1.y, xr1.z, xr1.w, xr2.x, xr2.y};
    float s1 = 0.f, s2 = 0.f;
#pragma unroll
    for (int t = 0; t < 5; ++t) {
      hq[2 * t] = pk2(o[t][0] + bflo(xr[2 * t]), o[t][1] + bfhi(xr[2 * t]));
      hq[2 * t + 1] = pk2(o[t][2] + bflo(xr[2 * t + 1]), o[t][3] + bfhi(xr[2 * t + 1]));
    }
    const int sb = 5 * (cc >> 1) + 3 * (cc & 1);
    st128(c, v4u{hq[0], hq[1], hq[2], hq[3]}, xvoff, OFF_H + (mtB * 20 + sb) * 1024);
    st128(c, v4u{hq[4], hq[5], hq[6], hq[7]}, xvoff, OFF_H + (mtB * 20 + sb + 1) * 1024);
    st64(c, v2u{hq[8], hq[9]}, xvoff, OFF_H + (mtB * 20 + 5 * (cc >> 1) + 2) * 1024 + (cc & 1) * 8);
#pragma unroll
    for (int i = 0; i < 10; ++i) { const float a = bflo(hq[i]), d = bfhi(hq[i]); s1 += a + d; s2 += a * a + d * d; }
    s1 = sum_g(s1); s2 = sum_g(s2);
    if (g == 0) st64(c, v2u{__builtin_bit_cast(unsigned, s1), __builtin_bit_cast(unsigned, s2)}, n16 * 64, OFF_HS + mtB * 1024 + cc * 8);
  }
  if (w == 0) STAMP(10);
  panel_sync(c, FL3, C_ARR3, C_GO3, b, mate);
  if (w == 0) STAMP(11);

  {
    constexpr int XW = 12;
    const int mt0 = 8 * pi + 2 * w;
    const v4u st0 = ld128(c, n16 * 64 + g * 16, OFF_HS + mt0 * 1024), st1 = ld128(c, n16 * 64 + g * 16, OFF_HS + mt0 * 1024 + 1024);
    v4u xh[2][XW];
    xload<XW>(c, xh, xvoff, OFF_H + mt0 * 20480, 20480);
    ctl_wait(c, C_CS, 1u);
    float mean[2], sd[2], c2[2];
#pragma unroll
    for (int m = 0; m < 2; ++m) {
      const f32x4 sv = __builtin_bit_cast(f32x4, m ? st1 : st0);
      const float s1 = sum_g(sv[0] + sv[2]), s2 = sum_g(sv[1] + sv[3]);
      mean[m] = s1 * (1.f / 640.f);
      const float var = fmaxf(s2 * (1.f / 640.f) - mean[m] * mean[m], 0.f) + 1e-5f;
      sd[m] = sqrtf(var);
      c2[m] = -1.4426950408889634f * rsqrtf(var);
    }
    f32x4 acc[2][7];
#pragma unroll
    for (int t = 0; t < 7; ++t) {
      const f32x4 cs = *(const LAS f32x4*)(c.L + L_CSUM + t * 64 + g * 16);
      acc[0][t] = cs * (-mean[0]); acc[1][t] = cs * (-mean[1]);
    }
    gemm_pass<7, 20, 7, V_C, 1, XW, false>(c, r, w, wb, acc, xh, xvoff, OFF_H + mt0 * 20480, 20480);
    if (w == 0) STAMP(12);
#pragma unroll
    for (int m = 0; m < 2; ++m) {
      unsigned fq[14];
#pragma unroll
      for (int t = 0; t < 7; ++t) {
        float y[4];
#pragma unroll
        for (int i = 0; i < 4; ++i) {
          const float a = acc[m][t][i];
          const float e = __builtin_amdgcn_exp2f(a * c2[m]);
          y[i] = a * __builtin_amdgcn_rcpf(fmaf(e, sd[m], sd[m]));
        }
        fq[2 * t] = pk2(y[0], y[1]); fq[2 * t + 1] = pk2(y[2], y[3]);
      }
      const int fb = OFF_F + ((mt0 + m) * 54) * 1024;
#pragma unroll
      for (int v = 0; v < 3; ++v) st128(c, v4u{fq[4 * v], fq[4 * v + 1], fq[4 * v + 2], fq[4 * v + 3]}, xvoff, fb + (3 * pj + v) * 1024);
      if ((pj & 3) != 3) { const int l = 3 * (pj >> 2) + (pj & 3); st64(c, v2u{fq[12], fq[13]}, xvoff, fb + (48 + (l >> 1)) * 1024 + (l & 1) * 8); }
    }
  }
  if (w == 0) STAMP(13);
  panel_sync(c, FL4, C_ARR4, C_GO4, b, mate);
  if (w == 0) STAMP(14);

  {
    constexpr int XW = 14;
    const int xs = OFF_F + (4 * hp + 2 * ka) * 55296 + kb * 27648;
    v4u xf[2][XW];
    xload<XW>(c, xf, xvoff, xs, 55296);
    f32x4 acc[2][5];
    gemm_pass<5, 27, 10, V_D, 1, XW, true>(c, r, w, make_wb(wb.b0 + kb * 5120), acc, xf, xvoff, xs, 55296);
    f32x4 o[5];
    pair_reduce(c, w, C_XD0, acc, o);
    unsigned y[10];
#pragma unroll
    for (int t = 0; t < 5; ++t) {
      y[2 * t] = pk2(o[t][0] + bflo(hq[2 * t]), o[t][1] + bfhi(hq[2 * t]));
      y[2 * t + 1] = pk2(o[t][2] + bflo(hq[2 * t + 1]), o[t][3] + bfhi(hq[2 * t + 1]));
    }
    *(GAS v4u*)(out + rowoff + g * 16) = v4u{y[0], y[1], y[2], y[3]};
    *(GAS v4u*)(out + rowoff + 64 + g * 16) = v4u{y[4], y[5], y[6], y[7]};
    *(GAS v2u*)(out + rowoff + 128 + g * 8) = v2u{y[8], y[9]};
  }
  if (w == 0) STAMP(15);
}

extern "C" void kernel_launch(void* const* d_in, const int* in_sizes, int n_in, void* d_out, int out_size, void* d_ws, size_t ws_size,
                              hipStream_t stream) {
  (void)hipFuncSetAttribute((const void*)mk_kernel, hipFuncAttributeMaxDynamicSharedMemorySize, LDS_BYTES);
  mk_kernel<<<256, 512, LDS_BYTES, stream>>>((const char*)d_in[0], (const char*)d_in[1], (const char*)d_in[2], (const char*)d_in[4],
                                             (const char*)d_in[5], (char*)d_out, (char*)d_ws);
}
```

```cpp
#include <hip/hip_runtime.h>
#include <stdint.h>

typedef unsigned v4u __attribute__((ext_vector_type(4)));
typedef unsigned v2u __attribute__((ext_vector_type(2)));
typedef short v4s __attribute__((ext_vector_type(4)));
typedef float f32x4 __attribute__((ext_vector_type(4)));
typedef float f32x2 __attribute__((ext_vector_type(2)));
typedef __bf16 bf16x2 __attribute__((ext_vector_type(2)));
typedef __bf16 bf16x8 __attribute__((ext_vector_type(8)));
typedef unsigned long long u64;
typedef __amdgpu_buffer_rsrc_t rsrc_t;
#define LAS __attribute__((address_space(3)))
#define GAS __attribute__((address_space(1)))
#define DI __device__ __forceinline__
#define AI __attribute__((always_inline))
#define RLX_AGENT __ATOMIC_RELAXED, __HIP_MEMORY_SCOPE_AGENT
#define SCHED() __builtin_amdgcn_sched_barrier(0)
#define CFENCE() asm volatile("" ::: "memory")
#define DRAIN() asm volatile("s_waitcnt vmcnt(0)" ::: "memory")
#define VMWAIT(n) asm volatile("s_waitcnt vmcnt(" #n ")" ::: "memory")
#define LGKM0() asm volatile("s_waitcnt lgkmcnt(0)" ::: "memory")
#define PIN(v) asm volatile("" : "+v"(v))
#define LP(T, a) ((LAS T*)(uintptr_t)(unsigned)(a))
constexpr int SC1 = 16;
constexpr unsigned ONE2 = 0x3f803f80u;

#define DEP_A 55
#define DEP_P 55
#define XPRE 6
#define RPK_AT 19
#define STR_(x) #x
#define STR(x) STR_(x)

#define RPK_AT1 22
#define RPK_AUX 0
#define RPK_PRIO 3

#define ROT 1
#define PRE_B 6
#define PRE_C 8
#define PRE_D 8

constexpr int OFF_EPOCH = 0;
constexpr int OFF_FLAGS = 4096;
constexpr int OFF_CS = 262144;
constexpr int OFF_HS = 327680;
constexpr int OFF_KVP = 524288;
constexpr int KVP_STR = 3456;
constexpr int OFF_WB = 2 << 20;
constexpr int OFF_WC = OFF_WB + 819200;
constexpr int OFF_WD = OFF_WC + 2293760;
constexpr int OFF_O = OFF_WD + 2211840;
constexpr int OFF_H = OFF_O + 2621440;
constexpr int OFF_F = OFF_H + 2621440;
constexpr int WS_BYTES = OFF_F + 7077888;
constexpr int OFF_PROF = 32 << 20;
enum { FLR = 0, FL1, FL2, FL3, FL4A, FL4B };
constexpr u64 EPOCH_STEP = 0x9E3779B97F4A7C15ull;

constexpr int RCH = 13;
constexpr int L_SCR = RCH * 10240;
constexpr int L_RPK = L_SCR + 20480;
constexpr int L_CSUM = L_RPK + 4096;
constexpr int L_DONE = L_CSUM + 512;
constexpr int L_LAND = L_DONE + 1024;
constexpr int L_CTL = L_LAND + 512;
constexpr int L_PROF = L_CTL + 128;
constexpr int LDS_BYTES = L_PROF;
static_assert(LDS_BYTES <= 163840, "lds");
enum { C_CAP = 0, C_CSK, C_CSQ, C_RED, C_CS, C_ARR1, C_GO1, C_BAR1, C_ARR2, C_GO2, C_XB0, C_XB1, C_ARR3, C_GO3, C_ARR4A, C_GO4A,
       C_ARR4B, C_GO4B, C_XD0, C_XD1, C_ADR };

constexpr int CH_KV = 20, CH_Q = 30, CH_B = 35, CH_C1 = 45, CH_C2 = 53, CH_D = 59, CH_END = 86;
constexpr int needp(int ch) { return ch < 11 ? 4 * ch + 4 : ch < CH_KV ? 4 * ch + 14 : ch < CH_KV + 2 ? 5 * ch - 51 : 5 * ch - 15; }
constexpr int slot(int ch) { return ch < 11 ? ch : ch < CH_KV ? ch - 11 : (ch - 9) % RCH; }
constexpr int KVSLOT = slot(CH_B);
static_assert(KVSLOT + 4 <= RCH, "kv slots");

struct Ctx {
  rsrc_t rs;
  GAS char* ws;
  u64 ep;
  int lane;
  unsigned landed, budget;
};

DI f32x4 mfma(v4u a, v4u b, f32x4 c) {
  return __builtin_amdgcn_mfma_f32_16x16x32_bf16(__builtin_bit_cast(bf16x8, a), __builtin_bit_cast(bf16x8, b), c, 0, 0, 0);
}
DI float asf(unsigned u) { return __builtin_bit_cast(float, u); }
DI unsigned asu(float f) { return __builtin_bit_cast(unsigned, f); }
DI unsigned pk2(float a, float b) { f32x2 f = {a, b}; return __builtin_bit_cast(unsigned, __builtin_convertvector(f, bf16x2)); }
DI float bflo(unsigned u) { return asf(u << 16); }
DI float bfhi(unsigned u) { return asf(u & 0xffff0000u); }
DI f32x4 unpk4(unsigned a, unsigned b) { return f32x4{bflo(a), bfhi(a), bflo(b), bfhi(b)}; }
DI float dot2(unsigned a, unsigned b, float c) {
  return __builtin_amdgcn_fdot2_f32_bf16(__builtin_bit_cast(bf16x2, a), __builtin_bit_cast(bf16x2, b), c, false);
}
DI v4u ld128(const Ctx& c, int voff, int soff) { return __builtin_amdgcn_raw_buffer_load_b128(c.rs, voff, soff, SC1); }
DI v2u ld64(const Ctx& c, int voff, int soff) { return __builtin_amdgcn_raw_buffer_load_b64(c.rs, voff, soff, SC1); }
DI void st128(const Ctx& c, v4u v, int voff, int soff) { __builtin_amdgcn_raw_buffer_store_b128(v, c.rs, voff + soff, 0, SC1); }
DI void st64(const Ctx& c, v2u v, int voff, int soff) { __builtin_amdgcn_raw_buffer_store_b64(v, c.rs, voff, soff, SC1); }
DI void st32(const Ctx& c, unsigned v, int voff, int soff) { __builtin_amdgcn_raw_buffer_store_b32(v, c.rs, voff, soff, SC1); }
DI u64 now() { return __builtin_amdgcn_s_memrealtime(); }

DI void ctl_add(int i) {
  asm volatile("v_mov_b32 v254, %0\n\tv_mov_b32 v255, %1\n\ts_mov_b64 exec, 1\n\tds_add_u32 v254, v255\n\ts_mov_b64 exec, -1"
               :: "s"(L_CTL + 4 * i), "i"(L_CTL) : "memory", "v254", "v255");
}
template <int I> DI void ctl_addc() {
  asm volatile("v_mov_b32 v254, %0\n\ts_mov_b64 exec, 1\n\tds_add_u32 v254, v254 offset:%1\n\ts_mov_b64 exec, -1"
               :: "i"(L_CTL), "i"(4 * I) : "memory", "v254");
}
template <int SLP = 0> DI void ctl_wait(Ctx& c, int i, int n) {
  unsigned t;
  asm volatile(
      "v_mov_b32 v254, %2\n"
      ".Lw%=:\n\t"
      ".if %4\n\ts_sleep %4\n\t.endif\n\t"
      "ds_read_b32 v255, v254\n\t"
      "s_sub_u32 %0, %0, 1\n\t"
      "s_max_i32 %0, %0, 0\n\t"
      "s_waitcnt lgkmcnt(0)\n\t"
      "v_readfirstlane_b32 %1, v255\n\t"
      "s_cmp_lt_u32 %1, %3\n\t"
      "s_cselect_b32 %1, %0, 0\n\t"
      "s_cmp_lg_u32 %1, 0\n\t"
      "s_cbranch_scc1 .Lw%="
      : "+{s71}"(c.budget), "=&s"(t) : "s"(L_CTL + 4 * i), "s"(n * L_CTL), "i"(SLP) : "memory", "scc", "v254", "v255");
}
DI unsigned ctl_read(int i) { return __builtin_amdgcn_readfirstlane(*LP(volatile unsigned, L_CTL + 4 * i)); }
DI void flag_set(const Ctx& c, int kind, int idx) {
  if (c.lane == 0) __hip_atomic_store((GAS u64*)(c.ws + OFF_FLAGS + (kind * 256 + idx) * 128), c.ep, RLX_AGENT);
}
DI void flag_wait(const Ctx& c, int kind, int idx, int nl) {
  if (c.lane < nl) {
    GAS u64* p = (GAS u64*)(c.ws + OFF_FLAGS + (kind * 256 + idx) * 128);
    unsigned n = 1u << 17;
    bool go;
    do {
      const u64 v = __hip_atomic_load(p, RLX_AGENT);
      go = __any(v != c.ep) && --n != 0;
    } while (go);
  }
  CFENCE();
}
#define STAMP(i) do {} while (0)
#define DUMP(lo, hi) do {} while (0)

DI v2u swap16(unsigned a, unsigned b) { return __builtin_amdgcn_permlane16_swap(a, b, false, false); }
DI v2u swap32(unsigned a, unsigned b) { return __builtin_amdgcn_permlane32_swap(a, b, false, false); }
DI float sum_g(float v) {
  v2u r = swap16(asu(v), asu(v));
  v = asf(r.x) + asf(r.y);
  r = swap32(asu(v), asu(v));
  return asf(r.x) + asf(r.y);
}
DI v2u tr1(unsigned a, int off) { return __builtin_bit_cast(v2u, __builtin_amdgcn_ds_read_tr16_b64_v4i16(LP(v4s, a + off))); }
DI v4u tr2(unsigned a, int off, int hioff) {
  const v2u lo = tr1(a, off), hi = tr1(a, off + hioff);
  return v4u{lo.x, lo.y, hi.x, hi.y};
}

DI int gam0(int j) { return 27 * (j >> 2) + 7 * (j & 3); }
template <int KIND> DI int kbase(int s, int g) {
  if (KIND == 1) return s < 16 ? 40 * s + 5 * g : 40 * (4 * (s - 16) + g) + 23;
  if (KIND == 2) {
    const int u = s / 5, v = s - 5 * u;
    return 160 * u + (v < 2 ? 32 * v + 8 * g : v == 2 ? 64 + (g >> 1) * 80 + 8 * (g & 1) : 32 * v - 16 + 8 * g);
  }
  if (s < 48) return 16 * (s < 32 ? gam0(s >> 1) + 2 * (s & 1) : gam0(s - 32) + 4) + 4 * g;
  const int l = 2 * (s - 48) + (g >> 1), q = l / 3;
  return 16 * (gam0(4 * q + (l - 3 * q)) + 6) + 8 * (g & 1);
}
template <int KIND> DI int kpat(int s, int j) {
  if (KIND == 1) return s < 16 ? (j >= 5 ? 15 + j : j) : 5 * (j >> 1) + (j & 1);
  if (KIND == 2) return j;
  return s < 48 ? 16 * (j >> 2) + (j & 3) : j;
}
template <int KIND> DI int sglob(int rb, int sp) {
  if (KIND == 1) return sp < 4 ? 4 * rb + sp : 16 + rb;
  if (KIND == 3) return 6 * rb + sp;
  return 5 * rb + sp;
}

static_assert(needp(RPK_AT) >= needp(CH_KV), "loader addresses live in the bounce buffer");
template <int KIND> DI void repack(Ctx& c, const char* W, int rb, int cb, unsigned bb, unsigned bs) {
  constexpr int NS = KIND == 3 ? 6 : 5;
  constexpr int PITCH = KIND == 2 ? 3456 : 1280;
  const rsrc_t rr_ = __builtin_amdgcn_make_buffer_rsrc((void*)W, 0, KIND == 1 ? 819200 : 2211840, 0x00020000);
  const int l = c.lane, n = l & 15, g = l >> 4, q = (l >> 2) & 3, p = l & 3;
  int va = kpat<KIND>(sglob<KIND>(rb, 0), l >> 3) * PITCH + (l & 7) * 16, vb = kpat<KIND>(sglob<KIND>(rb, NS - 1), l >> 3) * PITCH + (l & 7) * 16;
  int so[NS][4];
#pragma unroll
  for (int sp = 0; sp < NS; ++sp)
#pragma unroll
    for (int gg = 0; gg < 4; ++gg) { so[sp][gg] = kbase<KIND>(sglob<KIND>(rb, sp), gg) * PITCH + cb * 128; asm volatile("" : "+s"(so[sp][gg])); }
  int base[4];
  unsigned src[4];
#pragma unroll
  for (int ww = 0; ww < 4; ++ww) {
    const int gam = 4 * cb + ww;
    int sig = p;
    if (KIND == 2) {
      const int grp = gam / 27, rr = gam - 27 * grp, jj = min(rr / 7, 3), t = rr - 7 * jj;
      base[ww] = OFF_WC + (4 * grp + jj) * 143360 + (t < 4 ? t + 20 * rb : 76 + t + 15 * rb) * 1024 + l * 16;
      if (t >= 4) base[ww] |= 1;
    } else {
      const int cc = gam / 5, cq = gam - 5 * cc;
      int tile = 4, slot = n;
      if (cq < 4) { sig = ((p & 1) << 1) | (p >> 1); tile = 2 * (cq >> 1) + (n >> 3); slot = 8 * (cq & 1) + (n & 7); }
      base[ww] = (KIND == 1 ? OFF_WB + cc * 102400 : OFF_WD + cc * 276480) + tile * 1024 + (g * 16 + slot) * 16;
    }
    src[ww] = bb + g * bs + q * 128 + 32 * ww + 8 * sig;
  }
#pragma unroll
  for (int ww = 0; ww < 4; ++ww) { PIN(base[ww]); PIN(src[ww]); }
  PIN(va); PIN(vb);
  {
    unsigned n_ = 0;
    while (min(__builtin_amdgcn_readfirstlane(*LP(volatile unsigned, L_LAND)), __builtin_amdgcn_readfirstlane(*LP(volatile unsigned, L_LAND + 256))) < needp(RPK_AT) && ++n_ < (1u << 16)) __builtin_amdgcn_s_sleep(4);
    CFENCE();
  }
  v4u r[NS][4];
#pragma unroll
  for (int sp = 0; sp < NS; ++sp)
#pragma unroll
    for (int gg = 0; gg < 4; ++gg) r[sp][gg] = __builtin_amdgcn_raw_buffer_load_b128(rr_, sp == NS - 1 ? vb : va, so[sp][gg], RPK_AUX);
  float cs[4] = {0.f, 0.f, 0.f, 0.f};
#pragma unroll
  for (int sp = 0; sp < NS; ++sp) {
#pragma unroll
    for (int gg = 0; gg < 4; ++gg) *LP(v4u, bb + gg * bs + l * 16) = r[sp][gg];
    CFENCE();
    const int s = sglob<KIND>(rb, sp);
    v4u v[4];
#pragma unroll
    for (int ww = 0; ww < 4; ++ww) v[ww] = tr2(src[ww], 0, 512);
    CFENCE();
#pragma unroll
    for (int ww = 0; ww < 4; ++ww) {
      int off;
      if (KIND == 1) off = base[ww] + (s >= 10 ? (s - 10) * 10 + 5 : s * 10) * 1024;
      else if (KIND == 3) off = base[ww] + (s < 16 ? s * 10 : s < 32 ? (s - 16) * 10 + 5 : s < 43 ? (s - 16) * 10 : (s - 27) * 10 + 5) * 1024;
      else off = (base[ww] & ~1) + sp * ((base[ww] & 1) ? 3072 : 4096);
      st128(c, v[ww], off, 0);
      if (KIND == 2) cs[ww] = dot2(v[ww].x, ONE2, dot2(v[ww].y, ONE2, dot2(v[ww].z, ONE2, dot2(v[ww].w, ONE2, cs[ww]))));
    }
  }
  if (KIND == 2) {
#pragma unroll
    for (int ww = 0; ww < 4; ++ww) cs[ww] = sum_g(cs[ww]);
    const float v = g == 0 ? cs[0] : g == 1 ? cs[1] : g == 2 ? cs[2] : cs[3];
    st32(c, asu(v), l * 4, OFF_CS + (rb * 1792 + 64 * cb) * 4);
  }
}

template <int CH> DI void ring_need(Ctx& c) {
  asm volatile("s_getpc_b64 s[72:73]\n\ts_cmpk_lt_u32 %0, %2\n\ts_cbranch_scc1 .Lmk_ring"
               : "+{s70}"(c.landed), "+{s71}"(c.budget) : "i"(needp(CH)) : "memory", "scc", "s72", "s73", "v254", "v255");
}
constexpr int upto(int ch) {
  if (ch < CH_Q) return ch;
  if (ch == CH_Q) return CH_B - 1;
  if (ch == CH_B) return CH_C1 - 1;
  if (ch == CH_C1) return CH_C2 - 1;
  if (ch == CH_C2) return CH_D - 2;
  if (ch == CH_D - 1) return ch;
  if (ch == CH_D) return CH_D + 6;
  if (ch == CH_D + 7) return CH_D + 12;
  if (ch >= CH_D + 13 && (ch - CH_D - 13) % 2 == 0) return ch + 1;
  return -1;
}
constexpr bool released(int n) { return n + RCH <= CH_END + 1 && (n <= CH_B || (n > CH_C1 && n <= CH_D) || (n - CH_B) % 2 == 0); }
template <int I> struct IC { static constexpr int v = I; };
template <int B, int E, class Fn> DI void static_for(Fn&& fn) {
  if constexpr (B < E) { fn(IC<B>{}); static_for<B + 1, E>(fn); }
}
template <int D> DI void bump(unsigned& b) { if constexpr (D != 0) asm volatile("v_add_u32 %0, %1, %0" : "+v"(b) : "i"(D)); }
enum { W_PK = 0, W_KV = 1, W_Q = 2 };
constexpr int region(int ch) { return (slot(ch) / 6) * 61440; }
constexpr int inreg(int ch) { return (slot(ch) % 6) * 10240; }
template <int WK> constexpr int foff(int e) { return WK == W_PK ? e * 1024 : WK == W_KV ? (e / 5) * 5120 + 32 * (e % 5) : (e / 6) * 5120 + ((e / 3) & 1) * 80 + 32 * (e % 3); }

template <int TL, bool Z> DI void mm(f32x4& acc, const v4u& wf, const v4u& x) {
  if constexpr (Z && TL) asm volatile("v_mfma_f32_16x16x32_bf16 %0, %1, %2, 0" : "=&v"(acc) : "v"(wf), "v"(x));
  else if constexpr (Z) asm volatile("v_mfma_f32_16x16x32_bf16 %0, %2, %1, 0" : "=&v"(acc) : "v"(wf), "v"(x));
  else if constexpr (TL) asm volatile("v_mfma_f32_16x16x32_bf16 %0, %1, %2, %0" : "+v"(acc) : "v"(wf), "v"(x));
  else asm volatile("v_mfma_f32_16x16x32_bf16 %0, %2, %1, %0" : "+v"(acc) : "v"(wf), "v"(x));
}
template <int WK, int NT, int NKS, int CH0, int FPC, int TL, int XW, int ZM, class Hook>
DI void gemm_pass(Ctx& c, unsigned dn, unsigned wb, f32x4 (&acc)[2][NT], v4u (&x)[2][XW], Hook&& hook) {
  constexpr int LA = 3, WBN = LA + 1, NF = NKS * NT;
  v4u wf[WBN];
  unsigned la, lb, dv;
  bump<region(CH0)>(wb);
  SCHED();
  static_for<-LA, NF>([&](auto ic) AI {
    constexpr int f = decltype(ic)::v, fn = f + LA;
    constexpr int s = f < 0 ? 0 : f / NT, t = f < 0 ? 0 : f % NT;
    constexpr int ch = CH0 + fn / FPC, off = inreg(ch) + foff<WK>(fn % FPC);
    constexpr auto chk = [](int i) { return i > 0 && i < NF && i % FPC == 0 && upto(CH0 + i / FPC) >= 0; };
    constexpr bool rel = f >= 0 && (f + 1) % FPC == 0 && released(CH0 + (f + 1) / FPC);
    if constexpr (f >= 0) { mm<TL, (ZM & 1) && s == 0>(acc[0][t], wf[f % WBN], x[0][s % XW]); SCHED(); }
    v2u lo;
    if constexpr (fn < NF) {
      if constexpr (fn == 0 || chk(fn)) ring_need<upto(ch)>(c);
      if constexpr (WK == W_PK) wf[fn % WBN] = *LP(v4u, wb + off);
      else lo = tr1(wb, off);
      SCHED();
    }
    if constexpr (chk(fn + 4)) { la = *LP(unsigned, dn + 1024); lb = *LP(unsigned, dn + 1280); SCHED(); }
    if constexpr (chk(fn + 2)) { la = min(la, lb); PIN(la); SCHED(); }
    if constexpr (chk(fn + 1)) { c.landed = __builtin_amdgcn_readfirstlane(la); asm volatile("" : "+{s70}"(c.landed)); SCHED(); }
    if constexpr (rel) { asm volatile("v_mov_b32 %0, %1" : "=v"(dv) : "i"(CH0 + (f + 1) / FPC)); SCHED(); }
    if constexpr (f >= 0) { mm<TL, (ZM & 2) && s == 0>(acc[1][t], wf[f % WBN], x[1][s % XW]); SCHED(); }
    if constexpr (fn < NF && WK != W_PK) { const v2u hi = tr1(wb, off + 2560); wf[fn % WBN] = v4u{lo.x, lo.y, hi.x, hi.y}; SCHED(); }
    if constexpr (fn + 1 < NF && (fn + 1) % FPC == 0) bump<region(ch + 1) - region(ch)>(wb);
    if constexpr (rel) *LP(volatile unsigned, dn) = dv;
    hook(ic);
    SCHED();
  });
  asm volatile("s_nop 7");
  SCHED();
}
template <int XW, int S0, int S1, int N, int LEN> DI void xload(const Ctx& c, v4u (&x)[2][XW], int xvoff, int xsoff, int xmt, int rot) {
#pragma unroll
  for (int s = S1; s < S1 + N; ++s) {
    const int o = xsoff + (rot + s >= LEN ? rot + s - LEN : rot + s) * 1024;
    x[0][(S0 + s) % XW] = ld128(c, xvoff, o);
    x[1][(S0 + s) % XW] = ld128(c, xvoff, o + xmt);
  }
  SCHED();
}
DI int opaque(int v) { asm volatile("" : "+v"(v)); return v; }
DI v2u phi4(f32x4 te) {
  const f32x4 u = te * 1.4426950408889634f - 1.4426950408889634e-6f;
  float y[4];
#pragma unroll
  for (int i = 0; i < 4; ++i) y[i] = fmaxf(te[i], 1e-6f) + __builtin_amdgcn_fmed3f(__builtin_amdgcn_exp2f(u[i]), 0.f, 1.f);
  return v2u{pk2(y[0], y[1]), pk2(y[2], y[3])};
}

DI void pair_reduce(Ctx& c, int w, int cx, f32x4 (&acc)[2][5], unsigned (&y)[10]) {
  const unsigned lo = opaque(c.lane) * 16 + L_SCR;
  LAS f32x4* mine = LP(f32x4, lo + w * 5120);
  LAS f32x4* other = LP(f32x4, lo + (w ^ 1) * 5120);
#pragma unroll
  for (int t = 0; t < 5; ++t) mine[t * 64] = acc[1][t];
  ctl_add(cx + (w >> 1));
  ctl_wait(c, cx + (w >> 1), 2);
#pragma unroll
  for (int t = 0; t < 5; ++t) {
    const f32x4 o = acc[0][t] + other[t * 64];
    y[2 * t] = pk2(o[0], o[1]); y[2 * t + 1] = pk2(o[2], o[3]);
  }
}
DI int mod10(int v) { return v >= 20 ? v - 20 : v >= 10 ? v - 10 : v; }

__global__ void __launch_bounds__(512, 2)
mk_kernel(const char* __restrict__ x, const char* __restrict__ wqkv, const char* __restrict__ wo, const char* __restrict__ w1,
          const char* __restrict__ w2, char* __restrict__ out, char* ws) {
  u64 cpc; unsigned clen;
  asm volatile(
      "s_getpc_b64 %0\n"
      ".Lpc%=:\n\t"
      "s_mov_b32 %1, .Lend%=-.Lpc%=-4\n\t"
      ".subsection 1\n"
      ".Lmk_ring:\n\t"
      "v_mov_b32 v254, %2\n\t"
      "ds_read2st64_b32 v[254:255], v254 offset1:1\n\t"
      "s_sub_u32 s71, s71, 1\n\t"
      "s_cbranch_scc1 .Lmk_give\n\t"
      "s_waitcnt lgkmcnt(0)\n\t"
      "v_min_u32 v254, v254, v255\n\t"
      "s_nop 0\n\t"
      "v_readfirstlane_b32 s70, v254\n\t"
      "s_setpc_b64 s[72:73]\n"
      ".Lmk_give:\n\t"
      "s_waitcnt lgkmcnt(0)\n\t"
      "s_mov_b32 s71, 0\n\t"
      "s_movk_i32 s70, 0x7fff\n\t"
      "s_setpc_b64 s[72:73]\n\t"
      ".subsection 2\n"
      ".Lend%=:\n\t"
      ".fill 128, 4, 0xBF800000\n\t"
      ".subsection 0"
      : "=s"(cpc), "=s"(clen) : "i"(L_LAND));
  Ctx c;
  c.ws = (GAS char*)ws;
  c.rs = __builtin_amdgcn_make_buffer_rsrc((void*)ws, 0, WS_BYTES, 0x00020000);
  c.landed = 0; c.budget = 1u << 22;
  const int tid = threadIdx.x, lane = tid & 63, w = __builtin_amdgcn_readfirstlane(tid >> 6);
  c.lane = lane;
  const int b = blockIdx.x, xc = b & 7, sx = b >> 3;
  const int pi = 4 * (xc >> 1) + (sx >> 3), pj = (xc & 1) * 8 + (sx & 7);
  const int hp = 4 * xc + (sx >> 3), cc = sx & 7;
  const int qi = 2 * xc + (sx >> 4), qj = sx & 15;
  const int rot = mod10((sx & 7) + ((5 * (sx >> 3)) >> 1) + (xc >> 1));
  const int rotB = ROT * ((5 * cc) >> 2), rotC = ROT * 5 * (qj & 3), rotD1 = ROT * ((2 * cc + 5 * (sx >> 3)) & 15);
  const int rd2 = ((11 * cc) >> 3) + 3 * (sx >> 3), rotD2 = ROT * (rd2 >= 11 ? rd2 - 11 : rd2);
  const int n16 = lane & 15, g = lane >> 4;
  asm volatile("" :: "s"(x), "s"(wqkv), "s"(wo), "s"(w1), "s"(w2), "s"(out), "s"(ws));
  const rsrc_t rx = __builtin_amdgcn_make_buffer_rsrc((void*)(x - 3072), 0, 2048 * 1280 + 3072, 0x00020000);
  const int xr = lane >> 3;
  const int v0 = xr * 1280 + (((lane & 7) ^ (xr >> 1)) * 16), v1 = (8 + xr) * 1280 + (((lane & 7) ^ (4 + (xr >> 1))) * 16);
  if (w == 0) STAMP(0);
  STAMP(32 + w);
  if (__builtin_expect((w >> 1) != 2, 1)) {
    *LP(v4u, L_DONE + lane * 16) = v4u{0u, 0u, 0u, 0u};
    *LP(unsigned, L_CTL + (lane & 31) * 4) = (lane & 31) == C_CAP ? CH_B : 0u;
    LGKM0();
    __builtin_amdgcn_s_barrier();
    CFENCE();
  }
  if (__builtin_expect(w >= 4, 1)) {
    __builtin_amdgcn_s_setprio(3);
    if (__builtin_expect(w < 6, 1)) {
      const int Lw = w - 4;
      unsigned lim = RCH, bud = 1u << 20, dep = DEP_A;
      const unsigned pub = L_LAND + lane * 4 + Lw * 256, lw = Lw * 5120;
#define MK_ST "+{s70}"(lim), "+{s71}"(bud), "+{s76}"(dep)
#define MK_IN "{v249}"(pub), "{s81}"(lw)
#define MK_CL "memory", "scc", "s72", "s73", "s74", "s75", "s77", "s78", "s79", "s80", "v250", "v251", "v252", "v253", "v254", "v255"
      if (w == 4) STAMP(40);
      asm volatile(
          ".macro MK_PUB\n\t"
          "s_getpc_b64 s[72:73]\n\t"
          "s_getreg_b32 s74, hwreg(HW_REG_IB_STS)\n\t"
          "s_and_b32 s75, s74, 15\n\t"
          "s_bfe_u32 s74, s74, 0x20016\n\t"
          "s_lshl4_add_u32 s74, s74, s75\n\t"
          "s_sub_u32 s75, mk_p, s74\n\t"
          "v_mov_b32 v250, s75\n\t"
          "ds_write_b32 v249, v250\n\t"
          "s_cmp_gt_u32 s74, s76\n\t"
          "s_cbranch_scc1 .Lmk_lslow\n\t"
          ".endm\n\t"
          ".macro MK_HEAD key, sl\n\t"
          "MK_PUB\n\t"
          ".if (\\key) >= 13\n\t"
          "s_cmpk_le_u32 s70, \\key\n\t"
          "s_cbranch_scc1 .Lmk_lslow\n\t"
          ".endif\n\t"
          "s_add_u32 m0, s81, (\\sl)*10240\n\t"
          ".endm\n\t"
          ".macro MK_X i, a0, a1, a2, a3, rs, sb\n\t"
          ".if (\\i) == 2\n\ts_waitcnt lgkmcnt(0)\n\ts_barrier\n\t.endif\n\t"
          ".if (\\i) < 11\n\t"
          ".if (\\i) == 0 || (\\i) >= " STR(XPRE) "\n\tMK_PUB\n\t.endif\n\t"
          "s_add_u32 m0, s81, (\\i)*10240\n\t"
          ".else\n\t"
          "MK_HEAD ((\\i)+2), ((\\i)-11)\n\t"
          ".endif\n\t"
          "s_add_u32 s77, s80, \\sb\n\t"
          ".if (\\i) %% 2\n\ts_add_u32 s77, s77, 81920\n\t.endif\n\t"
          "s_add_u32 s78, s77, 20480\n\t"
          "buffer_load_dwordx4 \\a0, \\rs, s77 offen lds\n\t"
          "buffer_load_dwordx4 \\a1, \\rs, s77 offen offset:1024 lds\n\t"
          "buffer_load_dwordx4 \\a2, \\rs, s78 offen offset:2048 lds\n\t"
          "buffer_load_dwordx4 \\a3, \\rs, s78 offen offset:3072 lds\n\t"
          ".if (\\i) %% 2\n\t"
          "s_add_u32 s80, s80, 128\n\t"
          "s_sub_u32 s79, s80, 1280\n\t"
          "s_min_u32 s80, s80, s79\n\t"
          ".endif\n\t"
          ".set mk_p, mk_p+4\n\t"
          ".endm\n\t"
          ".macro MK_RAW key, a0, a1, a2, a3, a4, rs, step\n\t"
          "MK_HEAD \\key, ((mk_i-9) %% 13)\n\t"
          "s_add_u32 s78, s77, \\step\n\t"
          "buffer_load_dwordx4 \\a0, \\rs, s77 offen lds\n\t"
          "s_add_u32 m0, m0, 1024\n\t"
          "s_sub_u32 s79, s78, 2457600\n\t"
          "buffer_load_dwordx4 \\a1, \\rs, s77 offen lds\n\t"
          "s_add_u32 m0, m0, 1024\n\t"
          "s_min_u32 s78, s78, s79\n\t"
          "buffer_load_dwordx4 \\a2, \\rs, s77 offen lds\n\t"
          "s_add_u32 m0, m0, 1024\n\t"
          "s_nop 0\n\t"
          "buffer_load_dwordx4 \\a3, \\rs, s77 offen lds\n\t"
          "s_add_u32 m0, m0, 1024\n\t"
          "s_nop 0\n\t"
          "buffer_load_dwordx4 \\a4, \\rs, s77 offen lds\n\t"
          "s_mov_b32 s77, s78\n\t"
          ".set mk_p, mk_p+5\n\t"
          ".set mk_i, mk_i+1\n\t"
          ".endm\n\t"
          ".macro MK_PK l0, l1, rs, base, len\n\t"
          "MK_HEAD mk_i, ((mk_i-9) %% 13)\n\t"
          "s_add_u32 s77, s80, \\base\n\t"
          "buffer_load_dwordx4 \\l0, \\rs, s77 offen sc1 lds\n\t"
          "s_add_u32 s80, s80, 10240\n\t"
          "buffer_load_dwordx4 \\l0, \\rs, s77 offen offset:1024 sc1 lds\n\t"
          "s_sub_u32 s79, s80, \\len\n\t"
          "buffer_load_dwordx4 \\l0, \\rs, s77 offen offset:2048 sc1 lds\n\t"
          "buffer_load_dwordx4 \\l0, \\rs, s77 offen offset:3072 sc1 lds\n\t"
          "s_add_u32 m0, m0, 4096\n\t"
          "s_min_u32 s80, s80, s79\n\t"
          "buffer_load_dwordx4 \\l1, \\rs, s77 offen sc1 lds\n\t"
          ".set mk_p, mk_p+5\n\t"
          ".set mk_i, mk_i+1\n\t"
          ".endm\n\t"
          ".subsection 1\n"
          ".Lmk_lslow:\n\t"
          "s_sleep 1\n\t"
          "v_mov_b32 v251, %[dn]\n\t"
          "ds_read_b128 v[252:255], v251\n\t"
          "ds_read_b32 v251, v251 offset:%[cap]\n\t"
          "s_sub_u32 s71, s71, 1\n\t"
          "s_cbranch_scc1 .Lmk_lgive\n\t"
          "s_waitcnt lgkmcnt(0)\n\t"
          "v_min3_u32 v252, v252, v253, v254\n\t"
          "v_min_u32 v252, v252, v255\n\t"
          "v_add_u32 v252, 13, v252\n\t"
          "v_min_u32 v252, v252, v251\n\t"
          "s_nop 0\n\t"
          "v_readfirstlane_b32 s70, v252\n\t"
          "s_setpc_b64 s[72:73]\n"
          ".Lmk_lgive:\n\t"
          "s_endpgm\n\t"
          ".subsection 0\n\t"
          ".set mk_p, 0\n\t"
          ".set mk_x, 0\n\t"
          "s_mov_b32 s80, %[sr]\n\t"
          ".rept 11\n\tMK_X mk_x, %[x0], %[x1], %[x2], %[x3], %[rx], %[sx]\n\t.set mk_x, mk_x+1\n\t.endr"
          : MK_ST
          : MK_IN, [x0] "v"(v0 + 3072), [x1] "v"(v1 + 2048), [x2] "v"(v0 + 1024), [x3] "v"(v1), [rx] "s"(rx), [sr] "s"(rot * 128),
            [sx] "s"((128 * pi + 32 * Lw) * 1280), [dn] "i"(L_DONE), [cap] "i"(L_CTL + 4 * C_CAP - L_DONE)
          : MK_CL);
      if (w == 4) STAMP(41);
      c.budget = bud;
      ctl_wait(c, C_ADR, 1);
      bud = c.budget;
      const v4u a0 = *LP(v4u, L_RPK + lane * 16), a1 = *LP(v4u, L_RPK + 1024 + lane * 16);
      const v2u a2 = *LP(v2u, L_RPK + 2048 + lane * 8);
      const rsrc_t rw = __builtin_amdgcn_make_buffer_rsrc((void*)wqkv, 0, 640 * 3840, 0x00020000);
      const rsrc_t rk = __builtin_amdgcn_make_buffer_rsrc((void*)(wqkv + Lw * 122880), 0, 640 * 3840 - Lw * 122880, 0x00020000);
      if (w == 4) STAMP(42);
      asm volatile(
          ".set mk_i, 20\n\t"
          "s_mov_b32 s77, %[sk]\n\t"
          ".rept 2\n\tMK_RAW 0, %[k0], %[k1], %[k2], %[k3], %[k4], %[rk], 245760\n\t.endr\n\t"
          "s_mov_b32 s82, s77\n\t"
          "s_mov_b32 s80, %[sr]\n\t"
          ".rept 9\n\tMK_X mk_x, %[x0], %[x1], %[x2], %[x3], %[rx], %[sx]\n\t.set mk_x, mk_x+1\n\t.endr\n\t"
          "s_mov_b32 s77, s82\n\t"
          ".rept 8\n\tMK_RAW (mk_i+2), %[k0], %[k1], %[k2], %[k3], %[k4], %[rk], 245760\n\t.endr\n\t"
          "s_mov_b32 s77, %[sq]\n\t"
          "MK_RAW (mk_i+2), %[q0], %[q1], %[q2], %[q3], %[q4], %[rw], 491520\n\t"
          ".rept 4\n\tMK_RAW mk_i, %[q0], %[q1], %[q2], %[q3], %[q4], %[rw], 491520\n\t.endr"
          : MK_ST
          : MK_IN, [x0] "v"(v0 + 3072), [x1] "v"(v1 + 2048), [x2] "v"(v0 + 1024), [x3] "v"(v1), [rx] "s"(rx), [sr] "s"(mod10(rot + 5) * 128),
            [sx] "s"((128 * pi + 32 * Lw) * 1280), [k0] "v"(a0.x), [k1] "v"(a0.y), [k2] "v"(a0.z), [k3] "v"(a0.w), [k4] "v"(a1.x),
            [q0] "v"(a1.y), [q1] "v"(a1.z), [q2] "v"(a1.w), [q3] "v"(a2.x), [q4] "v"(a2.y), [rw] "s"(rw), [rk] "s"(rk),
            [sk] "s"(rot * 245760), [sq] "s"(mod10(rot + Lw) * 245760)
          : MK_CL, "s82");
      dep = DEP_P;
      asm volatile(
          "s_mov_b32 s80, %[rb]\n\t"
          ".rept 10\n\tMK_PK %[l0], %[l1], %[rs], %[sb], 102400\n\t.endr\n\t"
          "s_mov_b32 s80, %[rc1]\n\t"
          ".rept 8\n\tMK_PK %[l0], %[l1], %[rs], %[sc1], 81920\n\t.endr\n\t"
          "s_mov_b32 s80, %[rc2]\n\t"
          ".rept 6\n\tMK_PK %[l0], %[l1], %[rs], %[sc2], 61440\n\t.endr\n\t"
          "s_mov_b32 s80, %[rd1]\n\t"
          ".rept 16\n\tMK_PK %[l0], %[l1], %[rs], %[sd1], 163840\n\t.endr\n\t"
          "s_mov_b32 s80, %[rd2]\n\t"
          ".rept 11\n\tMK_PK %[l0], %[l1], %[rs], %[sd2], 112640\n\t.endr\n\t"
          "s_mov_b32 s76, 0\n\t"
          "MK_PUB\n\t"
          ".purgem MK_PUB\n\t.purgem MK_HEAD\n\t.purgem MK_RAW\n\t.purgem MK_PK\n\t.purgem MK_X\n\t"
          "s_waitcnt vmcnt(0) lgkmcnt(0)"
          : MK_ST
          : MK_IN, [l0] "v"(lane * 16), [l1] "v"(lane * 16 + 4096), [rs] "s"(c.rs),
            [sb] "s"(OFF_WB + cc * 102400), [rb] "s"(rotB * 10240 + lw),
            [sc1] "s"(OFF_WC + qj * 143360), [rc1] "s"(rotC * 4096 + lw),
            [sc2] "s"(OFF_WC + qj * 143360 + 81920), [rc2] "s"(rotC * 3072 + lw),
            [sd1] "s"(OFF_WD + cc * 276480), [rd1] "s"(rotD1 * 10240 + lw),
            [sd2] "s"(OFF_WD + cc * 276480 + 163840), [rd2] "s"(rotD2 * 10240 + lw)
          : MK_CL);
      return;
    }
    const int fq = (pj & 7) >> 1;
    const bool fast = ((0xa5 >> (pj & 7)) & 1) != 0;
    const int unit = fast ? (w - 6) * 128 + pi * 8 + (pj >> 3) * 4 + fq : 1000;
    const auto do_unit = [&](unsigned bb, unsigned bs) AI {
      if (unit < 40) repack<1>(c, wo, unit / 10, unit % 10, bb, bs);
      else if (unit < 148) repack<2>(c, w1, (unit - 40) / 27, (unit - 40) % 27, bb, bs);
      else if (unit < 238) repack<3>(c, w2, (unit - 148) / 10, (unit - 148) % 10, bb, bs);
    };
    if (w == 7) {
      unsigned ad[10];
#pragma unroll
      for (int i = 0; i < 5; ++i) {
        const int ci = 64 * i + lane, rho = ci / 10, ch = ci - 10 * rho;
        const int k = 8 * ((rho >> 2) & 3) + 4 * (rho >> 4) + (rho & 3);
        ad[i] = k * 3840 + pj * 80 + ch * 16 + (ch < 5 ? 1280 : 2480);
        ad[5 + i] = k * 3840 + pj * 80 + ch * 16 + (ch < 5 ? 0 : 32 * 3840 - 80);
      }
      *LP(v4u, L_RPK + lane * 16) = v4u{ad[0], ad[1], ad[2], ad[3]};
      *LP(v4u, L_RPK + 1024 + lane * 16) = v4u{ad[4], ad[5], ad[6], ad[7]};
      *LP(v2u, L_RPK + 2048 + lane * 8) = v2u{ad[8], ad[9]};
      ctl_addc<C_ADR>();
    }
    const u64 e0 = __hip_atomic_load((GAS u64*)(c.ws + OFF_EPOCH), RLX_AGENT);
    {
      const unsigned lo = __builtin_amdgcn_readfirstlane((unsigned)e0), hi = __builtin_amdgcn_readfirstlane((unsigned)(e0 >> 32));
      c.ep = (((u64)hi << 32) | lo) + EPOCH_STEP;
    }
    do_unit(w == 6 ? L_RPK : L_SCR + 3072, w == 6 ? 1024 : 5120);
    if (unit < 238) { DRAIN(); flag_set(c, FLR, unit); }
    if (w == 6) {
      STAMP(22);
      {
        unsigned n_ = 0;
        while (min(__builtin_amdgcn_readfirstlane(*LP(volatile unsigned, L_LAND)), __builtin_amdgcn_readfirstlane(*LP(volatile unsigned, L_LAND + 256))) < needp(CH_KV) && ++n_ < (1u << 16)) __builtin_amdgcn_s_sleep(4);
        CFENCE();
      }
#pragma unroll
      for (int i = 0; i < 4; ++i) *LP(v4u, L_RPK + i * 1024 + lane * 16) = v4u{0u, 0u, 0u, 0u};
      ctl_addc<C_GO1>();
      const int g0 = gam0(qj);
      int idx = (lane & 3) * 10 + ((80 * cc + ((lane >> 2) & 1) * 79) >> 6);
      if (lane >= 8 && lane < 20) { const int cb = (g0 >> 2) + ((lane - 8) >> 2); if (cb < 27 && 4 * cb <= g0 + 6) idx = 40 + (lane & 3) * 27 + cb; }
      else if (lane >= 20 && lane < 38) { const int l = lane - 20; idx = 148 + (l >> 1) * 10 + ((80 * cc + (l & 1) * 79) >> 6); }
      ctl_wait<1>(c, C_RED, 1);
      flag_wait(c, FLR, idx, 8);
      if (lane == 0) *LP(volatile unsigned, L_CTL + 4 * C_CAP) = CH_C1;
      STAMP(20);
      flag_wait(c, FLR, idx, 38);
      if (lane == 0) *LP(volatile unsigned, L_CTL + 4 * C_CAP) = 1000u;
      if (lane < 28) {
        f32x4 s = {0.f, 0.f, 0.f, 0.f};
#pragma unroll
        for (int rb = 0; rb < 4; ++rb) s += __builtin_bit_cast(f32x4, ld128(c, lane * 16, OFF_CS + (rb * 1792 + 16 * g0) * 4));
        *LP(f32x4, L_CSUM + lane * 16) = s;
      }
      ctl_addc<C_CS>();
      DUMP(20, 21); DUMP(22, 23);
      return;
    }
    STAMP(21);
    unsigned ra[4];
#pragma unroll
    for (int i = 0; i < 4; ++i) {
      const int it = min(i * 64 + lane, 209);
      const int z = it >= 200, he = z ? (it - 200) / 5 : it / 5, d4 = it - 5 * (it / 5);
      const int hd = z ? he : he / 20, d4g = 5 * hd + d4, e8 = z ? 0 : he + 8;
      ra[i] = KVSLOT * 10240 + ((d4g >> 2) * 3 + (e8 >> 4)) * 1024 + ((d4g & 3) * 16 + (e8 & 15)) * 16;
      PIN(ra[i]);
    }
    int kvo = OFF_KVP + (pj * 16 + pi) * KVP_STR + lane * 16;
    PIN(kvo);
    ctl_wait<1>(c, C_ARR1, 4);
    {
      f32x4 p[4][4];
#pragma unroll
      for (int i = 0; i < 4; ++i)
#pragma unroll
        for (int j = 0; j < 4; ++j) p[i][j] = *LP(f32x4, ra[i] + j * 10240);
      ctl_addc<C_RED>();
#pragma unroll
      for (int i = 0; i < 4; ++i) {
        const f32x4 v = (p[i][0] + p[i][1]) + (p[i][2] + p[i][3]);
        if (i < 3 || lane < 24) st128(c, __builtin_bit_cast(v4u, v), kvo, i * 1024);
      }
    }
    DRAIN();
    flag_set(c, FL1, pj * 16 + pi);
    STAMP(23);
    flag_wait(c, FL1, pj * 16 + n16, 16);
    ctl_addc<C_GO1>();
    const int mate = qi * 16 + n16;
    ctl_wait<1>(c, C_ARR2, 4);
    flag_set(c, FL2, pi * 16 + pj);
    STAMP(24);
    flag_wait(c, FL2, mate, 16);
    ctl_addc<C_GO2>();
    if (b == 0 && lane == 0) __hip_atomic_store((GAS u64*)(c.ws + OFF_EPOCH), c.ep, RLX_AGENT);
    ctl_wait<1>(c, C_ARR3, 4);
    flag_set(c, FL3, qi * 16 + qj);
    STAMP(25);
    flag_wait(c, FL3, mate, 16);
    ctl_addc<C_GO3>();
    ctl_wait<1>(c, C_ARR4A, 4);
    flag_set(c, FL4A, qi * 16 + qj);
    STAMP(26);
    flag_wait(c, FL4A, mate, 16);
    ctl_addc<C_GO4A>();
    STAMP(27);
    ctl_wait<1>(c, C_ARR4B, 4);
    flag_set(c, FL4B, qi * 16 + qj);
    STAMP(28);
    flag_wait(c, FL4B, mate, 16);
    ctl_addc<C_GO4B>();
    STAMP(29);
    DUMP(23, 30); DUMP(21, 22);
    return;
  }

  const int xvoff = lane * 16;
  const unsigned dn = L_DONE + lane * 16 + w * 4;
  const auto nohook = [](auto) AI {};
  const unsigned scr = L_SCR + w * 5120;

  {
    v4u xa[2][20];
    unsigned pf = 0u;
    {
      const unsigned oc = (sx * 16 + w * 4 + lane) * 128;
      if (lane < 4 && oc < clen) pf = *(const GAS unsigned*)(cpc + oc);
    }
    {
      const int t2 = tid < 240 ? tid : tid - 40;
      const int z = t2 >= 200, zi = t2 - 200, zg = zi / 10;
      const int it = z ? 200 + zi - 10 * zg : t2;
      const int he = z ? (it - 200) / 5 : it / 5, d4 = it - 5 * (it / 5);
      const int hd = z ? he : he / 20, e = he - 20 * hd, gi = e / 5, r5 = e - 5 * gi;
      const int R = z || r5 == 4, row = z ? 4 * zg + 1 : r5 == 4 ? 4 * gi : 4 * gi + r5;
      const int dd = d4 + hd;
      *LP(v2u, scr + 2048 + lane * 8) = v2u{(unsigned)it * 16, (unsigned)(L_RPK + (hd * 2 + R) * 1024 + ((dd & 3) * 16 + row) * 16 + (dd >> 2) * 8)};
      CFENCE();
    }
    if (w == 0) STAMP(30);
    v4u ones = {ONE2, ONE2, ONE2, ONE2};
    PIN(ones);
    {
      const unsigned bw = scr + lane * 16;
      unsigned br = (w >> 1) * 10240 + (w & 1) * 5120 + (n16 >> 3) * 1024 + (n16 & 7) * 128 + ((g ^ (n16 >> 1)) * 16);
      f32x4 sm[2], sq[2];
#pragma unroll
      for (int m = 0; m < 2; ++m) { sm[m] = f32x4{0.f, 0.f, 0.f, 0.f}; sq[m] = sm[m]; }
      static_for<0, 11>([&](auto ic) AI {
        constexpr int i = decltype(ic)::v;
        if constexpr (i < 10) {
          ring_need<2 * i + 1>(c);
          if constexpr (i == 3) bump<61440>(br);
          if constexpr (i == 5) br += w >> 1 ? -71680 : 40960;
          if constexpr (i == 6) br += w >> 1 ? 20480 : -92160;
          if constexpr (i == 9) bump<61440>(br);
          constexpr int off = i < 3 ? i * 20480 : i < 5 ? (i - 3) * 20480 : i > 5 && i < 9 ? (i - 6) * 20480 : 0;
#pragma unroll
          for (int m = 0; m < 2; ++m) { xa[m][2 * i] = *LP(v4u, br + off + m * 2048); xa[m][2 * i + 1] = *LP(v4u, (br ^ 64) + off + m * 2048); }
          CFENCE();
          *LP(volatile unsigned, dn) = 2 * i + 2;
        }
        SCHED();
        if constexpr (i > 0) {
#pragma unroll
          for (int e = 0; e < 2; ++e)
#pragma unroll
            for (int m = 0; m < 2; ++m) {
              sm[m] = mfma(ones, xa[m][2 * i - 2 + e], sm[m]);
              sq[m] = mfma(xa[m][2 * i - 2 + e], xa[m][2 * i - 2 + e], sq[m]);
            }
        }
        SCHED();
      });
      *LP(f32x4, bw) = sq[0];
      *LP(f32x4, bw + 1024) = sq[1];
      CFENCE();
      const float q2 = *LP(float, scr + (g >> 1) * 1024 + ((n16 >> 2) * 16 + n16) * 16 + (n16 & 3) * 4);
      const float mean = (g >> 1 ? sm[1][0] : sm[0][0]) * (1.f / 640.f);
      const float a = rsqrtf(fmaxf(q2 * (1.f / 640.f) - mean * mean, 0.f) + 1e-5f);
      *LP(float, scr + 4864 + lane * 4) = (g & 1) ? -mean * a : a;
      CFENCE();
    }
    asm volatile("" :: "v"(pf));
    if (w == 0) STAMP(31);
    const unsigned rawlane = (4 * g + ((lane >> 2) & 3)) * 160 + 8 * (lane & 3);
    {
      f32x4 acc[2][5];
      {
        f32x4 ca = {0.f, 0.f, 0.f, 0.f}, c4 = ca;
        v4u cf;
        unsigned bc = rawlane + 32 * w + region(CH_KV), b4 = rawlane + 5120 * w + 128 + region(CH_KV);
        gemm_pass<W_KV, 5, 20, CH_KV, 10, 0, 20, 3>(c, dn, rawlane, acc, xa, [&](auto ic) AI {
          constexpr int f = decltype(ic)::v, ch = CH_KV + (f + 3) / 10, k = (f + 3) % 10;
          if constexpr (ch < CH_Q) {
            if constexpr (k == 0 && (ch & 1)) {
              static_assert(slot(ch) == slot(ch - 1) + 1, "chunk pair must be contiguous");
              if constexpr (ch > CH_KV + 1) bump<region(ch - 1) - region(ch - 3)>(b4);
              cf = tr2(b4, inreg(ch - 1), 2560);
            }
            if constexpr (k == 3) {
              if constexpr (ch & 1) c4 = mfma(ones, cf, c4);
              if constexpr (ch > CH_KV) bump<region(ch) - region(ch - 1)>(bc);
              cf = tr2(bc, inreg(ch), 2560);
            }
            if constexpr (k == 6) { ca = mfma(ones, cf, ca); cf = tr2(bc, inreg(ch) + 5120, 2560); }
            if constexpr (k == 9) ca = mfma(ones, cf, ca);
          }
          if constexpr (f == 98) {
            const unsigned a = scr + 4096 + opaque(lane) * 4;
            *LP(float, a) = ca[0]; *LP(float, a + 256) = c4[0];
            ctl_addc<C_CSK>();
          }
        });
      }
      if (w == 0) STAMP(4);
      f32x4 a4[2], b4[2];
#pragma unroll
      for (int m = 0; m < 2; ++m) { a4[m] = *LP(f32x4, scr + 4864 + m * 128 + g * 16); b4[m] = *LP(f32x4, scr + 4864 + m * 128 + 64 + g * 16); }
      ctl_wait(c, C_CSK, 4);
      float cs[5];
      {
        const unsigned a = L_SCR + 4096 + n16 * 4;
#pragma unroll
        for (int t = 0; t < 4; ++t) cs[t] = *LP(float, a + t * 5120);
        cs[4] = (*LP(float, a + 256) + *LP(float, a + 5376)) + (*LP(float, a + 10496) + *LP(float, a + 15616));
      }
      v4u pk[3], vv[3];
#pragma unroll
      for (int t = 0; t < 3; ++t) {
        const f32x4 t0 = acc[0][t] * a4[0] + (b4[0] * cs[t] + 1e-6f), t1 = acc[1][t] * a4[1] + (b4[1] * cs[t] + 1e-6f);
        const v2u p0 = phi4(t0), p1 = phi4(t1);
        pk[t] = v4u{p0.x, p0.y, p1.x, p1.y};
      }
#pragma unroll
      for (int t = 2; t < 5; ++t) {
        const f32x4 t0 = acc[0][t] * a4[0] + b4[0] * cs[t], t1 = acc[1][t] * a4[1] + b4[1] * cs[t];
        vv[t - 2] = v4u{pk2(t0[0], t0[1]), pk2(t0[2], t0[3]), pk2(t1[0], t1[1]), pk2(t1[2], t1[3])};
      }
      if (n16 < 8) vv[0] = ones;
      const unsigned kp = (KVSLOT + w) * 10240 + opaque(lane) * 16;
      const f32x4 z4 = {0.f, 0.f, 0.f, 0.f};
#pragma unroll
      for (int ta = 0; ta < 3; ++ta)
#pragma unroll
        for (int tb = 0; tb < 3; ++tb)
          if (ta != 0 || tb != 2) *LP(f32x4, kp + (ta * 3 + tb) * 1024) = mfma(pk[ta], vv[tb], z4);
      ctl_addc<C_ARR1>();
    }
    if (w == 0) STAMP(3);
    v2u pq[2][3];
    {
      f32x4 aq[2][3];
      f32x4 ca = {0.f, 0.f, 0.f, 0.f};
      v4u cf;
      unsigned bc = rawlane + 32 * min(w, 2) + region(CH_Q);
      gemm_pass<W_Q, 3, 20, CH_Q, 12, 1, 20, 3>(c, dn, rawlane, aq, xa, [&](auto ic) AI {
        constexpr int f = decltype(ic)::v, ch = CH_Q + (f + 3) / 12, k = (f + 3) % 12;
        if constexpr (k % 3 == 0 && f <= 57) {
          if constexpr (f > -3) ca = mfma(ones, cf, ca);
          if constexpr (k == 0 && f > -3 && f < 57) bump<region(ch) - region(ch - 1)>(bc);
          if constexpr (f < 57) cf = tr2(bc, inreg(ch) + foff<W_Q>(k), 2560);
        }
        if constexpr (f == 59) { *LP(float, scr + 4096 + 512 + opaque(lane) * 4) = ca[0]; ctl_addc<C_CSQ>(); }
      });
      float sa[2], sb[2];
#pragma unroll
      for (int m = 0; m < 2; ++m) { sa[m] = *LP(float, scr + 4864 + m * 128 + n16 * 4); sb[m] = *LP(float, scr + 4864 + m * 128 + 64 + n16 * 4); }
      ctl_wait(c, C_CSQ, 4);
#pragma unroll
      for (int t = 0; t < 3; ++t) {
        const f32x4 cs = *LP(f32x4, L_SCR + t * 5120 + 4096 + 512 + g * 16);
#pragma unroll
        for (int m = 0; m < 2; ++m) {
          pq[m][t] = phi4(aq[m][t] * sa[m] + (cs * sb[m] + 1e-6f));
          if (t == 2 && g >= 2) pq[m][t] = v2u{0u, 0u};
        }
      }
    }
    v4u qb[2][2];
#pragma unroll
    for (int m = 0; m < 2; ++m)
#pragma unroll
      for (int hh = 0; hh < 2; ++hh) { qb[m][hh] = v4u{pq[m][hh].x, pq[m][hh].y, pq[m][hh + 1].x, pq[m][hh + 1].y}; PIN(qb[m][hh]); }
    v2u ga = *LP(v2u, scr + 2048 + opaque(lane) * 8);
    PIN(ga);
    const int gsrc = ga.x;
    const unsigned gdst = ga.y;
    const int mt0 = 8 * pi + 2 * w;
    int oa = OFF_O + (mt0 * 20 + pj) * 1024 + xvoff;
    int ob = OFF_O + (mt0 * 20 + 16 + (pj >> 2)) * 1024 + (pj & 3) * 256 + g * 20480 + n16 * 16;
    unsigned fa = L_RPK + xvoff;
    PIN(oa); PIN(ob); PIN(fa);
    if (w == 0) STAMP(5);
    ctl_wait(c, C_GO1, 2);
    if (w == 0) STAMP(6);
    {
      v4u p[16];
#pragma unroll
      for (int i = 0; i < 16; ++i) p[i] = ld128(c, gsrc, OFF_KVP + (pj * 16 + i) * KVP_STR);
      f32x4 s = __builtin_bit_cast(f32x4, p[0]);
#pragma unroll
      for (int i = 1; i < 16; ++i) s += __builtin_bit_cast(f32x4, p[i]);
      *LP(v2u, gdst) = v2u{pk2(s[0], s[1]), pk2(s[2], s[3])};
    }
    ctl_addc<C_BAR1>();
    ctl_wait(c, C_BAR1, 4);
    if (w == 0) STAMP(7);
    v4u ka_[4];
#pragma unroll
    for (int i = 0; i < 4; ++i) ka_[i] = *LP(v4u, fa + (i ^ 1) * 1024);
    f32x4 on[2][2][2];
    const f32x4 z4 = {0.f, 0.f, 0.f, 0.f};
#pragma unroll
    for (int i = 0; i < 4; ++i)
#pragma unroll
      for (int m = 0; m < 2; ++m) on[m][i >> 1][(i & 1) ^ 1] = mfma(ka_[i], qb[m][i >> 1], z4);
    unsigned tl[2];
#pragma unroll
    for (int m = 0; m < 2; ++m) {
      const float r0 = __builtin_amdgcn_rcpf(on[m][0][1][1] + 1e-6f), r1 = __builtin_amdgcn_rcpf(on[m][1][1][1] + 1e-6f);
      const v4u o = {pk2(on[m][0][0][0] * r0, on[m][0][0][1] * r0), pk2(on[m][0][0][2] * r0, on[m][0][0][3] * r0),
                     pk2(on[m][0][1][0] * r0, on[m][1][0][0] * r1), pk2(on[m][1][0][1] * r1, on[m][1][0][2] * r1)};
      tl[m] = pk2(on[m][1][0][3] * r1, on[m][1][1][0] * r1);
      st128(c, o, oa, m * 20480);
    }
    const v2u t0 = swap16(tl[0], tl[1]);
    const v2u u0 = swap32(t0.x, t0.x), u1 = swap32(t0.y, t0.y);
    if (lane < 32) st128(c, v4u{u0.x, u1.x, u0.y, u1.y}, ob, 0);
  }
  if (w == 0) STAMP(8);
  DRAIN();
  ctl_addc<C_ARR2>();
  const int kb = w & 1;
  const int mtB = 4 * hp + w;
  unsigned hq[10];

  {
    f32x4 acc[2][5];
    {
      const char* xb0 = x + (size_t)(16 * mtB * 1280 + cc * 160);
      const unsigned rowoff = n16 * 1280 + g * 16;
      const v4u xr0 = *(const GAS v4u*)(xb0 + rowoff), xr1 = *(const GAS v4u*)(xb0 + rowoff + 64);
      const v2u xr2 = *(const GAS v2u*)(xb0 + (rowoff - g * 8 + 128));
      acc[0][0] = unpk4(xr0.x, xr0.y); acc[0][1] = unpk4(xr0.z, xr0.w);
      acc[0][2] = unpk4(xr1.x, xr1.y); acc[0][3] = unpk4(xr1.z, xr1.w);
      acc[0][4] = unpk4(xr2.x, xr2.y);
#pragma unroll
      for (int t = 0; t < 5; ++t) PIN(acc[0][t]);
    }
    const int sb = 5 * (cc >> 1) + 3 * (cc & 1);
    int ha = OFF_H + (mtB * 20 + sb) * 1024 + xvoff;
    int hb = OFF_H + (mtB * 20 + 5 * (cc >> 1) + 2) * 1024 + (cc & 1) * 512 + g * 128 + n16 * 16;
    PIN(ha); PIN(hb);
    ctl_wait(c, C_GO2, 1);
    if (w == 0) STAMP(9);
    v4u xb[2][10];
    const int xs = OFF_O + mtB * 20480 + kb * 10240, xm = kb ? -20480 : 20480;
    xload<10, 0, 0, PRE_B, 10>(c, xb, xvoff, xs, xm, rotB);
    gemm_pass<W_PK, 5, 10, CH_B, 5, 1, 10, 2>(c, dn, xvoff + kb * 5120, acc, xb, [&](auto ic) AI {
      constexpr int f = decltype(ic)::v;
      if constexpr (f >= 0 && f % 5 == 1 && f / 5 + PRE_B < 10) xload<10, 0, f / 5 + PRE_B, 1, 10>(c, xb, xvoff, xs, xm, rotB);
    });
    if (w == 0) STAMP(1);
    pair_reduce(c, w, C_XB0, acc, hq);
    st128(c, v4u{hq[0], hq[1], hq[2], hq[3]}, ha, 0);
    st128(c, v4u{hq[4], hq[5], hq[6], hq[7]}, ha, 1024);
    const v2u ta = swap16(hq[8], hq[8]), tb = swap16(hq[9], hq[9]);
    if ((g & 1) == 0) st128(c, v4u{ta.x, tb.x, ta.y, tb.y}, hb, 0);
    float s1 = 0.f, s2 = 0.f;
#pragma unroll
    for (int i = 0; i < 10; ++i) { s1 = dot2(hq[i], ONE2, s1); s2 = dot2(hq[i], hq[i], s2); }
    s1 = sum_g(s1); s2 = sum_g(s2);
    if (g == 0) st64(c, v2u{asu(s1), asu(s2)}, n16 * 8, OFF_HS + mtB * 1024 + cc * 128);
  }
  if (w == 0) STAMP(10);
  DRAIN();
  ctl_addc<C_ARR3>();

  {
    const int mt0 = 8 * qi + 2 * w;
    ctl_wait(c, C_CS, 1);
    f32x4 acc[2][4];
#pragma unroll
    for (int t = 0; t < 4; ++t) { acc[0][t] = *LP(f32x4, L_CSUM + t * 64 + g * 16); PIN(acc[0][t]); }
    ctl_wait(c, C_GO3, 1);
    if (w == 0) STAMP(11);
    v2u st[2][2];
#pragma unroll
    for (int m = 0; m < 2; ++m)
#pragma unroll
      for (int i = 0; i < 2; ++i) st[m][i] = ld64(c, lane * 8, OFF_HS + (mt0 + m) * 1024 + i * 512);
    v4u xh[2][20];
    xload<20, 0, 0, PRE_C, 20>(c, xh, xvoff, OFF_H + mt0 * 20480, 20480, rotC);
    float mneg[2], s2[2];
#pragma unroll
    for (int m = 0; m < 2; ++m) {
      mneg[m] = sum_g(asf(st[m][0].x) + asf(st[m][1].x)) * (-1.f / 640.f);
      s2[m] = asf(st[m][0].y) + asf(st[m][1].y);
    }
#pragma unroll
    for (int t = 0; t < 4; ++t) { acc[1][t] = acc[0][t] * mneg[1]; acc[0][t] = acc[0][t] * mneg[0]; PIN(acc[0][t]); PIN(acc[1][t]); }
    float sd[2], c2[2];
    gemm_pass<W_PK, 4, 20, CH_C1, 10, 1, 20, 0>(c, dn, xvoff, acc, xh, [&](auto ic) AI {
      constexpr int f = decltype(ic)::v;
      if constexpr (f >= 0 && f % 4 == 1 && f / 4 + PRE_C < 20) xload<20, 0, f / 4 + PRE_C, 1, 20>(c, xh, xvoff, OFF_H + mt0 * 20480, 20480, rotC);
      if constexpr (f == 2 || f == 6) {
        constexpr int m = f == 6;
        const float var = fmaxf(sum_g(s2[m]) * (1.f / 640.f) - mneg[m] * mneg[m], 0.f) + 1e-5f;
        const float rs = __builtin_amdgcn_rsqf(var);
        sd[m] = var * rs;
        c2[m] = -1.4426950408889634f * rs;
        PIN(sd[m]); PIN(c2[m]);
      }
    });
    if (w == 0) STAMP(12);
    const auto silu2 = [&](int m, const f32x4& a) AI {
      float y[4];
#pragma unroll
      for (int i = 0; i < 4; ++i) {
        const float e = __builtin_amdgcn_exp2f(a[i] * c2[m]);
        y[i] = a[i] * __builtin_amdgcn_rcpf(fmaf(e, sd[m], sd[m]));
      }
      return v2u{pk2(y[0], y[1]), pk2(y[2], y[3])};
    };
    const int fb = OFF_F + mt0 * 55296;
    f32x4 ac2[2][3];
#pragma unroll
    for (int t = 0; t < 3; ++t) ac2[0][t] = *LP(f32x4, L_CSUM + (4 + t) * 64 + g * 16);
#pragma unroll
    for (int m = 0; m < 2; ++m)
#pragma unroll
      for (int v = 0; v < 2; ++v) {
        const v2u a = silu2(m, acc[m][2 * v]), d = silu2(m, acc[m][2 * v + 1]);
        st128(c, v4u{a.x, a.y, d.x, d.y}, xvoff, fb + m * 55296 + (2 * qj + v) * 1024);
      }
#pragma unroll
    for (int t = 0; t < 3; ++t) { ac2[1][t] = ac2[0][t] * mneg[1]; ac2[0][t] = ac2[0][t] * mneg[0]; PIN(ac2[0][t]); PIN(ac2[1][t]); }
    if (w == 0) STAMP(2);
    gemm_pass<W_PK, 3, 20, CH_C2, 10, 1, 20, 0>(c, dn, xvoff, ac2, xh, [&](auto ic) AI {
      if constexpr (decltype(ic)::v == 18) { DRAIN(); ctl_addc<C_ARR4A>(); }
    });
    if (w == 0) STAMP(13);
#pragma unroll
    for (int m = 0; m < 2; ++m) {
      const v2u a = silu2(m, ac2[m][0]), d = silu2(m, ac2[m][1]);
      st128(c, v4u{a.x, a.y, d.x, d.y}, xvoff, fb + m * 55296 + (32 + qj) * 1024);
    }
    if ((qj & 3) != 3) {
      const v2u a = silu2(0, ac2[0][2]), d = silu2(1, ac2[1][2]);
      const v2u ta = swap16(a.x, d.x), tb = swap16(a.y, d.y);
      const int l = 3 * (qj >> 2) + (qj & 3);
      st128(c, v4u{ta.x, tb.x, ta.y, tb.y}, (g & 1) * 55296 + (g >> 1) * 256 + n16 * 16, fb + (48 + (l >> 1)) * 1024 + (l & 1) * 512);
    }
  }
  if (w == 0) STAMP(14);

  {
    const int xs = OFF_F + mtB * 55296, xm = kb ? -55296 : 55296;
    const bool early = ctl_read(C_GO4A) != 0u;
    CFENCE();
    if (!early) { DRAIN(); ctl_addc<C_ARR4B>(); ctl_wait(c, C_GO4A, 1); }
    v4u xf[2][20];
    xload<20, 0, 0, PRE_D, 16>(c, xf, xvoff, xs + kb * 16384, xm, rotD1);
    f32x4 acc[2][5];
#pragma unroll
    for (int t = 0; t < 5; ++t) { acc[0][t] = unpk4(hq[2 * t], hq[2 * t + 1]); PIN(acc[0][t]); }
    if (early) { asm volatile("s_waitcnt vmcnt(%0)" :: "i"(2 * PRE_D) : "memory"); ctl_addc<C_ARR4B>(); }
    if (w == 0) STAMP(16);
    gemm_pass<W_PK, 5, 27, CH_D, 5, 1, 20, 2>(c, dn, xvoff + kb * 5120, acc, xf, [&](auto ic) AI {
      constexpr int f = decltype(ic)::v, sn = f / 5 + PRE_D;
      if constexpr (f >= 0 && f % 5 == 1 && sn < 27) {
        if constexpr (sn == 16) {
          if (w == 0) STAMP(17);
          ctl_wait(c, C_GO4B, 1);
          if (w == 0) STAMP(18);
        }
        if constexpr (sn < 16) xload<20, 0, sn, 1, 16>(c, xf, xvoff, xs + kb * 16384, xm, rotD1);
        else xload<20, 16, sn - 16, 1, 11>(c, xf, xvoff, xs + 32768 + kb * 11264, xm, rotD2);
      }
    });
    if (w == 0) STAMP(19);
    unsigned y[10];
    pair_reduce(c, w, C_XD0, acc, y);
    const int l2 = opaque(lane);
    char* ob = out + (size_t)(16 * mtB * 1280 + cc * 160);
    const unsigned ro = (l2 & 15) * 1280 + (l2 >> 4) * 16;
    *(GAS v4u*)(ob + ro) = v4u{y[0], y[1], y[2], y[3]};
    *(GAS v4u*)(ob + ro + 64) = v4u{y[4], y[5], y[6], y[7]};
    *(GAS v2u*)(ob + (ro - (l2 >> 4) * 8 + 128)) = v2u{y[8], y[9]};
  }
  if (w == 0) { STAMP(15); DUMP(0, 20); DUMP(30, 32); }
}

extern "C" void kernel_launch(void* const* d_in, const int* in_sizes, int n_in, void* d_out, int out_size, void* d_ws, size_t ws_size,
                              hipStream_t stream) {
  (void)hipFuncSetAttribute((const void*)mk_kernel, hipFuncAttributeMaxDynamicSharedMemorySize, LDS_BYTES);
  mk_kernel<<<256, 512, LDS_BYTES, stream>>>((const char*)d_in[0], (const char*)d_in[1], (const char*)d_in[2], (const char*)d_in[4],
                                             (const char*)d_in[5], (char*)d_out, (char*)d_ws);
}
```

```cpp
#define PADK 5
#include <hip/hip_runtime.h>
#include <stdint.h>

typedef unsigned v4u __attribute__((ext_vector_type(4)));
typedef unsigned v2u __attribute__((ext_vector_type(2)));
typedef short v4s __attribute__((ext_vector_type(4)));
typedef float f32x4 __attribute__((ext_vector_type(4)));
typedef float f32x2 __attribute__((ext_vector_type(2)));
typedef __bf16 bf16x2 __attribute__((ext_vector_type(2)));
typedef __bf16 bf16x8 __attribute__((ext_vector_type(8)));
typedef unsigned long long u64;
typedef __amdgpu_buffer_rsrc_t rsrc_t;
#define LAS __attribute__((address_space(3)))
#define GAS __attribute__((address_space(1)))
#define DI __device__ __forceinline__
#define AI __attribute__((always_inline))
#define RLX_AGENT __ATOMIC_RELAXED, __HIP_MEMORY_SCOPE_AGENT
#define SCHED() __builtin_amdgcn_sched_barrier(0)
#define CFENCE() asm volatile("" ::: "memory")
#define DRAIN() asm volatile("s_waitcnt vmcnt(0)" ::: "memory")
#define VMWAIT(n) asm volatile("s_waitcnt vmcnt(" #n ")" ::: "memory")
#define LGKM0() asm volatile("s_waitcnt lgkmcnt(0)" ::: "memory")
#define PIN(v) asm volatile("" : "+v"(v))
#define LP(T, a) ((LAS T*)(uintptr_t)(unsigned)(a))
constexpr int SC1 = 16;
constexpr unsigned ONE2 = 0x3f803f80u;

#define DEP_A 55
#define DEP_P 55
#define XPRE 6
#define RPK_AT 19
#define STR_(x) #x
#define STR(x) STR_(x)

#define RPK_AT1 22
#define RPK_AUX 0
#define RPK_PRIO 3

#define ROT 1
#define PRE_B 6
#define PRE_C 8
#define PRE_D 8

constexpr int OFF_EPOCH = 0;
constexpr int OFF_FLAGS = 4096;
constexpr int OFF_CS = 262144;
constexpr int OFF_HS = 327680;
constexpr int OFF_KVP = 524288;
constexpr int KVP_STR = 3456;
constexpr int OFF_WB = 2 << 20;
constexpr int OFF_WC = OFF_WB + 819200;
constexpr int OFF_WD = OFF_WC + 2293760;
constexpr int OFF_O = OFF_WD + 2211840;
constexpr int OFF_H = OFF_O + 2621440;
constexpr int OFF_F = OFF_H + 2621440;
constexpr int WS_BYTES = OFF_F + 7077888;
constexpr int OFF_PROF = 32 << 20;
enum { FLR = 0, FL1, FL2, FL3, FL4A, FL4B };
constexpr u64 EPOCH_STEP = 0x9E3779B97F4A7C15ull;

constexpr int RCH = 13;
constexpr int L_SCR = RCH * 10240;
constexpr int L_RPK = L_SCR + 20480;
constexpr int L_CSUM = L_RPK + 4096;
constexpr int L_DONE = L_CSUM + 512;
constexpr int L_LAND = L_DONE + 1024;
constexpr int L_CTL = L_LAND + 512;
constexpr int L_PROF = L_CTL + 128;
constexpr int LDS_BYTES = L_PROF;
static_assert(LDS_BYTES <= 163840, "lds");
enum { C_CAP = 0, C_CSK, C_CSQ, C_RED, C_CS, C_ARR1, C_GO1, C_BAR1, C_ARR2, C_GO2, C_XB0, C_XB1, C_ARR3, C_GO3, C_ARR4A, C_GO4A,
       C_ARR4B, C_GO4B, C_XD0, C_XD1, C_ADR };

constexpr int CH_KV = 20, CH_Q = 30, CH_B = 35, CH_C1 = 45, CH_C2 = 53, CH_D = 59, CH_END = 86;
constexpr int needp(int ch) { return ch < 11 ? 4 * ch + 4 : ch < CH_KV ? 4 * ch + 14 : ch < CH_KV + 2 ? 5 * ch - 51 : 5 * ch - 15; }
constexpr int slot(int ch) { return ch < 11 ? ch : ch < CH_KV ? ch - 11 : (ch - 9) % RCH; }
constexpr int KVSLOT = slot(CH_B);
static_assert(KVSLOT + 4 <= RCH, "kv slots");

struct Ctx {
  rsrc_t rs;
  GAS char* ws;
  u64 ep;
  int lane;
  unsigned landed, budget;
};

DI f32x4 mfma(v4u a, v4u b, f32x4 c) {
  return __builtin_amdgcn_mfma_f32_16x16x32_bf16(__builtin_bit_cast(bf16x8, a), __builtin_bit_cast(bf16x8, b), c, 0, 0, 0);
}
DI float asf(unsigned u) { return __builtin_bit_cast(float, u); }
DI unsigned asu(float f) { return __builtin_bit_cast(unsigned, f); }
DI unsigned pk2(float a, float b) { f32x2 f = {a, b}; return __builtin_bit_cast(unsigned, __builtin_convertvector(f, bf16x2)); }
DI float bflo(unsigned u) { return asf(u << 16); }
DI float bfhi(unsigned u) { return asf(u & 0xffff0000u); }
DI f32x4 unpk4(unsigned a, unsigned b) { return f32x4{bflo(a), bfhi(a), bflo(b), bfhi(b)}; }
DI float dot2(unsigned a, unsigned b, float c) {
  return __builtin_amdgcn_fdot2_f32_bf16(__builtin_bit_cast(bf16x2, a), __builtin_bit_cast(bf16x2, b), c, false);
}
DI v4u ld128(const Ctx& c, int voff, int soff) { return __builtin_amdgcn_raw_buffer_load_b128(c.rs, voff, soff, SC1); }
DI v2u ld64(const Ctx& c, int voff, int soff) { return __builtin_amdgcn_raw_buffer_load_b64(c.rs, voff, soff, SC1); }
DI void st128(const Ctx& c, v4u v, int voff, int soff) { __builtin_amdgcn_raw_buffer_store_b128(v, c.rs, voff + soff, 0, SC1); }
DI void st64(const Ctx& c, v2u v, int voff, int soff) { __builtin_amdgcn_raw_buffer_store_b64(v, c.rs, voff, soff, SC1); }
DI void st32(const Ctx& c, unsigned v, int voff, int soff) { __builtin_amdgcn_raw_buffer_store_b32(v, c.rs, voff, soff, SC1); }
DI u64 now() { return __builtin_amdgcn_s_memrealtime(); }

DI void ctl_add(int i) {
  asm volatile("v_mov_b32 v254, %0\n\tv_mov_b32 v255, %1\n\ts_mov_b64 exec, 1\n\tds_add_u32 v254, v255\n\ts_mov_b64 exec, -1"
               :: "s"(L_CTL + 4 * i), "i"(L_CTL) : "memory", "v254", "v255");
}
template <int I> DI void ctl_addc() {
  asm volatile("v_mov_b32 v254, %0\n\ts_mov_b64 exec, 1\n\tds_add_u32 v254, v254 offset:%1\n\ts_mov_b64 exec, -1"
               :: "i"(L_CTL), "i"(4 * I) : "memory", "v254");
}
template <int SLP = 0> DI void ctl_wait(Ctx& c, int i, int n) {
  unsigned t;
  asm volatile(
      "v_mov_b32 v254, %2\n"
      ".Lw%=:\n\t"
      ".if %4\n\ts_sleep %4\n\t.endif\n\t"
      "ds_read_b32 v255, v254\n\t"
      "s_sub_u32 %0, %0, 1\n\t"
      "s_max_i32 %0, %0, 0\n\t"
      "s_waitcnt lgkmcnt(0)\n\t"
      "v_readfirstlane_b32 %1, v255\n\t"
      "s_cmp_lt_u32 %1, %3\n\t"
      "s_cselect_b32 %1, %0, 0\n\t"
      "s_cmp_lg_u32 %1, 0\n\t"
      "s_cbranch_scc1 .Lw%="
      : "+{s71}"(c.budget), "=&s"(t) : "s"(L_CTL + 4 * i), "s"(n * L_CTL), "i"(SLP) : "memory", "scc", "v254", "v255");
}
DI unsigned ctl_read(int i) { return __builtin_amdgcn_readfirstlane(*LP(volatile unsigned, L_CTL + 4 * i)); }
DI void flag_set(const Ctx& c, int kind, int idx) {
  if (c.lane == 0) __hip_atomic_store((GAS u64*)(c.ws + OFF_FLAGS + (kind * 256 + idx) * 128), c.ep, RLX_AGENT);
}
DI void flag_wait(const Ctx& c, int kind, int idx, int nl) {
  if (c.lane < nl) {
    GAS u64* p = (GAS u64*)(c.ws + OFF_FLAGS + (kind * 256 + idx) * 128);
    unsigned n = 1u << 17;
    bool go;
    do {
      const u64 v = __hip_atomic_load(p, RLX_AGENT);
      go = __any(v != c.ep) && --n != 0;
    } while (go);
  }
  CFENCE();
}
#define PADTO(k, us) do { if (PADK == (k)) { while (now() - tstart < (u64)(us) * 100) __builtin_amdgcn_s_sleep(1); } } while (0)
#define STAMP(i) do {} while (0)
#define DUMP(lo, hi) do {} while (0)

DI v2u swap16(unsigned a, unsigned b) { return __builtin_amdgcn_permlane16_swap(a, b, false, false); }
DI v2u swap32(unsigned a, unsigned b) { return __builtin_amdgcn_permlane32_swap(a, b, false, false); }
DI float sum_g(float v) {
  v2u r = swap16(asu(v), asu(v));
  v = asf(r.x) + asf(r.y);
  r = swap32(asu(v), asu(v));
  return asf(r.x) + asf(r.y);
}
DI v2u tr1(unsigned a, int off) { return __builtin_bit_cast(v2u, __builtin_amdgcn_ds_read_tr16_b64_v4i16(LP(v4s, a + off))); }
DI v4u tr2(unsigned a, int off, int hioff) {
  const v2u lo = tr1(a, off), hi = tr1(a, off + hioff);
  return v4u{lo.x, lo.y, hi.x, hi.y};
}

DI int gam0(int j) { return 27 * (j >> 2) + 7 * (j & 3); }
template <int KIND> DI int kbase(int s, int g) {
  if (KIND == 1) return s < 16 ? 40 * s + 5 * g : 40 * (4 * (s - 16) + g) + 23;
  if (KIND == 2) {
    const int u = s / 5, v = s - 5 * u;
    return 160 * u + (v < 2 ? 32 * v + 8 * g : v == 2 ? 64 + (g >> 1) * 80 + 8 * (g & 1) : 32 * v - 16 + 8 * g);
  }
  if (s < 48) return 16 * (s < 32 ? gam0(s >> 1) + 2 * (s & 1) : gam0(s - 32) + 4) + 4 * g;
  const int l = 2 * (s - 48) + (g >> 1), q = l / 3;
  return 16 * (gam0(4 * q + (l - 3 * q)) + 6) + 8 * (g & 1);
}
template <int KIND> DI int kpat(int s, int j) {
  if (KIND == 1) return s < 16 ? (j >= 5 ? 15 + j : j) : 5 * (j >> 1) + (j & 1);
  if (KIND == 2) return j;
  return s < 48 ? 16 * (j >> 2) + (j & 3) : j;
}
template <int KIND> DI int sglob(int rb, int sp) {
  if (KIND == 1) return sp < 4 ? 4 * rb + sp : 16 + rb;
  if (KIND == 3) return 6 * rb + sp;
  return 5 * rb + sp;
}

static_assert(needp(RPK_AT) >= needp(CH_KV), "loader addresses live in the bounce buffer");
template <int KIND> DI void repack(Ctx& c, const char* W, int rb, int cb, unsigned bb, unsigned bs) {
  constexpr int NS = KIND == 3 ? 6 : 5;
  constexpr int PITCH = KIND == 2 ? 3456 : 1280;
  const rsrc_t rr_ = __builtin_amdgcn_make_buffer_rsrc((void*)W, 0, KIND == 1 ? 819200 : 2211840, 0x00020000);
  const int l = c.lane, n = l & 15, g = l >> 4, q = (l >> 2) & 3, p = l & 3;
  int va = kpat<KIND>(sglob<KIND>(rb, 0), l >> 3) * PITCH + (l & 7) * 16, vb = kpat<KIND>(sglob<KIND>(rb, NS - 1), l >> 3) * PITCH + (l & 7) * 16;
  int so[NS][4];
#pragma unroll
  for (int sp = 0; sp < NS; ++sp)
#pragma unroll
    for (int gg = 0; gg < 4; ++gg) { so[sp][gg] = kbase<KIND>(sglob<KIND>(rb, sp), gg) * PITCH + cb * 128; asm volatile("" : "+s"(so[sp][gg])); }
  int base[4];
  unsigned src[4];
#pragma unroll
  for (int ww = 0; ww < 4; ++ww) {
    const int gam = 4 * cb + ww;
    int sig = p;
    if (KIND == 2) {
      const int grp = gam / 27, rr = gam - 27 * grp, jj = min(rr / 7, 3), t = rr - 7 * jj;
      base[ww] = OFF_WC + (4 * grp + jj) * 143360 + (t < 4 ? t + 20 * rb : 76 + t + 15 * rb) * 1024 + l * 16;
      if (t >= 4) base[ww] |= 1;
    } else {
      const int cc = gam / 5, cq = gam - 5 * cc;
      int tile = 4, slot = n;
      if (cq < 4) { sig = ((p & 1) << 1) | (p >> 1); tile = 2 * (cq >> 1) + (n >> 3); slot = 8 * (cq & 1) + (n & 7); }
      base[ww] = (KIND == 1 ? OFF_WB + cc * 102400 : OFF_WD + cc * 276480) + tile * 1024 + (g * 16 + slot) * 16;
    }
    src[ww] = bb + g * bs + q * 128 + 32 * ww + 8 * sig;
  }
#pragma unroll
  for (int ww = 0; ww < 4; ++ww) { PIN(base[ww]); PIN(src[ww]); }
  PIN(va); PIN(vb);
  {
    unsigned n_ = 0;
    while (min(__builtin_amdgcn_readfirstlane(*LP(volatile unsigned, L_LAND)), __builtin_amdgcn_readfirstlane(*LP(volatile unsigned, L_LAND + 256))) < needp(RPK_AT) && ++n_ < (1u << 16)) __builtin_amdgcn_s_sleep(4);
    CFENCE();
  }
  v4u r[NS][4];
#pragma unroll
  for (int sp = 0; sp < NS; ++sp)
#pragma unroll
    for (int gg = 0; gg < 4; ++gg) r[sp][gg] = __builtin_amdgcn_raw_buffer_load_b128(rr_, sp == NS - 1 ? vb : va, so[sp][gg], RPK_AUX);
  float cs[4] = {0.f, 0.f, 0.f, 0.f};
#pragma unroll
  for (int sp = 0; sp < NS; ++sp) {
#pragma unroll
    for (int gg = 0; gg < 4; ++gg) *LP(v4u, bb + gg * bs + l * 16) = r[sp][gg];
    CFENCE();
    const int s = sglob<KIND>(rb, sp);
    v4u v[4];
#pragma unroll
    for (int ww = 0; ww < 4; ++ww) v[ww] = tr2(src[ww], 0, 512);
    CFENCE();
#pragma unroll
    for (int ww = 0; ww < 4; ++ww) {
      int off;
      if (KIND == 1) off = base[ww] + (s >= 10 ? (s - 10) * 10 + 5 : s * 10) * 1024;
      else if (KIND == 3) off = base[ww] + (s < 16 ? s * 10 : s < 32 ? (s - 16) * 10 + 5 : s < 43 ? (s - 16) * 10 : (s - 27) * 10 + 5) * 1024;
      else off = (base[ww] & ~1) + sp * ((base[ww] & 1) ? 3072 : 4096);
      st128(c, v[ww], off, 0);
      if (KIND == 2) cs[ww] = dot2(v[ww].x, ONE2, dot2(v[ww].y, ONE2, dot2(v[ww].z, ONE2, dot2(v[ww].w, ONE2, cs[ww]))));
    }
  }
  if (KIND == 2) {
#pragma unroll
    for (int ww = 0; ww < 4; ++ww) cs[ww] = sum_g(cs[ww]);
    const float v = g == 0 ? cs[0] : g == 1 ? cs[1] : g == 2 ? cs[2] : cs[3];
    st32(c, asu(v), l * 4, OFF_CS + (rb * 1792 + 64 * cb) * 4);
  }
}

template <int CH> DI void ring_need(Ctx& c) {
  asm volatile("s_getpc_b64 s[72:73]\n\ts_cmpk_lt_u32 %0, %2\n\ts_cbranch_scc1 .Lmk_ring"
               : "+{s70}"(c.landed), "+{s71}"(c.budget) : "i"(needp(CH)) : "memory", "scc", "s72", "s73", "v254", "v255");
}
constexpr int upto(int ch) {
  if (ch < CH_Q) return ch;
  if (ch == CH_Q) return CH_B - 1;
  if (ch == CH_B) return CH_C1 - 1;
  if (ch == CH_C1) return CH_C2 - 1;
  if (ch == CH_C2) return CH_D - 2;
  if (ch == CH_D - 1) return ch;
  if (ch == CH_D) return CH_D + 6;
  if (ch == CH_D + 7) return CH_D + 12;
  if (ch >= CH_D + 13 && (ch - CH_D - 13) % 2 == 0) return ch + 1;
  return -1;
}
constexpr bool released(int n) { return n + RCH <= CH_END + 1 && (n <= CH_B || (n > CH_C1 && n <= CH_D) || (n - CH_B) % 2 == 0); }
template <int I> struct IC { static constexpr int v = I; };
template <int B, int E, class Fn> DI void static_for(Fn&& fn) {
  if constexpr (B < E) { fn(IC<B>{}); static_for<B + 1, E>(fn); }
}
template <int D> DI void bump(unsigned& b) { if constexpr (D != 0) asm volatile("v_add_u32 %0, %1, %0" : "+v"(b) : "i"(D)); }
enum { W_PK = 0, W_KV = 1, W_Q = 2 };
constexpr int region(int ch) { return (slot(ch) / 6) * 61440; }
constexpr int inreg(int ch) { return (slot(ch) % 6) * 10240; }
template <int WK> constexpr int foff(int e) { return WK == W_PK ? e * 1024 : WK == W_KV ? (e / 5) * 5120 + 32 * (e % 5) : (e / 6) * 5120 + ((e / 3) & 1) * 80 + 32 * (e % 3); }

template <int TL, bool Z> DI void mm(f32x4& acc, const v4u& wf, const v4u& x) {
  if constexpr (Z && TL) asm volatile("v_mfma_f32_16x16x32_bf16 %0, %1, %2, 0" : "=&v"(acc) : "v"(wf), "v"(x));
  else if constexpr (Z) asm volatile("v_mfma_f32_16x16x32_bf16 %0, %2, %1, 0" : "=&v"(acc) : "v"(wf), "v"(x));
  else if constexpr (TL) asm volatile("v_mfma_f32_16x16x32_bf16 %0, %1, %2, %0" : "+v"(acc) : "v"(wf), "v"(x));
  else asm volatile("v_mfma_f32_16x16x32_bf16 %0, %2, %1, %0" : "+v"(acc) : "v"(wf), "v"(x));
}
template <int WK, int NT, int NKS, int CH0, int FPC, int TL, int XW, int ZM, class Hook>
DI void gemm_pass(Ctx& c, unsigned dn, unsigned wb, f32x4 (&acc)[2][NT], v4u (&x)[2][XW], Hook&& hook) {
  constexpr int LA = 3, WBN = LA + 1, NF = NKS * NT;
  v4u wf[WBN];
  unsigned la, lb, dv;
  bump<region(CH0)>(wb);
  SCHED();
  static_for<-LA, NF>([&](auto ic) AI {
    constexpr int f = decltype(ic)::v, fn = f + LA;
    constexpr int s = f < 0 ? 0 : f / NT, t = f < 0 ? 0 : f % NT;
    constexpr int ch = CH0 + fn / FPC, off = inreg(ch) + foff<WK>(fn % FPC);
    constexpr auto chk = [](int i) { return i > 0 && i < NF && i % FPC == 0 && upto(CH0 + i / FPC) >= 0; };
    constexpr bool rel = f >= 0 && (f + 1) % FPC == 0 && released(CH0 + (f + 1) / FPC);
    if constexpr (f >= 0) { mm<TL, (ZM & 1) && s == 0>(acc[0][t], wf[f % WBN], x[0][s % XW]); SCHED(); }
    v2u lo;
    if constexpr (fn < NF) {
      if constexpr (fn == 0 || chk(fn)) ring_need<upto(ch)>(c);
      if constexpr (WK == W_PK) wf[fn % WBN] = *LP(v4u, wb + off);
      else lo = tr1(wb, off);
      SCHED();
    }
    if constexpr (chk(fn + 4)) { la = *LP(unsigned, dn + 1024); lb = *LP(unsigned, dn + 1280); SCHED(); }
    if constexpr (chk(fn + 2)) { la = min(la, lb); PIN(la); SCHED(); }
    if constexpr (chk(fn + 1)) { c.landed = __builtin_amdgcn_readfirstlane(la); asm volatile("" : "+{s70}"(c.landed)); SCHED(); }
    if constexpr (rel) { asm volatile("v_mov_b32 %0, %1" : "=v"(dv) : "i"(CH0 + (f + 1) / FPC)); SCHED(); }
    if constexpr (f >= 0) { mm<TL, (ZM & 2) && s == 0>(acc[1][t], wf[f % WBN], x[1][s % XW]); SCHED(); }
    if constexpr (fn < NF && WK != W_PK) { const v2u hi = tr1(wb, off + 2560); wf[fn % WBN] = v4u{lo.x, lo.y, hi.x, hi.y}; SCHED(); }
    if constexpr (fn + 1 < NF && (fn + 1) % FPC == 0) bump<region(ch + 1) - region(ch)>(wb);
    if constexpr (rel) *LP(volatile unsigned, dn) = dv;
    hook(ic);
    SCHED();
  });
  asm volatile("s_nop 7");
  SCHED();
}
template <int XW, int S0, int S1, int N, int LEN> DI void xload(const Ctx& c, v4u (&x)[2][XW], int xvoff, int xsoff, int xmt, int rot) {
#pragma unroll
  for (int s = S1; s < S1 + N; ++s) {
    const int o = xsoff + (rot + s >= LEN ? rot + s - LEN : rot + s) * 1024;
    x[0][(S0 + s) % XW] = ld128(c, xvoff, o);
    x[1][(S0 + s) % XW] = ld128(c, xvoff, o + xmt);
  }
  SCHED();
}
DI int opaque(int v) { asm volatile("" : "+v"(v)); return v; }
DI v2u phi4(f32x4 te) {
  const f32x4 u = te * 1.4426950408889634f - 1.4426950408889634e-6f;
  float y[4];
#pragma unroll
  for (int i = 0; i < 4; ++i) y[i] = fmaxf(te[i], 1e-6f) + __builtin_amdgcn_fmed3f(__builtin_amdgcn_exp2f(u[i]), 0.f, 1.f);
  return v2u{pk2(y[0], y[1]), pk2(y[2], y[3])};
}

DI void pair_reduce(Ctx& c, int w, int cx, f32x4 (&acc)[2][5], unsigned (&y)[10]) {
  const unsigned lo = opaque(c.lane) * 16 + L_SCR;
  LAS f32x4* mine = LP(f32x4, lo + w * 5120);
  LAS f32x4* other = LP(f32x4, lo + (w ^ 1) * 5120);
#pragma unroll
  for (int t = 0; t < 5; ++t) mine[t * 64] = acc[1][t];
  ctl_add(cx + (w >> 1));
  ctl_wait(c, cx + (w >> 1), 2);
#pragma unroll
  for (int t = 0; t < 5; ++t) {
    const f32x4 o = acc[0][t] + other[t * 64];
    y[2 * t] = pk2(o[0], o[1]); y[2 * t + 1] = pk2(o[2], o[3]);
  }
}
DI int mod10(int v) { return v >= 20 ? v - 20 : v >= 10 ? v - 10 : v; }

__global__ void __launch_bounds__(512, 2)
mk_kernel(const char* __restrict__ x, const char* __restrict__ wqkv, const char* __restrict__ wo, const char* __restrict__ w1,
          const char* __restrict__ w2, char* __restrict__ out, char* ws) {
  u64 cpc; unsigned clen;
  asm volatile(
      "s_getpc_b64 %0\n"
      ".Lpc%=:\n\t"
      "s_mov_b32 %1, .Lend%=-.Lpc%=-4\n\t"
      ".subsection 1\n"
      ".Lmk_ring:\n\t"
      "v_mov_b32 v254, %2\n\t"
      "ds_read2st64_b32 v[254:255], v254 offset1:1\n\t"
      "s_sub_u32 s71, s71, 1\n\t"
      "s_cbranch_scc1 .Lmk_give\n\t"
      "s_waitcnt lgkmcnt(0)\n\t"
      "v_min_u32 v254, v254, v255\n\t"
      "s_nop 0\n\t"
      "v_readfirstlane_b32 s70, v254\n\t"
      "s_setpc_b64 s[72:73]\n"
      ".Lmk_give:\n\t"
      "s_waitcnt lgkmcnt(0)\n\t"
      "s_mov_b32 s71, 0\n\t"
      "s_movk_i32 s70, 0x7fff\n\t"
      "s_setpc_b64 s[72:73]\n\t"
      ".subsection 2\n"
      ".Lend%=:\n\t"
      ".fill 128, 4, 0xBF800000\n\t"
      ".subsection 0"
      : "=s"(cpc), "=s"(clen) : "i"(L_LAND));
  const u64 tstart = now();
  Ctx c;
  c.ws = (GAS char*)ws;
  c.rs = __builtin_amdgcn_make_buffer_rsrc((void*)ws, 0, WS_BYTES, 0x00020000);
  c.landed = 0; c.budget = 1u << 22;
  const int tid = threadIdx.x, lane = tid & 63, w = __builtin_amdgcn_readfirstlane(tid >> 6);
  c.lane = lane;
  const int b = blockIdx.x, xc = b & 7, sx = b >> 3;
  const int pi = 4 * (xc >> 1) + (sx >> 3), pj = (xc & 1) * 8 + (sx & 7);
  const int hp = 4 * xc + (sx >> 3), cc = sx & 7;
  const int qi = 2 * xc + (sx >> 4), qj = sx & 15;
  const int rot = mod10((sx & 7) + ((5 * (sx >> 3)) >> 1) + (xc >> 1));
  const int rotB = ROT * ((5 * cc) >> 2), rotC = ROT * 5 * (qj & 3), rotD1 = ROT * ((2 * cc + 5 * (sx >> 3)) & 15);
  const int rd2 = ((11 * cc) >> 3) + 3 * (sx >> 3), rotD2 = ROT * (rd2 >= 11 ? rd2 - 11 : rd2);
  const int n16 = lane & 15, g = lane >> 4;
  asm volatile("" :: "s"(x), "s"(wqkv), "s"(wo), "s"(w1), "s"(w2), "s"(out), "s"(ws));
  const rsrc_t rx = __builtin_amdgcn_make_buffer_rsrc((void*)(x - 3072), 0, 2048 * 1280 + 3072, 0x00020000);
  const int xr = lane >> 3;
  const int v0 = xr * 1280 + (((lane & 7) ^ (xr >> 1)) * 16), v1 = (8 + xr) * 1280 + (((lane & 7) ^ (4 + (xr >> 1))) * 16);
  if (w == 0) STAMP(0);
  STAMP(32 + w);
  if (__builtin_expect((w >> 1) != 2, 1)) {
    *LP(v4u, L_DONE + lane * 16) = v4u{0u, 0u, 0u, 0u};
    *LP(unsigned, L_CTL + (lane & 31) * 4) = (lane & 31) == C_CAP ? CH_B : 0u;
    LGKM0();
    __builtin_amdgcn_s_barrier();
    CFENCE();
  }
  if (__builtin_expect(w >= 4, 1)) {
    __builtin_amdgcn_s_setprio(3);
    if (__builtin_expect(w < 6, 1)) {
      const int Lw = w - 4;
      unsigned lim = RCH, bud = 1u << 20, dep = DEP_A;
      const unsigned pub = L_LAND + lane * 4 + Lw * 256, lw = Lw * 5120;
#define MK_ST "+{s70}"(lim), "+{s71}"(bud), "+{s76}"(dep)
#define MK_IN "{v249}"(pub), "{s81}"(lw)
#define MK_CL "memory", "scc", "s72", "s73", "s74", "s75", "s77", "s78", "s79", "s80", "v250", "v251", "v252", "v253", "v254", "v255"
      if (w == 4) STAMP(40);
      asm volatile(
          ".macro MK_PUB\n\t"
          "s_getpc_b64 s[72:73]\n\t"
          "s_getreg_b32 s74, hwreg(HW_REG_IB_STS)\n\t"
          "s_and_b32 s75, s74, 15\n\t"
          "s_bfe_u32 s74, s74, 0x20016\n\t"
          "s_lshl4_add_u32 s74, s74, s75\n\t"
          "s_sub_u32 s75, mk_p, s74\n\t"
          "v_mov_b32 v250, s75\n\t"
          "ds_write_b32 v249, v250\n\t"
          "s_cmp_gt_u32 s74, s76\n\t"
          "s_cbranch_scc1 .Lmk_lslow\n\t"
          ".endm\n\t"
          ".macro MK_HEAD key, sl\n\t"
          "MK_PUB\n\t"
          ".if (\\key) >= 13\n\t"
          "s_cmpk_le_u32 s70, \\key\n\t"
          "s_cbranch_scc1 .Lmk_lslow\n\t"
          ".endif\n\t"
          "s_add_u32 m0, s81, (\\sl)*10240\n\t"
          ".endm\n\t"
          ".macro MK_X i, a0, a1, a2, a3, rs, sb\n\t"
          ".if (\\i) == 2\n\ts_waitcnt lgkmcnt(0)\n\ts_barrier\n\t.endif\n\t"
          ".if (\\i) < 11\n\t"
          ".if (\\i) == 0 || (\\i) >= " STR(XPRE) "\n\tMK_PUB\n\t.endif\n\t"
          "s_add_u32 m0, s81, (\\i)*10240\n\t"
          ".else\n\t"
          "MK_HEAD ((\\i)+2), ((\\i)-11)\n\t"
          ".endif\n\t"
          "s_add_u32 s77, s80, \\sb\n\t"
          ".if (\\i) %% 2\n\ts_add_u32 s77, s77, 81920\n\t.endif\n\t"
          "s_add_u32 s78, s77, 20480\n\t"
          "buffer_load_dwordx4 \\a0, \\rs, s77 offen lds\n\t"
          "buffer_load_dwordx4 \\a1, \\rs, s77 offen offset:1024 lds\n\t"
          "buffer_load_dwordx4 \\a2, \\rs, s78 offen offset:2048 lds\n\t"
          "buffer_load_dwordx4 \\a3, \\rs, s78 offen offset:3072 lds\n\t"
          ".if (\\i) %% 2\n\t"
          "s_add_u32 s80, s80, 128\n\t"
          "s_sub_u32 s79, s80, 1280\n\t"
          "s_min_u32 s80, s80, s79\n\t"
          ".endif\n\t"
          ".set mk_p, mk_p+4\n\t"
          ".endm\n\t"
          ".macro MK_RAW key, a0, a1, a2, a3, a4, rs, step\n\t"
          "MK_HEAD \\key, ((mk_i-9) %% 13)\n\t"
          "s_add_u32 s78, s77, \\step\n\t"
          "buffer_load_dwordx4 \\a0, \\rs, s77 offen lds\n\t"
          "s_add_u32 m0, m0, 1024\n\t"
          "s_sub_u32 s79, s78, 2457600\n\t"
          "buffer_load_dwordx4 \\a1, \\rs, s77 offen lds\n\t"
          "s_add_u32 m0, m0, 1024\n\t"
          "s_min_u32 s78, s78, s79\n\t"
          "buffer_load_dwordx4 \\a2, \\rs, s77 offen lds\n\t"
          "s_add_u32 m0, m0, 1024\n\t"
          "s_nop 0\n\t"
          "buffer_load_dwordx4 \\a3, \\rs, s77 offen lds\n\t"
          "s_add_u32 m0, m0, 1024\n\t"
          "s_nop 0\n\t"
          "buffer_load_dwordx4 \\a4, \\rs, s77 offen lds\n\t"
          "s_mov_b32 s77, s78\n\t"
          ".set mk_p, mk_p+5\n\t"
          ".set mk_i, mk_i+1\n\t"
          ".endm\n\t"
          ".macro MK_PK l0, l1, rs, base, len\n\t"
          "MK_HEAD mk_i, ((mk_i-9) %% 13)\n\t"
          "s_add_u32 s77, s80, \\base\n\t"
          "buffer_load_dwordx4 \\l0, \\rs, s77 offen sc1 lds\n\t"
          "s_add_u32 s80, s80, 10240\n\t"
          "buffer_load_dwordx4 \\l0, \\rs, s77 offen offset:1024 sc1 lds\n\t"
          "s_sub_u32 s79, s80, \\len\n\t"
          "buffer_load_dwordx4 \\l0, \\rs, s77 offen offset:2048 sc1 lds\n\t"
          "buffer_load_dwordx4 \\l0, \\rs, s77 offen offset:3072 sc1 lds\n\t"
          "s_add_u32 m0, m0, 4096\n\t"
          "s_min_u32 s80, s80, s79\n\t"
          "buffer_load_dwordx4 \\l1, \\rs, s77 offen sc1 lds\n\t"
          ".set mk_p, mk_p+5\n\t"
          ".set mk_i, mk_i+1\n\t"
          ".endm\n\t"
          ".subsection 1\n"
          ".Lmk_lslow:\n\t"
          "s_sleep 1\n\t"
          "v_mov_b32 v251, %[dn]\n\t"
          "ds_read_b128 v[252:255], v251\n\t"
          "ds_read_b32 v251, v251 offset:%[cap]\n\t"
          "s_sub_u32 s71, s71, 1\n\t"
          "s_cbranch_scc1 .Lmk_lgive\n\t"
          "s_waitcnt lgkmcnt(0)\n\t"
          "v_min3_u32 v252, v252, v253, v254\n\t"
          "v_min_u32 v252, v252, v255\n\t"
          "v_add_u32 v252, 13, v252\n\t"
          "v_min_u32 v252, v252, v251\n\t"
          "s_nop 0\n\t"
          "v_readfirstlane_b32 s70, v252\n\t"
          "s_setpc_b64 s[72:73]\n"
          ".Lmk_lgive:\n\t"
          "s_endpgm\n\t"
          ".subsection 0\n\t"
          ".set mk_p, 0\n\t"
          ".set mk_x, 0\n\t"
          "s_mov_b32 s80, %[sr]\n\t"
          ".rept 11\n\tMK_X mk_x, %[x0], %[x1], %[x2], %[x3], %[rx], %[sx]\n\t.set mk_x, mk_x+1\n\t.endr"
          : MK_ST
          : MK_IN, [x0] "v"(v0 + 3072), [x1] "v"(v1 + 2048), [x2] "v"(v0 + 1024), [x3] "v"(v1), [rx] "s"(rx), [sr] "s"(rot * 128),
            [sx] "s"((128 * pi + 32 * Lw) * 1280), [dn] "i"(L_DONE), [cap] "i"(L_CTL + 4 * C_CAP - L_DONE)
          : MK_CL);
      if (w == 4) STAMP(41);
      c.budget = bud;
      ctl_wait(c, C_ADR, 1);
      bud = c.budget;
      const v4u a0 = *LP(v4u, L_RPK + lane * 16), a1 = *LP(v4u, L_RPK + 1024 + lane * 16);
      const v2u a2 = *LP(v2u, L_RPK + 2048 + lane * 8);
      const rsrc_t rw = __builtin_amdgcn_make_buffer_rsrc((void*)wqkv, 0, 640 * 3840, 0x00020000);
      const rsrc_t rk = __builtin_amdgcn_make_buffer_rsrc((void*)(wqkv + Lw * 122880), 0, 640 * 3840 - Lw * 122880, 0x00020000);
      if (w == 4) STAMP(42);
      asm volatile(
          ".set mk_i, 20\n\t"
          "s_mov_b32 s77, %[sk]\n\t"
          ".rept 2\n\tMK_RAW 0, %[k0], %[k1], %[k2], %[k3], %[k4], %[rk], 245760\n\t.endr\n\t"
          "s_mov_b32 s82, s77\n\t"
          "s_mov_b32 s80, %[sr]\n\t"
          ".rept 9\n\tMK_X mk_x, %[x0], %[x1], %[x2], %[x3], %[rx], %[sx]\n\t.set mk_x, mk_x+1\n\t.endr\n\t"
          "s_mov_b32 s77, s82\n\t"
          ".rept 8\n\tMK_RAW (mk_i+2), %[k0], %[k1], %[k2], %[k3], %[k4], %[rk], 245760\n\t.endr\n\t"
          "s_mov_b32 s77, %[sq]\n\t"
          "MK_RAW (mk_i+2), %[q0], %[q1], %[q2], %[q3], %[q4], %[rw], 491520\n\t"
          ".rept 4\n\tMK_RAW mk_i, %[q0], %[q1], %[q2], %[q3], %[q4], %[rw], 491520\n\t.endr"
          : MK_ST
          : MK_IN, [x0] "v"(v0 + 3072), [x1] "v"(v1 + 2048), [x2] "v"(v0 + 1024), [x3] "v"(v1), [rx] "s"(rx), [sr] "s"(mod10(rot + 5) * 128),
            [sx] "s"((128 * pi + 32 * Lw) * 1280), [k0] "v"(a0.x), [k1] "v"(a0.y), [k2] "v"(a0.z), [k3] "v"(a0.w), [k4] "v"(a1.x),
            [q0] "v"(a1.y), [q1] "v"(a1.z), [q2] "v"(a1.w), [q3] "v"(a2.x), [q4] "v"(a2.y), [rw] "s"(rw), [rk] "s"(rk),
            [sk] "s"(rot * 245760), [sq] "s"(mod10(rot + Lw) * 245760)
          : MK_CL, "s82");
      dep = DEP_P;
      asm volatile(
          "s_mov_b32 s80, %[rb]\n\t"
          ".rept 10\n\tMK_PK %[l0], %[l1], %[rs], %[sb], 102400\n\t.endr\n\t"
          "s_mov_b32 s80, %[rc1]\n\t"
          ".rept 8\n\tMK_PK %[l0], %[l1], %[rs], %[sc1], 81920\n\t.endr\n\t"
          "s_mov_b32 s80, %[rc2]\n\t"
          ".rept 6\n\tMK_PK %[l0], %[l1], %[rs], %[sc2], 61440\n\t.endr\n\t"
          "s_mov_b32 s80, %[rd1]\n\t"
          ".rept 16\n\tMK_PK %[l0], %[l1], %[rs], %[sd1], 163840\n\t.endr\n\t"
          "s_mov_b32 s80, %[rd2]\n\t"
          ".rept 11\n\tMK_PK %[l0], %[l1], %[rs], %[sd2], 112640\n\t.endr\n\t"
          "s_mov_b32 s76, 0\n\t"
          "MK_PUB\n\t"
          ".purgem MK_PUB\n\t.purgem MK_HEAD\n\t.purgem MK_RAW\n\t.purgem MK_PK\n\t.purgem MK_X\n\t"
          "s_waitcnt vmcnt(0) lgkmcnt(0)"
          : MK_ST
          : MK_IN, [l0] "v"(lane * 16), [l1] "v"(lane * 16 + 4096), [rs] "s"(c.rs),
            [sb] "s"(OFF_WB + cc * 102400), [rb] "s"(rotB * 10240 + lw),
            [sc1] "s"(OFF_WC + qj * 143360), [rc1] "s"(rotC * 4096 + lw),
            [sc2] "s"(OFF_WC + qj * 143360 + 81920), [rc2] "s"(rotC * 3072 + lw),
            [sd1] "s"(OFF_WD + cc * 276480), [rd1] "s"(rotD1 * 10240 + lw),
            [sd2] "s"(OFF_WD + cc * 276480 + 163840), [rd2] "s"(rotD2 * 10240 + lw)
          : MK_CL);
      return;
    }
    const int fq = (pj & 7) >> 1;
    const bool fast = ((0xa5 >> (pj & 7)) & 1) != 0;
    const int unit = fast ? (w - 6) * 128 + pi * 8 + (pj >> 3) * 4 + fq : 1000;
    const auto do_unit = [&](unsigned bb, unsigned bs) AI {
      if (unit < 40) repack<1>(c, wo, unit / 10, unit % 10, bb, bs);
      else if (unit < 148) repack<2>(c, w1, (unit - 40) / 27, (unit - 40) % 27, bb, bs);
      else if (unit < 238) repack<3>(c, w2, (unit - 148) / 10, (unit - 148) % 10, bb, bs);
    };
    if (w == 7) {
      unsigned ad[10];
#pragma unroll
      for (int i = 0; i < 5; ++i) {
        const int ci = 64 * i + lane, rho = ci / 10, ch = ci - 10 * rho;
        const int k = 8 * ((rho >> 2) & 3) + 4 * (rho >> 4) + (rho & 3);
        ad[i] = k * 3840 + pj * 80 + ch * 16 + (ch < 5 ? 1280 : 2480);
        ad[5 + i] = k * 3840 + pj * 80 + ch * 16 + (ch < 5 ? 0 : 32 * 3840 - 80);
      }
      *LP(v4u, L_RPK + lane * 16) = v4u{ad[0], ad[1], ad[2], ad[3]};
      *LP(v4u, L_RPK + 1024 + lane * 16) = v4u{ad[4], ad[5], ad[6], ad[7]};
      *LP(v2u, L_RPK + 2048 + lane * 8) = v2u{ad[8], ad[9]};
      ctl_addc<C_ADR>();
    }
    const u64 e0 = __hip_atomic_load((GAS u64*)(c.ws + OFF_EPOCH), RLX_AGENT);
    {
      const unsigned lo = __builtin_amdgcn_readfirstlane((unsigned)e0), hi = __builtin_amdgcn_readfirstlane((unsigned)(e0 >> 32));
      c.ep = (((u64)hi << 32) | lo) + EPOCH_STEP;
    }
    do_unit(w == 6 ? L_RPK : L_SCR + 3072, w == 6 ? 1024 : 5120);
    if (unit < 238) { DRAIN(); flag_set(c, FLR, unit); }
    if (w == 6) {
      STAMP(22);
      {
        unsigned n_ = 0;
        while (min(__builtin_amdgcn_readfirstlane(*LP(volatile unsigned, L_LAND)), __builtin_amdgcn_readfirstlane(*LP(volatile unsigned, L_LAND + 256))) < needp(CH_KV) && ++n_ < (1u << 16)) __builtin_amdgcn_s_sleep(4);
        CFENCE();
      }
#pragma unroll
      for (int i = 0; i < 4; ++i) *LP(v4u, L_RPK + i * 1024 + lane * 16) = v4u{0u, 0u, 0u, 0u};
      ctl_addc<C_GO1>();
      const int g0 = gam0(qj);
      int idx = (lane & 3) * 10 + ((80 * cc + ((lane >> 2) & 1) * 79) >> 6);
      if (lane >= 8 && lane < 20) { const int cb = (g0 >> 2) + ((lane - 8) >> 2); if (cb < 27 && 4 * cb <= g0 + 6) idx = 40 + (lane & 3) * 27 + cb; }
      else if (lane >= 20 && lane < 38) { const int l = lane - 20; idx = 148 + (l >> 1) * 10 + ((80 * cc + (l & 1) * 79) >> 6); }
      ctl_wait<1>(c, C_RED, 1);
      flag_wait(c, FLR, idx, 8);
      if (lane == 0) *LP(volatile unsigned, L_CTL + 4 * C_CAP) = CH_C1;
      STAMP(20);
      flag_wait(c, FLR, idx, 38);
      if (lane == 0) *LP(volatile unsigned, L_CTL + 4 * C_CAP) = 1000u;
      if (lane < 28) {
        f32x4 s = {0.f, 0.f, 0.f, 0.f};
#pragma unroll
        for (int rb = 0; rb < 4; ++rb) s += __builtin_bit_cast(f32x4, ld128(c, lane * 16, OFF_CS + (rb * 1792 + 16 * g0) * 4));
        *LP(f32x4, L_CSUM + lane * 16) = s;
      }
      ctl_addc<C_CS>();
      DUMP(20, 21); DUMP(22, 23);
      return;
    }
    STAMP(21);
    unsigned ra[4];
#pragma unroll
    for (int i = 0; i < 4; ++i) {
      const int it = min(i * 64 + lane, 209);
      const int z = it >= 200, he = z ? (it - 200) / 5 : it / 5, d4 = it - 5 * (it / 5);
      const int hd = z ? he : he / 20, d4g = 5 * hd + d4, e8 = z ? 0 : he + 8;
      ra[i] = KVSLOT * 10240 + ((d4g >> 2) * 3 + (e8 >> 4)) * 1024 + ((d4g & 3) * 16 + (e8 & 15)) * 16;
      PIN(ra[i]);
    }
    int kvo = OFF_KVP + (pj * 16 + pi) * KVP_STR + lane * 16;
    PIN(kvo);
    ctl_wait<1>(c, C_ARR1, 4);
    {
      f32x4 p[4][4];
#pragma unroll
      for (int i = 0; i < 4; ++i)
#pragma unroll
        for (int j = 0; j < 4; ++j) p[i][j] = *LP(f32x4, ra[i] + j * 10240);
      ctl_addc<C_RED>();
#pragma unroll
      for (int i = 0; i < 4; ++i) {
        const f32x4 v = (p[i][0] + p[i][1]) + (p[i][2] + p[i][3]);
        if (i < 3 || lane < 24) st128(c, __builtin_bit_cast(v4u, v), kvo, i * 1024);
      }
    }
    DRAIN();
    flag_set(c, FL1, pj * 16 + pi);
    STAMP(23);
    flag_wait(c, FL1, pj * 16 + n16, 16);
    ctl_addc<C_GO1>();
    const int mate = qi * 16 + n16;
    ctl_wait<1>(c, C_ARR2, 4);
    flag_set(c, FL2, pi * 16 + pj);
    STAMP(24);
    flag_wait(c, FL2, mate, 16);
    ctl_addc<C_GO2>();
    if (b == 0 && lane == 0) __hip_atomic_store((GAS u64*)(c.ws + OFF_EPOCH), c.ep, RLX_AGENT);
    ctl_wait<1>(c, C_ARR3, 4);
    flag_set(c, FL3, qi * 16 + qj);
    STAMP(25);
    flag_wait(c, FL3, mate, 16);
    ctl_addc<C_GO3>();
    ctl_wait<1>(c, C_ARR4A, 4);
    flag_set(c, FL4A, qi * 16 + qj);
    STAMP(26);
    flag_wait(c, FL4A, mate, 16);
    ctl_addc<C_GO4A>();
    STAMP(27);
    ctl_wait<1>(c, C_ARR4B, 4);
    flag_set(c, FL4B, qi * 16 + qj);
    STAMP(28);
    flag_wait(c, FL4B, mate, 16);
    ctl_addc<C_GO4B>();
    STAMP(29);
    DUMP(23, 30); DUMP(21, 22);
    return;
  }

  const int xvoff = lane * 16;
  const unsigned dn = L_DONE + lane * 16 + w * 4;
  const auto nohook = [](auto) AI {};
  const unsigned scr = L_SCR + w * 5120;

  {
    v4u xa[2][20];
    unsigned pf = 0u;
    {
      const unsigned oc = (sx * 16 + w * 4 + lane) * 128;
      if (lane < 4 && oc < clen) pf = *(const GAS unsigned*)(cpc + oc);
    }
    {
      const int t2 = tid < 240 ? tid : tid - 40;
      const int z = t2 >= 200, zi = t2 - 200, zg = zi / 10;
      const int it = z ? 200 + zi - 10 * zg : t2;
      const int he = z ? (it - 200) / 5 : it / 5, d4 = it - 5 * (it / 5);
      const int hd = z ? he : he / 20, e = he - 20 * hd, gi = e / 5, r5 = e - 5 * gi;
      const int R = z || r5 == 4, row = z ? 4 * zg + 1 : r5 == 4 ? 4 * gi : 4 * gi + r5;
      const int dd = d4 + hd;
      *LP(v2u, scr + 2048 + lane * 8) = v2u{(unsigned)it * 16, (unsigned)(L_RPK + (hd * 2 + R) * 1024 + ((dd & 3) * 16 + row) * 16 + (dd >> 2) * 8)};
      CFENCE();
    }
    if (w == 0) STAMP(30);
    v4u ones = {ONE2, ONE2, ONE2, ONE2};
    PIN(ones);
    {
      const unsigned bw = scr + lane * 16;
      unsigned br = (w >> 1) * 10240 + (w & 1) * 5120 + (n16 >> 3) * 1024 + (n16 & 7) * 128 + ((g ^ (n16 >> 1)) * 16);
      f32x4 sm[2], sq[2];
#pragma unroll
      for (int m = 0; m < 2; ++m) { sm[m] = f32x4{0.f, 0.f, 0.f, 0.f}; sq[m] = sm[m]; }
      static_for<0, 11>([&](auto ic) AI {
        constexpr int i = decltype(ic)::v;
        if constexpr (i < 10) {
          ring_need<2 * i + 1>(c);
          if constexpr (i == 3) bump<61440>(br);
          if constexpr (i == 5) br += w >> 1 ? -71680 : 40960;
          if constexpr (i == 6) br += w >> 1 ? 20480 : -92160;
          if constexpr (i == 9) bump<61440>(br);
          constexpr int off = i < 3 ? i * 20480 : i < 5 ? (i - 3) * 20480 : i > 5 && i < 9 ? (i - 6) * 20480 : 0;
#pragma unroll
          for (int m = 0; m < 2; ++m) { xa[m][2 * i] = *LP(v4u, br + off + m * 2048); xa[m][2 * i + 1] = *LP(v4u, (br ^ 64) + off + m * 2048); }
          CFENCE();
          *LP(volatile unsigned, dn) = 2 * i + 2;
        }
        SCHED();
        if constexpr (i > 0) {
#pragma unroll
          for (int e = 0; e < 2; ++e)
#pragma unroll
            for (int m = 0; m < 2; ++m) {
              sm[m] = mfma(ones, xa[m][2 * i - 2 + e], sm[m]);
              sq[m] = mfma(xa[m][2 * i - 2 + e], xa[m][2 * i - 2 + e], sq[m]);
            }
        }
        SCHED();
      });
      *LP(f32x4, bw) = sq[0];
      *LP(f32x4, bw + 1024) = sq[1];
      CFENCE();
      const float q2 = *LP(float, scr + (g >> 1) * 1024 + ((n16 >> 2) * 16 + n16) * 16 + (n16 & 3) * 4);
      const float mean = (g >> 1 ? sm[1][0] : sm[0][0]) * (1.f / 640.f);
      const float a = rsqrtf(fmaxf(q2 * (1.f / 640.f) - mean * mean, 0.f) + 1e-5f);
      *LP(float, scr + 4864 + lane * 4) = (g & 1) ? -mean * a : a;
      CFENCE();
    }
    asm volatile("" :: "v"(pf));
    PADTO(1, 20);
    if (w == 0) STAMP(31);
    const unsigned rawlane = (4 * g + ((lane >> 2) & 3)) * 160 + 8 * (lane & 3);
    {
      f32x4 acc[2][5];
      {
        f32x4 ca = {0.f, 0.f, 0.f, 0.f}, c4 = ca;
        v4u cf;
        unsigned bc = rawlane + 32 * w + region(CH_KV), b4 = rawlane + 5120 * w + 128 + region(CH_KV);
        gemm_pass<W_KV, 5, 20, CH_KV, 10, 0, 20, 3>(c, dn, rawlane, acc, xa, [&](auto ic) AI {
          constexpr int f = decltype(ic)::v, ch = CH_KV + (f + 3) / 10, k = (f + 3) % 10;
          if constexpr (ch < CH_Q) {
            if constexpr (k == 0 && (ch & 1)) {
              static_assert(slot(ch) == slot(ch - 1) + 1, "chunk pair must be contiguous");
              if constexpr (ch > CH_KV + 1) bump<region(ch - 1) - region(ch - 3)>(b4);
              cf = tr2(b4, inreg(ch - 1), 2560);
            }
            if constexpr (k == 3) {
              if constexpr (ch & 1) c4 = mfma(ones, cf, c4);
              if constexpr (ch > CH_KV) bump<region(ch) - region(ch - 1)>(bc);
              cf = tr2(bc, inreg(ch), 2560);
            }
            if constexpr (k == 6) { ca = mfma(ones, cf, ca); cf = tr2(bc, inreg(ch) + 5120, 2560); }
            if constexpr (k == 9) ca = mfma(ones, cf, ca);
          }
          if constexpr (f == 98) {
            const unsigned a = scr + 4096 + opaque(lane) * 4;
            *LP(float, a) = ca[0]; *LP(float, a + 256) = c4[0];
            ctl_addc<C_CSK>();
          }
        });
      }
      if (w == 0) STAMP(4);
      f32x4 a4[2], b4[2];
#pragma unroll
      for (int m = 0; m < 2; ++m) { a4[m] = *LP(f32x4, scr + 4864 + m * 128 + g * 16); b4[m] = *LP(f32x4, scr + 4864 + m * 128 + 64 + g * 16); }
      ctl_wait(c, C_CSK, 4);
      float cs[5];
      {
        const unsigned a = L_SCR + 4096 + n16 * 4;
#pragma unroll
        for (int t = 0; t < 4; ++t) cs[t] = *LP(float, a + t * 5120);
        cs[4] = (*LP(float, a + 256) + *LP(float, a + 5376)) + (*LP(float, a + 10496) + *LP(float, a + 15616));
      }
      v4u pk[3], vv[3];
#pragma unroll
      for (int t = 0; t < 3; ++t) {
        const f32x4 t0 = acc[0][t] * a4[0] + (b4[0] * cs[t] + 1e-6f), t1 = acc[1][t] * a4[1] + (b4[1] * cs[t] + 1e-6f);
        const v2u p0 = phi4(t0), p1 = phi4(t1);
        pk[t] = v4u{p0.x, p0.y, p1.x, p1.y};
      }
#pragma unroll
      for (int t = 2; t < 5; ++t) {
        const f32x4 t0 = acc[0][t] * a4[0] + b4[0] * cs[t], t1 = acc[1][t] * a4[1] + b4[1] * cs[t];
        vv[t - 2] = v4u{pk2(t0[0], t0[1]), pk2(t0[2], t0[3]), pk2(t1[0], t1[1]), pk2(t1[2], t1[3])};
      }
      if (n16 < 8) vv[0] = ones;
      const unsigned kp = (KVSLOT + w) * 10240 + opaque(lane) * 16;
      const f32x4 z4 = {0.f, 0.f, 0.f, 0.f};
#pragma unroll
      for (int ta = 0; ta < 3; ++ta)
#pragma unroll
        for (int tb = 0; tb < 3; ++tb)
          if (ta != 0 || tb != 2) *LP(f32x4, kp + (ta * 3 + tb) * 1024) = mfma(pk[ta], vv[tb], z4);
      ctl_addc<C_ARR1>();
    }
    if (w == 0) STAMP(3);
    v2u pq[2][3];
    {
      f32x4 aq[2][3];
      f32x4 ca = {0.f, 0.f, 0.f, 0.f};
      v4u cf;
      unsigned bc = rawlane + 32 * min(w, 2) + region(CH_Q);
      gemm_pass<W_Q, 3, 20, CH_Q, 12, 1, 20, 3>(c, dn, rawlane, aq, xa, [&](auto ic) AI {
        constexpr int f = decltype(ic)::v, ch = CH_Q + (f + 3) / 12, k = (f + 3) % 12;
        if constexpr (k % 3 == 0 && f <= 57) {
          if constexpr (f > -3) ca = mfma(ones, cf, ca);
          if constexpr (k == 0 && f > -3 && f < 57) bump<region(ch) - region(ch - 1)>(bc);
          if constexpr (f < 57) cf = tr2(bc, inreg(ch) + foff<W_Q>(k), 2560);
        }
        if constexpr (f == 59) { *LP(float, scr + 4096 + 512 + opaque(lane) * 4) = ca[0]; ctl_addc<C_CSQ>(); }
      });
      float sa[2], sb[2];
#pragma unroll
      for (int m = 0; m < 2; ++m) { sa[m] = *LP(float, scr + 4864 + m * 128 + n16 * 4); sb[m] = *LP(float, scr + 4864 + m * 128 + 64 + n16 * 4); }
      ctl_wait(c, C_CSQ, 4);
#pragma unroll
      for (int t = 0; t < 3; ++t) {
        const f32x4 cs = *LP(f32x4, L_SCR + t * 5120 + 4096 + 512 + g * 16);
#pragma unroll
        for (int m = 0; m < 2; ++m) {
          pq[m][t] = phi4(aq[m][t] * sa[m] + (cs * sb[m] + 1e-6f));
          if (t == 2 && g >= 2) pq[m][t] = v2u{0u, 0u};
        }
      }
    }
    v4u qb[2][2];
#pragma unroll
    for (int m = 0; m < 2; ++m)
#pragma unroll
      for (int hh = 0; hh < 2; ++hh) { qb[m][hh] = v4u{pq[m][hh].x, pq[m][hh].y, pq[m][hh + 1].x, pq[m][hh + 1].y}; PIN(qb[m][hh]); }
    v2u ga = *LP(v2u, scr + 2048 + opaque(lane) * 8);
    PIN(ga);
    const int gsrc = ga.x;
    const unsigned gdst = ga.y;
    const int mt0 = 8 * pi + 2 * w;
    int oa = OFF_O + (mt0 * 20 + pj) * 1024 + xvoff;
    int ob = OFF_O + (mt0 * 20 + 16 + (pj >> 2)) * 1024 + (pj & 3) * 256 + g * 20480 + n16 * 16;
    unsigned fa = L_RPK + xvoff;
    PIN(oa); PIN(ob); PIN(fa);
    PADTO(5, 25);
    if (w == 0) STAMP(5);
    ctl_wait(c, C_GO1, 2);
    if (w == 0) STAMP(6);
    {
      v4u p[16];
#pragma unroll
      for (int i = 0; i < 16; ++i) p[i] = ld128(c, gsrc, OFF_KVP + (pj * 16 + i) * KVP_STR);
      f32x4 s = __builtin_bit_cast(f32x4, p[0]);
#pragma unroll
      for (int i = 1; i < 16; ++i) s += __builtin_bit_cast(f32x4, p[i]);
      *LP(v2u, gdst) = v2u{pk2(s[0], s[1]), pk2(s[2], s[3])};
    }
    ctl_addc<C_BAR1>();
    ctl_wait(c, C_BAR1, 4);
    if (w == 0) STAMP(7);
    v4u ka_[4];
#pragma unroll
    for (int i = 0; i < 4; ++i) ka_[i] = *LP(v4u, fa + (i ^ 1) * 1024);
    f32x4 on[2][2][2];
    const f32x4 z4 = {0.f, 0.f, 0.f, 0.f};
#pragma unroll
    for (int i = 0; i < 4; ++i)
#pragma unroll
      for (int m = 0; m < 2; ++m) on[m][i >> 1][(i & 1) ^ 1] = mfma(ka_[i], qb[m][i >> 1], z4);
    unsigned tl[2];
#pragma unroll
    for (int m = 0; m < 2; ++m) {
      const float r0 = __builtin_amdgcn_rcpf(on[m][0][1][1] + 1e-6f), r1 = __builtin_amdgcn_rcpf(on[m][1][1][1] + 1e-6f);
      const v4u o = {pk2(on[m][0][0][0] * r0, on[m][0][0][1] * r0), pk2(on[m][0][0][2] * r0, on[m][0][0][3] * r0),
                     pk2(on[m][0][1][0] * r0, on[m][1][0][0] * r1), pk2(on[m][1][0][1] * r1, on[m][1][0][2] * r1)};
      tl[m] = pk2(on[m][1][0][3] * r1, on[m][1][1][0] * r1);
      st128(c, o, oa, m * 20480);
    }
    const v2u t0 = swap16(tl[0], tl[1]);
    const v2u u0 = swap32(t0.x, t0.x), u1 = swap32(t0.y, t0.y);
    if (lane < 32) st128(c, v4u{u0.x, u1.x, u0.y, u1.y}, ob, 0);
  }
  if (w == 0) STAMP(8);
  DRAIN();
  ctl_addc<C_ARR2>();
  const int kb = w & 1;
  const int mtB = 4 * hp + w;
  unsigned hq[10];

  {
    f32x4 acc[2][5];
    {
      const char* xb0 = x + (size_t)(16 * mtB * 1280 + cc * 160);
      const unsigned rowoff = n16 * 1280 + g * 16;
      const v4u xr0 = *(const GAS v4u*)(xb0 + rowoff), xr1 = *(const GAS v4u*)(xb0 + rowoff + 64);
      const v2u xr2 = *(const GAS v2u*)(xb0 + (rowoff - g * 8 + 128));
      acc[0][0] = unpk4(xr0.x, xr0.y); acc[0][1] = unpk4(xr0.z, xr0.w);
      acc[0][2] = unpk4(xr1.x, xr1.y); acc[0][3] = unpk4(xr1.z, xr1.w);
      acc[0][4] = unpk4(xr2.x, xr2.y);
#pragma unroll
      for (int t = 0; t < 5; ++t) PIN(acc[0][t]);
    }
    const int sb = 5 * (cc >> 1) + 3 * (cc & 1);
    int ha = OFF_H + (mtB * 20 + sb) * 1024 + xvoff;
    int hb = OFF_H + (mtB * 20 + 5 * (cc >> 1) + 2) * 1024 + (cc & 1) * 512 + g * 128 + n16 * 16;
    PIN(ha); PIN(hb);
    ctl_wait(c, C_GO2, 1);
    PADTO(2, 30);
    if (w == 0) STAMP(9);
    v4u xb[2][10];
    const int xs = OFF_O + mtB * 20480 + kb * 10240, xm = kb ? -20480 : 20480;
    xload<10, 0, 0, PRE_B, 10>(c, xb, xvoff, xs, xm, rotB);
    gemm_pass<W_PK, 5, 10, CH_B, 5, 1, 10, 2>(c, dn, xvoff + kb * 5120, acc, xb, [&](auto ic) AI {
      constexpr int f = decltype(ic)::v;
      if constexpr (f >= 0 && f % 5 == 1 && f / 5 + PRE_B < 10) xload<10, 0, f / 5 + PRE_B, 1, 10>(c, xb, xvoff, xs, xm, rotB);
    });
    if (w == 0) STAMP(1);
    pair_reduce(c, w, C_XB0, acc, hq);
    st128(c, v4u{hq[0], hq[1], hq[2], hq[3]}, ha, 0);
    st128(c, v4u{hq[4], hq[5], hq[6], hq[7]}, ha, 1024);
    const v2u ta = swap16(hq[8], hq[8]), tb = swap16(hq[9], hq[9]);
    if ((g & 1) == 0) st128(c, v4u{ta.x, tb.x, ta.y, tb.y}, hb, 0);
    float s1 = 0.f, s2 = 0.f;
#pragma unroll
    for (int i = 0; i < 10; ++i) { s1 = dot2(hq[i], ONE2, s1); s2 = dot2(hq[i], hq[i], s2); }
    s1 = sum_g(s1); s2 = sum_g(s2);
    if (g == 0) st64(c, v2u{asu(s1), asu(s2)}, n16 * 8, OFF_HS + mtB * 1024 + cc * 128);
  }
  if (w == 0) STAMP(10);
  DRAIN();
  ctl_addc<C_ARR3>();

  {
    const int mt0 = 8 * qi + 2 * w;
    ctl_wait(c, C_CS, 1);
    f32x4 acc[2][4];
#pragma unroll
    for (int t = 0; t < 4; ++t) { acc[0][t] = *LP(f32x4, L_CSUM + t * 64 + g * 16); PIN(acc[0][t]); }
    ctl_wait(c, C_GO3, 1);
    PADTO(3, 40);
    if (w == 0) STAMP(11);
    v2u st[2][2];
#pragma unroll
    for (int m = 0; m < 2; ++m)
#pragma unroll
      for (int i = 0; i < 2; ++i) st[m][i] = ld64(c, lane * 8, OFF_HS + (mt0 + m) * 1024 + i * 512);
    v4u xh[2][20];
    xload<20, 0, 0, PRE_C, 20>(c, xh, xvoff, OFF_H + mt0 * 20480, 20480, rotC);
    float mneg[2], s2[2];
#pragma unroll
    for (int m = 0; m < 2; ++m) {
      mneg[m] = sum_g(asf(st[m][0].x) + asf(st[m][1].x)) * (-1.f / 640.f);
      s2[m] = asf(st[m][0].y) + asf(st[m][1].y);
    }
#pragma unroll
    for (int t = 0; t < 4; ++t) { acc[1][t] = acc[0][t] * mneg[1]; acc[0][t] = acc[0][t] * mneg[0]; PIN(acc[0][t]); PIN(acc[1][t]); }
    float sd[2], c2[2];
    gemm_pass<W_PK, 4, 20, CH_C1, 10, 1, 20, 0>(c, dn, xvoff, acc, xh, [&](auto ic) AI {
      constexpr int f = decltype(ic)::v;
      if constexpr (f >= 0 && f % 4 == 1 && f / 4 + PRE_C < 20) xload<20, 0, f / 4 + PRE_C, 1, 20>(c, xh, xvoff, OFF_H + mt0 * 20480, 20480, rotC);
      if constexpr (f == 2 || f == 6) {
        constexpr int m = f == 6;
        const float var = fmaxf(sum_g(s2[m]) * (1.f / 640.f) - mneg[m] * mneg[m], 0.f) + 1e-5f;
        const float rs = __builtin_amdgcn_rsqf(var);
        sd[m] = var * rs;
        c2[m] = -1.4426950408889634f * rs;
        PIN(sd[m]); PIN(c2[m]);
      }
    });
    if (w == 0) STAMP(12);
    const auto silu2 = [&](int m, const f32x4& a) AI {
      float y[4];
#pragma unroll
      for (int i = 0; i < 4; ++i) {
        const float e = __builtin_amdgcn_exp2f(a[i] * c2[m]);
        y[i] = a[i] * __builtin_amdgcn_rcpf(fmaf(e, sd[m], sd[m]));
      }
      return v2u{pk2(y[0], y[1]), pk2(y[2], y[3])};
    };
    const int fb = OFF_F + mt0 * 55296;
    f32x4 ac2[2][3];
#pragma unroll
    for (int t = 0; t < 3; ++t) ac2[0][t] = *LP(f32x4, L_CSUM + (4 + t) * 64 + g * 16);
#pragma unroll
    for (int m = 0; m < 2; ++m)
#pragma unroll
      for (int v = 0; v < 2; ++v) {
        const v2u a = silu2(m, acc[m][2 * v]), d = silu2(m, acc[m][2 * v + 1]);
        st128(c, v4u{a.x, a.y, d.x, d.y}, xvoff, fb + m * 55296 + (2 * qj + v) * 1024);
      }
#pragma unroll
    for (int t = 0; t < 3; ++t) { ac2[1][t] = ac2[0][t] * mneg[1]; ac2[0][t] = ac2[0][t] * mneg[0]; PIN(ac2[0][t]); PIN(ac2[1][t]); }
    if (w == 0) STAMP(2);
    gemm_pass<W_PK, 3, 20, CH_C2, 10, 1, 20, 0>(c, dn, xvoff, ac2, xh, [&](auto ic) AI {
      if constexpr (decltype(ic)::v == 18) { DRAIN(); ctl_addc<C_ARR4A>(); }
    });
    if (w == 0) STAMP(13);
#pragma unroll
    for (int m = 0; m < 2; ++m) {
      const v2u a = silu2(m, ac2[m][0]), d = silu2(m, ac2[m][1]);
      st128(c, v4u{a.x, a.y, d.x, d.y}, xvoff, fb + m * 55296 + (32 + qj) * 1024);
    }
    if ((qj & 3) != 3) {
      const v2u a = silu2(0, ac2[0][2]), d = silu2(1, ac2[1][2]);
      const v2u ta = swap16(a.x, d.x), tb = swap16(a.y, d.y);
      const int l = 3 * (qj >> 2) + (qj & 3);
      st128(c, v4u{ta.x, tb.x, ta.y, tb.y}, (g & 1) * 55296 + (g >> 1) * 256 + n16 * 16, fb + (48 + (l >> 1)) * 1024 + (l & 1) * 512);
    }
  }
  PADTO(4, 45);
  if (w == 0) STAMP(14);

  {
    const int xs = OFF_F + mtB * 55296, xm = kb ? -55296 : 55296;
    const bool early = ctl_read(C_GO4A) != 0u;
    CFENCE();
    if (!early) { DRAIN(); ctl_addc<C_ARR4B>(); ctl_wait(c, C_GO4A, 1); }
    v4u xf[2][20];
    xload<20, 0, 0, PRE_D, 16>(c, xf, xvoff, xs + kb * 16384, xm, rotD1);
    f32x4 acc[2][5];
#pragma unroll
    for (int t = 0; t < 5; ++t) { acc[0][t] = unpk4(hq[2 * t], hq[2 * t + 1]); PIN(acc[0][t]); }
    if (early) { asm volatile("s_waitcnt vmcnt(%0)" :: "i"(2 * PRE_D) : "memory"); ctl_addc<C_ARR4B>(); }
    if (w == 0) STAMP(16);
    gemm_pass<W_PK, 5, 27, CH_D, 5, 1, 20, 2>(c, dn, xvoff + kb * 5120, acc, xf, [&](auto ic) AI {
      constexpr int f = decltype(ic)::v, sn = f / 5 + PRE_D;
      if constexpr (f >= 0 && f % 5 == 1 && sn < 27) {
        if constexpr (sn == 16) {
          if (w == 0) STAMP(17);
          ctl_wait(c, C_GO4B, 1);
          if (w == 0) STAMP(18);
        }
        if constexpr (sn < 16) xload<20, 0, sn, 1, 16>(c, xf, xvoff, xs + kb * 16384, xm, rotD1);
        else xload<20, 16, sn - 16, 1, 11>(c, xf, xvoff, xs + 32768 + kb * 11264, xm, rotD2);
      }
    });
    if (w == 0) STAMP(19);
    unsigned y[10];
    pair_reduce(c, w, C_XD0, acc, y);
    const int l2 = opaque(lane);
    char* ob = out + (size_t)(16 * mtB * 1280 + cc * 160);
    const unsigned ro = (l2 & 15) * 1280 + (l2 >> 4) * 16;
    *(GAS v4u*)(ob + ro) = v4u{y[0], y[1], y[2], y[3]};
    *(GAS v4u*)(ob + ro + 64) = v4u{y[4], y[5], y[6], y[7]};
    *(GAS v2u*)(ob + (ro - (l2 >> 4) * 8 + 128)) = v2u{y[8], y[9]};
  }
  PADTO(0, 60);
  if (w == 0) { STAMP(15); DUMP(0, 20); DUMP(30, 32); }
}

extern "C" void kernel_launch(void* const* d_in, const int* in_sizes, int n_in, void* d_out, int out_size, void* d_ws, size_t ws_size,
                              hipStream_t stream) {
  (void)hipFuncSetAttribute((const void*)mk_kernel, hipFuncAttributeMaxDynamicSharedMemorySize, LDS_BYTES);
  mk_kernel<<<256, 512, LDS_BYTES, stream>>>((const char*)d_in[0], (const char*)d_in[1], (const char*)d_in[2], (const char*)d_in[4],
                                             (const char*)d_in[5], (char*)d_out, (char*)d_ws);
}
```

```cpp
#include <hip/hip_runtime.h>
#include <stdint.h>

typedef unsigned v4u __attribute__((ext_vector_type(4)));
typedef unsigned v2u __attribute__((ext_vector_type(2)));
typedef short v4s __attribute__((ext_vector_type(4)));
typedef float f32x4 __attribute__((ext_vector_type(4)));
typedef float f32x2 __attribute__((ext_vector_type(2)));
typedef __bf16 bf16x2 __attribute__((ext_vector_type(2)));
typedef __bf16 bf16x8 __attribute__((ext_vector_type(8)));
typedef unsigned long long u64;
typedef __amdgpu_buffer_rsrc_t rsrc_t;
#define LAS __attribute__((address_space(3)))
#define GAS __attribute__((address_space(1)))
#define DI __device__ __forceinline__
#define AI __attribute__((always_inline))
#define RLX_AGENT __ATOMIC_RELAXED, __HIP_MEMORY_SCOPE_AGENT
#define SCHED() __builtin_amdgcn_sched_barrier(0)
#define CFENCE() asm volatile("" ::: "memory")
#define DRAIN() asm volatile("s_waitcnt vmcnt(0)" ::: "memory")
#define VMWAIT(n) asm volatile("s_waitcnt vmcnt(" #n ")" ::: "memory")
#define LGKM0() asm volatile("s_waitcnt lgkmcnt(0)" ::: "memory")
#define ENDPGM() asm volatile("s_endpgm" ::: "memory")
#define PIN(v) asm volatile("" : "+v"(v))
#define LP(T, a) ((LAS T*)(uintptr_t)(unsigned)(a))
constexpr int SC1 = 16;
constexpr unsigned ONE2 = 0x3f803f80u;

#define DEP_A 12
#define DEP_P 10
#define PTF 1
#define PTS 21
#define LPRIO 0
#define XPRE 1
#define RPK_F 21
#define RPK_S 31
#define ROTA 1
#define STR_(x) #x
#define STR(x) STR_(x)

#define ROT 1
#define PRE_B 6
#define PRE_C 8
#define PRE_D 8

constexpr int OFF_EPOCH = 0;
constexpr int OFF_FLAGS = 4096;
constexpr int OFF_CS = 262144;
constexpr int OFF_HS = 327680;
constexpr int OFF_KVP = 524288;
constexpr int KVP_STR = 3456;
constexpr int OFF_WB = 2 << 20;
constexpr int OFF_WC = OFF_WB + 819200;
constexpr int OFF_WD = OFF_WC + 2293760;
constexpr int OFF_O = OFF_WD + 2211840;
constexpr int OFF_H = OFF_O + 2621440;
constexpr int OFF_F = OFF_H + 2621440;
constexpr int WS_BYTES = OFF_F + 7077888;
constexpr int OFF_PROF = 32 << 20;
enum { FLR = 0, FL1, FL2, FL3, FL4A, FL4B };
constexpr u64 EPOCH_STEP = 0x9E3779B97F4A7C15ull;

constexpr int RCH = 13;
constexpr int L_SCR = RCH * 10240;
constexpr int L_RPK = L_SCR + 20480;
constexpr int L_CSUM = L_RPK + 4096;
constexpr int L_DONE = L_CSUM + 512;
constexpr int L_LAND = L_DONE + 1024;
constexpr int L_CTL = L_LAND + 512;
constexpr int LDS_BYTES = L_CTL + 128;
static_assert(LDS_BYTES <= 163840, "lds");
enum { C_CAP = 0, C_CSK, C_CSQ, C_RED, C_CS, C_ARR1, C_GO1, C_BAR1, C_ARR2, C_GO2, C_XB0, C_XB1, C_ARR3, C_GO3, C_ARR4A, C_GO4A,
       C_ARR4B, C_GO4B, C_XD0, C_XD1 };

constexpr int CH_Q = 30, CH_B = 35, CH_C1 = 45, CH_C2 = 53, CH_D = 59, CH_END = 86;
constexpr int BLK = 26624;
static_assert(5 * BLK == RCH * 10240, "ring");
constexpr int needp(int ch) { return ch + 1; }
constexpr int slot(int ch) { return (ch - CH_Q) % RCH; }
constexpr int KVSLOT = slot(CH_B);
static_assert(KVSLOT + 4 <= 10, "kv slots must not touch the last block");

struct Ctx {
  rsrc_t rs, rt;
  unsigned dm;
  GAS char* ws;
  u64 ep;
  int lane;
  unsigned budget;
};

DI f32x4 mfma(v4u a, v4u b, f32x4 c) {
  return __builtin_amdgcn_mfma_f32_16x16x32_bf16(__builtin_bit_cast(bf16x8, a), __builtin_bit_cast(bf16x8, b), c, 0, 0, 0);
}
DI float asf(unsigned u) { return __builtin_bit_cast(float, u); }
DI unsigned asu(float f) { return __builtin_bit_cast(unsigned, f); }
DI unsigned pk2(float a, float b) { f32x2 f = {a, b}; return __builtin_bit_cast(unsigned, __builtin_convertvector(f, bf16x2)); }
DI float bflo(unsigned u) { return asf(u << 16); }
DI float bfhi(unsigned u) { return asf(u & 0xffff0000u); }
DI f32x4 unpk4(unsigned a, unsigned b) { return f32x4{bflo(a), bfhi(a), bflo(b), bfhi(b)}; }
DI float dot2(unsigned a, unsigned b, float c) {
  return __builtin_amdgcn_fdot2_f32_bf16(__builtin_bit_cast(bf16x2, a), __builtin_bit_cast(bf16x2, b), c, false);
}
DI v4u ld128(const Ctx& c, int voff, int soff) { return __builtin_amdgcn_raw_buffer_load_b128(c.rs, voff, soff, SC1); }
DI v2u ld64(const Ctx& c, int voff, int soff) { return __builtin_amdgcn_raw_buffer_load_b64(c.rs, voff, soff, SC1); }
DI void st128(const Ctx& c, v4u v, int voff, int soff) { __builtin_amdgcn_raw_buffer_store_b128(v, c.rs, voff + soff, 0, SC1); }
DI void st64(const Ctx& c, v2u v, int voff, int soff) { __builtin_amdgcn_raw_buffer_store_b64(v, c.rs, voff, soff, SC1); }
DI void st32(const Ctx& c, unsigned v, int voff, int soff) { __builtin_amdgcn_raw_buffer_store_b32(v, c.rs, voff, soff, SC1); }
DI u64 now() { return __builtin_amdgcn_s_memrealtime(); }

DI void ctl_add(int i) {
  asm volatile("v_mov_b32 v254, %0\n\tv_mov_b32 v255, %1\n\ts_mov_b64 exec, 1\n\tds_add_u32 v254, v255\n\ts_mov_b64 exec, -1"
               :: "s"(L_CTL + 4 * i), "i"(L_CTL) : "memory", "v254", "v255");
}
template <int I> DI void ctl_addc() {
  asm volatile("v_mov_b32 v254, %0\n\ts_mov_b64 exec, 1\n\tds_add_u32 v254, v254 offset:%1\n\ts_mov_b64 exec, -1"
               :: "i"(L_CTL), "i"(4 * I) : "memory", "v254");
}
template <int SLP = 0> DI void ctl_wait(Ctx& c, int i, int n) {
  unsigned t;
  asm volatile(
      "v_mov_b32 v254, %2\n"
      ".Lw%=:\n\t"
      ".if %4\n\ts_sleep %4\n\t.endif\n\t"
      "ds_read_b32 v255, v254\n\t"
      "s_sub_u32 %0, %0, 1\n\t"
      "s_max_i32 %0, %0, 0\n\t"
      "s_waitcnt lgkmcnt(0)\n\t"
      "v_readfirstlane_b32 %1, v255\n\t"
      "s_cmp_lt_u32 %1, %3\n\t"
      "s_cselect_b32 %1, %0, 0\n\t"
      "s_cmp_lg_u32 %1, 0\n\t"
      "s_cbranch_scc1 .Lw%="
      : "+{s71}"(c.budget), "=&s"(t) : "s"(L_CTL + 4 * i), "s"(n * L_CTL), "i"(SLP) : "memory", "scc", "v254", "v255");
}
DI unsigned ctl_read(int i) { return __builtin_amdgcn_readfirstlane(*LP(volatile unsigned, L_CTL + 4 * i)); }
DI void flag_set(const Ctx& c, int kind, int idx) {
  if (c.lane == 0) __hip_atomic_store((GAS u64*)(c.ws + OFF_FLAGS + (kind * 256 + idx) * 128), c.ep, RLX_AGENT);
}
DI void flag_wait(const Ctx& c, int kind, int idx, int nl) {
  if (c.lane < nl) {
    GAS u64* p = (GAS u64*)(c.ws + OFF_FLAGS + (kind * 256 + idx) * 128);
    unsigned n = 1u << 17;
    bool go;
    do {
      const u64 v = __hip_atomic_load(p, RLX_AGENT);
      go = __any(v != c.ep) && --n != 0;
    } while (go);
  }
  CFENCE();
}
#define STAMP(i) do {} while (0)
#define STL(i) do {} while (0)
#define DUMPL(...) do {} while (0)

DI v2u swap16(unsigned a, unsigned b) { return __builtin_amdgcn_permlane16_swap(a, b, false, false); }
DI v2u swap32(unsigned a, unsigned b) { return __builtin_amdgcn_permlane32_swap(a, b, false, false); }
DI float sum_g(float v) {
  v2u r = swap16(asu(v), asu(v));
  v = asf(r.x) + asf(r.y);
  r = swap32(asu(v), asu(v));
  return asf(r.x) + asf(r.y);
}
DI v2u tr1(unsigned a, int off) { return __builtin_bit_cast(v2u, __builtin_amdgcn_ds_read_tr16_b64_v4i16(LP(v4s, a + off))); }
DI v4u tr2(unsigned a, int off, int hioff) {
  const v2u lo = tr1(a, off), hi = tr1(a, off + hioff);
  return v4u{lo.x, lo.y, hi.x, hi.y};
}

DI int gam0(int j) { return 27 * (j >> 2) + 7 * (j & 3); }
template <int KIND> DI int kbase(int s, int g) {
  if (KIND == 1) return s < 16 ? 40 * s + 5 * g : 40 * (4 * (s - 16) + g) + 23;
  if (KIND == 2) {
    const int u = s / 5, v = s - 5 * u;
    return 160 * u + (v < 2 ? 32 * v + 8 * g : v == 2 ? 64 + (g >> 1) * 80 + 8 * (g & 1) : 32 * v - 16 + 8 * g);
  }
  if (s < 48) return 16 * (s < 32 ? gam0(s >> 1) + 2 * (s & 1) : gam0(s - 32) + 4) + 4 * g;
  const int l = 2 * (s - 48) + (g >> 1), q = l / 3;
  return 16 * (gam0(4 * q + (l - 3 * q)) + 6) + 8 * (g & 1);
}
template <int KIND> DI int kpat(int s, int j) {
  if (KIND == 1) return s < 16 ? (j >= 5 ? 15 + j : j) : 5 * (j >> 1) + (j & 1);
  if (KIND == 2) return j;
  return s < 48 ? 16 * (j >> 2) + (j & 3) : j;
}
template <int KIND> DI int sglob(int rb, int sp) {
  if (KIND == 1) return sp < 4 ? 4 * rb + sp : 16 + rb;
  if (KIND == 3) return 6 * rb + sp;
  return 5 * rb + sp;
}

template <int KIND> DI void repack(Ctx& c, const char* W, int rb, int cb, unsigned at) {
  constexpr unsigned bb = L_SCR + 3072, bs = 5120;
  constexpr int NS = KIND == 3 ? 6 : 5;
  constexpr int PITCH = KIND == 2 ? 3456 : 1280;
  const rsrc_t rr_ = __builtin_amdgcn_make_buffer_rsrc((void*)W, 0, KIND == 1 ? 819200 : 2211840, 0x00020000);
  const int l = c.lane, n = l & 15, g = l >> 4, q = (l >> 2) & 3, p = l & 3;
  int va = kpat<KIND>(sglob<KIND>(rb, 0), l >> 3) * PITCH + (l & 7) * 16, vb = kpat<KIND>(sglob<KIND>(rb, NS - 1), l >> 3) * PITCH + (l & 7) * 16;
  int so[NS][4];
#pragma unroll
  for (int sp = 0; sp < NS; ++sp)
#pragma unroll
    for (int gg = 0; gg < 4; ++gg) { so[sp][gg] = kbase<KIND>(sglob<KIND>(rb, sp), gg) * PITCH + cb * 128; asm volatile("" : "+s"(so[sp][gg])); }
  int base[4];
  unsigned src[4];
#pragma unroll
  for (int ww = 0; ww < 4; ++ww) {
    const int gam = 4 * cb + ww;
    int sig = p;
    if (KIND == 2) {
      const int grp = gam / 27, rr = gam - 27 * grp, jj = min(rr / 7, 3), t = rr - 7 * jj;
      base[ww] = OFF_WC + (4 * grp + jj) * 143360 + (t < 4 ? t + 20 * rb : 76 + t + 15 * rb) * 1024 + l * 16;
      if (t >= 4) base[ww] |= 1;
    } else {
      const int cc = gam / 5, cq = gam - 5 * cc;
      int tile = 4, slot = n;
      if (cq < 4) { sig = ((p & 1) << 1) | (p >> 1); tile = 2 * (cq >> 1) + (n >> 3); slot = 8 * (cq & 1) + (n & 7); }
      base[ww] = (KIND == 1 ? OFF_WB + cc * 102400 : OFF_WD + cc * 276480) + tile * 1024 + (g * 16 + slot) * 16;
    }
    src[ww] = bb + g * bs + q * 128 + 32 * ww + 8 * sig;
  }
#pragma unroll
  for (int ww = 0; ww < 4; ++ww) { PIN(base[ww]); PIN(src[ww]); }
  PIN(va); PIN(vb);
  {
    unsigned n_ = 0;
    while (min(__builtin_amdgcn_readfirstlane(*LP(volatile unsigned, L_LAND)), __builtin_amdgcn_readfirstlane(*LP(volatile unsigned, L_LAND + 256))) < at && ++n_ < (1u << 16)) __builtin_amdgcn_s_sleep(4);
    CFENCE();
  }
  v4u r[NS][4];
#pragma unroll
  for (int sp = 0; sp < NS; ++sp)
#pragma unroll
    for (int gg = 0; gg < 4; ++gg) r[sp][gg] = __builtin_amdgcn_raw_buffer_load_b128(rr_, sp == NS - 1 ? vb : va, so[sp][gg], 0);
  float cs[4] = {0.f, 0.f, 0.f, 0.f};
#pragma unroll
  for (int sp = 0; sp < NS; ++sp) {
#pragma unroll
    for (int gg = 0; gg < 4; ++gg) *LP(v4u, bb + gg * bs + l * 16) = r[sp][gg];
    CFENCE();
    const int s = sglob<KIND>(rb, sp);
    v4u v[4];
#pragma unroll
    for (int ww = 0; ww < 4; ++ww) v[ww] = tr2(src[ww], 0, 512);
    CFENCE();
#pragma unroll
    for (int ww = 0; ww < 4; ++ww) {
      int off;
      if (KIND == 1) off = base[ww] + (s >= 10 ? (s - 10) * 10 + 5 : s * 10) * 1024;
      else if (KIND == 3) off = base[ww] + (s < 16 ? s * 10 : s < 32 ? (s - 16) * 10 + 5 : s < 43 ? (s - 16) * 10 : (s - 27) * 10 + 5) * 1024;
      else off = (base[ww] & ~1) + sp * ((base[ww] & 1) ? 3072 : 4096);
      st128(c, v[ww], off, 0);
      if (KIND == 2) cs[ww] = dot2(v[ww].x, ONE2, dot2(v[ww].y, ONE2, dot2(v[ww].z, ONE2, dot2(v[ww].w, ONE2, cs[ww]))));
    }
  }
  if (KIND == 2) {
#pragma unroll
    for (int ww = 0; ww < 4; ++ww) cs[ww] = sum_g(cs[ww]);
    const float v = g == 0 ? cs[0] : g == 1 ? cs[1] : g == 2 ? cs[2] : cs[3];
    st32(c, asu(v), l * 4, OFF_CS + (rb * 1792 + 64 * cb) * 4);
  }
}

template <int CH> DI void ring_wait(Ctx& c) {
  asm volatile("s_movk_i32 s70, %1\n\ts_call_b64 s[72:73], .Lmk_ring"
               : "+{s71}"(c.budget) : "i"(needp(CH)) : "memory", "scc", "vcc", "s70", "s72", "s73", "v254", "v255");
}
template <int CH> DI void ring_cmp(unsigned la) { asm volatile("v_cmp_gt_u32 vcc, %1, %0" :: "v"(la), "i"(needp(CH)) : "vcc"); }
template <int CH> DI void ring_br(Ctx& c) {
  asm volatile("s_cbranch_vccnz .Lrs%=\n\t.subsection 1\n.Lrs%=:\n\ts_movk_i32 s70, %1\n\ts_call_b64 s[72:73], .Lmk_ring\n\ts_branch .Lrb%=\n\t.subsection 0\n.Lrb%=:"
               : "+{s71}"(c.budget) : "i"(needp(CH)) : "memory", "scc", "vcc", "s70", "s72", "s73", "v254", "v255");
}
#define TOUCH(x) asm volatile("" :: "v"(x))
template <int N> DI void release(const Ctx& c) {
  asm volatile("s_mov_b32 m0, %1\n\ts_movk_i32 s70, %2\n\tbuffer_load_dword off, %0, s70 lds" :: "s"(c.rt), "s"(c.dm), "i"(4 * N) : "memory", "s70");
}
constexpr int upto(int ch) {
  if (ch == CH_Q) return CH_B - 1;
  if (ch == CH_B) return CH_C1 - 1;
  if (ch == CH_C1) return CH_C2 - 1;
  if (ch == CH_C2) return CH_D - 2;
  if (ch == CH_D - 1) return ch;
  if (ch == CH_D) return CH_D + 6;
  if (ch == CH_D + 7) return CH_D + 12;
  if (ch >= CH_D + 13 && (ch - CH_D - 13) % 2 == 0) return ch + 1;
  return -1;
}
constexpr bool released(int n) { return n + RCH <= CH_END + 1 && (n <= CH_B || (n > CH_C1 && n <= CH_D) || (n - CH_B) % 2 == 0); }
template <int I> struct IC { static constexpr int v = I; };
template <int B, int E, class Fn> DI void static_for(Fn&& fn) {
  if constexpr (B < E) { fn(IC<B>{}); static_for<B + 1, E>(fn); }
}
template <int D> DI void bump(unsigned& b) { if constexpr (D != 0) asm volatile("v_add_u32 %0, %1, %0" : "+v"(b) : "i"(D)); }
constexpr int breg(int b) { return ((b % 5) / 2) * 2 * BLK; }
constexpr int bin(int b) { return ((b % 5) % 2) * BLK; }
constexpr int region(int ch) { return (slot(ch) / 6) * 61440; }
constexpr int inreg(int ch) { return (slot(ch) % 6) * 10240; }

template <int TL, bool Z> DI void mm(f32x4& acc, const v4u& wf, const v4u& x) {
  if constexpr (Z && TL) asm volatile("v_mfma_f32_16x16x32_bf16 %0, %1, %2, 0" : "=&v"(acc) : "v"(wf), "v"(x));
  else if constexpr (Z) asm volatile("v_mfma_f32_16x16x32_bf16 %0, %2, %1, 0" : "=&v"(acc) : "v"(wf), "v"(x));
  else if constexpr (TL) asm volatile("v_mfma_f32_16x16x32_bf16 %0, %1, %2, %0" : "+v"(acc) : "v"(wf), "v"(x));
  else asm volatile("v_mfma_f32_16x16x32_bf16 %0, %2, %1, %0" : "+v"(acc) : "v"(wf), "v"(x));
}
template <int NT, int NKS, int CH0, int FPC, int XW, int ZM, class Hook>
DI void gemm_pass(Ctx& c, unsigned dn, unsigned wb, f32x4 (&acc)[2][NT], v4u (&x)[2][XW], Hook&& hook) {
  constexpr int LA = 3, WBN = LA + 1, NF = NKS * NT;
  v4u wf[WBN];
  unsigned la;
  bump<region(CH0)>(wb);
  SCHED();
  static_for<-LA, NF>([&](auto ic) AI {
    constexpr int f = decltype(ic)::v, fn = f + LA;
    constexpr int s = f < 0 ? 0 : f / NT, t = f < 0 ? 0 : f % NT;
    constexpr int ch = CH0 + fn / FPC, off = inreg(ch) + (fn % FPC) * 1024;
    constexpr auto chk = [](int i) { return i > 0 && i < NF && i % FPC == 0 && upto(CH0 + i / FPC) >= 0; };
    constexpr bool rel = f >= 0 && (f + 1) % FPC == 0 && released(CH0 + (f + 1) / FPC);
    if constexpr (f >= 0) { mm<1, (ZM & 1) && s == 0>(acc[0][t], wf[f % WBN], x[0][s % XW]); SCHED(); }
    if constexpr (fn < NF) {
      if constexpr (fn == 0) ring_wait<upto(ch)>(c);
      if constexpr (chk(fn)) ring_br<upto(ch)>(c);
      wf[fn % WBN] = *LP(v4u, wb + off);
      SCHED();
    }
    if constexpr (f >= 0) { mm<1, (ZM & 2) && s == 0>(acc[1][t], wf[f % WBN], x[1][s % XW]); SCHED(); }
    if constexpr (fn + 1 < NF && (fn + 1) % FPC == 0) bump<region(ch + 1) - region(ch)>(wb);
    if constexpr (rel) release<CH0 + (f + 1) / FPC>(c);
    if constexpr (chk(fn + 4)) la = *LP(unsigned, dn);
    hook(ic);
    SCHED();
    if constexpr (chk(fn + 1)) { ring_cmp<upto(CH0 + (fn + 1) / FPC)>(la); SCHED(); }
  });
  asm volatile("s_nop 7");
  SCHED();
}
template <int XW, int S0, int S1, int N, int LEN> DI void xload(const Ctx& c, v4u (&x)[2][XW], int xvoff, int xsoff, int xmt, int rot) {
#pragma unroll
  for (int s = S1; s < S1 + N; ++s) {
    const int o = xsoff + (rot + s >= LEN ? rot + s - LEN : rot + s) * 1024;
    x[0][(S0 + s) % XW] = ld128(c, xvoff, o);
    x[1][(S0 + s) % XW] = ld128(c, xvoff, o + xmt);
  }
  SCHED();
}
DI int opaque(int v) { asm volatile("" : "+v"(v)); return v; }
constexpr float LOG2E = 1.4426950408889634f, PHID = 1.4426950408889634e-6f;
DI v2u phi4(f32x4 u) {
  f32x4 e, m;
#pragma unroll
  for (int i = 0; i < 4; ++i) { e[i] = __builtin_amdgcn_fmed3f(__builtin_amdgcn_exp2f(u[i]), 0.f, 1.f); m[i] = fmaxf(u[i], PHID); }
  const f32x4 y = m * 0.6931471805599453f + e;
  return v2u{pk2(y[0], y[1]), pk2(y[2], y[3])};
}

DI void pair_reduce(Ctx& c, int w, int cx, f32x4 (&acc)[2][5], unsigned (&y)[10]) {
  const unsigned lo = opaque(c.lane) * 16 + L_SCR;
  LAS f32x4* mine = LP(f32x4, lo + w * 5120);
  LAS f32x4* other = LP(f32x4, lo + (w ^ 1) * 5120);
#pragma unroll
  for (int t = 0; t < 5; ++t) mine[t * 64] = acc[1][t];
  ctl_add(cx + (w >> 1));
  ctl_wait(c, cx + (w >> 1), 2);
#pragma unroll
  for (int t = 0; t < 5; ++t) {
    const f32x4 o = acc[0][t] + other[t * 64];
    y[2 * t] = pk2(o[0], o[1]); y[2 * t + 1] = pk2(o[2], o[3]);
  }
}
DI int mod10(int v) { return v >= 20 ? v - 20 : v >= 10 ? v - 10 : v; }

__global__ void __launch_bounds__(512, 2)
mk_kernel(const char* __restrict__ x, const char* __restrict__ wqkv, const char* __restrict__ wo, const char* __restrict__ w1,
          const char* __restrict__ w2, char* __restrict__ out, char* ws) {
  u64 cpc; unsigned clen, toff;
  asm volatile(
      "s_getpc_b64 %0\n"
      ".Lpc%=:\n\t"
      "s_mov_b32 %1, .Lend%=-.Lpc%=-4\n\t"
      "s_mov_b32 %2, .Lmk_tab-.Lpc%=\n\t"
      ".subsection 1\n"
      ".Lmk_ring:\n\t"
      "v_mov_b32 v254, %3\n\t"
      "ds_read2_b32 v[254:255], v254 offset1:64\n\t"
      "s_sub_u32 s71, s71, 1\n\t"
      "s_cbranch_scc1 .Lmk_give\n\t"
      "s_waitcnt lgkmcnt(0)\n\t"
      "v_min_u32 v254, v254, v255\n\t"
      "v_cmp_gt_u32 vcc, s70, v254\n\t"
      "s_cbranch_vccnz .Lmk_ring\n\t"
      "s_setpc_b64 s[72:73]\n"
      ".Lmk_give:\n\t"
      "s_waitcnt lgkmcnt(0)\n\t"
      "s_mov_b32 s71, 0\n\t"
      "s_setpc_b64 s[72:73]\n\t"
      ".subsection 2\n"
      ".Lend%=:\n\t"
      ".fill 128, 4, 0xBF800000\n\t"
      ".subsection 0"
      : "=s"(cpc), "=s"(clen), "=s"(toff) : "i"(L_LAND));
  const int tid = threadIdx.x, w = __builtin_amdgcn_readfirstlane(tid >> 6);
  int lane = tid & 63;
  const int b = blockIdx.x, xc = b & 7, sx = b >> 3;
  const int pi = 4 * (xc >> 1) + (sx >> 3), pj = (xc & 1) * 8 + (sx & 7);
  const int pq_ = pj ^ 1;
  const int hp = 4 * xc + (sx >> 3), cc = sx & 7;
  const int qi = 2 * xc + (sx >> 4), qj = sx & 15;
  const int rotB = ROT * ((5 * cc) >> 2), rotC = ROT * 5 * (qj & 3), rotD1 = ROT * ((2 * cc + 5 * (sx >> 3)) & 15);
  const int rd2 = ((11 * cc) >> 3) + 3 * (sx >> 3), rotD2 = ROT * (rd2 >= 11 ? rd2 - 11 : rd2);
  int n16 = lane & 15, g = lane >> 4;
  Ctx c;
  c.lane = lane;
  c.rt = __builtin_amdgcn_make_buffer_rsrc((void*)(cpc + toff), 0, 512, 0x00020000);
  if (__builtin_expect((w >> 1) == 2, 1)) {
    __builtin_amdgcn_s_setprio(LPRIO);
    const int rot = ROTA == 0 ? 0 : ROTA == 2 ? 5 * (sx & 1) : mod10((sx & 7) + ((5 * (sx >> 3)) >> 1) + (xc >> 1));
    const int Lw = w - 4;
    const int ln = opaque(lane), xr = ln >> 3;
    int xv[4], ka[5], qa[5];
    bool hi_[5];
    xv[0] = xr * 1280 + (((ln & 7) ^ (xr >> 1)) * 16) + 3072; xv[1] = (xv[0] ^ 64) + 10240 - 1024; xv[2] = xv[0] - 2048; xv[3] = xv[1] - 2048;
    const auto raw_addr = [&](int (&a)[5], int lo, int hi) AI {
#pragma unroll
      for (int i = 0; i < 5; ++i) {
        const unsigned m = ln * 205 + 64 * 205 * i, rho = m >> 11;
        const int ch16 = ln * 16 + 1024 * i - 160 * rho;
        const int k = ((m >> 10) & 24) | ((m >> 13) & 4) | (rho & 3);
        hi_[i] = ch16 >= 80;
        a[i] = k * 3840 + ch16 + 3072 - (i & 3) * 1024 + (ch16 < 80 ? lo : hi);
        PIN(a[i]);
      }
    };
#pragma unroll
    for (int i = 0; i < 4; ++i) PIN(xv[i]);
    unsigned lim = RCH, bud = 1u << 20, key = 0;
    const unsigned lw = Lw * 5120;
    int sxr = (128 * pi + 64 * Lw) * 1280, sr = rot * 128, sl = Lw * 8192;
    asm volatile("" : "+s"(sxr), "+s"(sr), "+s"(sl));
    SCHED();
    const rsrc_t rx = __builtin_amdgcn_make_buffer_rsrc((void*)(x - 3072), 0, 2048 * 1280 + 3072, 0x00020000);
#define MK_ST "+{s70}"(lim), "+{s71}"(bud), "+{s84}"(key), "+{s80}"(sr), "+{s85}"(sl)
#define MK_IN "{s81}"(lw), "{s76}"(L_LAND + Lw * 256), [rt] "s"(c.rt)
#define MK_CL "memory", "scc", "s72", "s73", "s74", "s75", "s77", "s78", "s79", "s82", "s83", "s86", "s87", "s88", "s90", "s91", "v248", "v252", "v253", "v254", "v255"
    asm volatile(
        ".macro MK_LD dep, ops:vararg\n\t"
        ".if \\dep\n\ts_waitcnt vmcnt(\\dep)\n\t.endif\n\t"
        "buffer_load_dwordx4 \\ops lds\n\t"
        ".endm\n\t"
        ".macro MK_FLAG rt, add\n\t"
        "s_mov_b32 m0, s76\n\t"
        "s_lshl2_add_u32 s83, s84, 4*\\add\n\t"
        "buffer_load_dword off, \\rt, s83 lds\n\t"
        ".endm\n\t"
        ".macro MK_HEAD key\n\t"
        "s_cmp_gt_i32 s70, \\key\n\t"
        "s_cbranch_scc1 .Lh\\@\n\t"
        "s_mov_b32 s74, \\key\n\t"
        "s_call_b64 s[72:73], .Lmk_lslow\n"
        ".Lh\\@:\n\t"
        ".endm\n\t"
        ".macro MK_RAW dep, a0, a1, a2, a3, a4, rs, step\n\t"
        "MK_LD \\dep, \\a0, \\rs, s82 offen\n\t"
        "s_add_u32 s78, s82, \\step\n\t"
        "MK_LD \\dep, \\a1, \\rs, s82 offen offset:1024\n\t"
        "s_sub_u32 s79, s78, 2457600\n\t"
        "MK_LD \\dep, \\a2, \\rs, s82 offen offset:2048\n\t"
        "MK_LD \\dep, \\a3, \\rs, s82 offen offset:3072\n\t"
        "s_add_u32 m0, m0, 4096\n\t"
        "s_min_u32 s78, s78, s79\n\t"
        "MK_LD \\dep, \\a4, \\rs, s82 offen\n\t"
        "s_mov_b32 s82, s78\n\t"
        ".endm\n\t"
        ".macro MK_X4 dep, a0, a1, a2, a3, rs\n\t"
        "MK_LD \\dep, \\a0, \\rs, s77 offen\n\t"
        "s_add_u32 s78, s77, 20480\n\t"
        "MK_LD \\dep, \\a1, \\rs, s77 offen offset:1024\n\t"
        "MK_LD \\dep, \\a2, \\rs, s78 offen offset:2048\n\t"
        "s_add_u32 s77, s77, 40960\n\t"
        "MK_LD \\dep, \\a3, \\rs, s78 offen offset:3072\n\t"
        ".endm\n\t"
        ".macro MK_X dep, a0, a1, a2, a3, rs, sx\n\t"
        "s_mov_b32 m0, s85\n\t"
        "s_add_u32 s77, s80, \\sx\n\t"
        "MK_X4 \\dep, \\a0, \\a1, \\a2, \\a3, \\rs\n\t"
        "s_add_u32 m0, s85, 4096\n\t"
        "s_add_u32 s80, s80, 128\n\t"
        "MK_X4 \\dep, \\a0, \\a1, \\a2, \\a3, \\rs\n\t"
        "s_sub_u32 s79, s80, 1280\n\t"
        "s_min_u32 s80, s80, s79\n\t"
        ".endm\n\t"
        ".subsection 1\n"
        ".Lmk_lsleep:\n\t"
        "s_sleep 1\n"
        ".Lmk_lslow:\n\t"
        "v_mov_b32 v248, %[dn]\n\t"
        "ds_read2st64_b32 v[252:253], v248 offset1:1\n\t"
        "ds_read2st64_b32 v[254:255], v248 offset0:2 offset1:3\n\t"
        "ds_read_b32 v248, v248 offset:%[cap]\n\t"
        "s_sub_u32 s71, s71, 1\n\t"
        "s_cbranch_scc1 .Lmk_lgive\n\t"
        "s_waitcnt lgkmcnt(0)\n\t"
        "v_min3_u32 v252, v252, v253, v254\n\t"
        "v_min_u32 v252, v252, v255\n\t"
        "v_add_u32 v252, 13, v252\n\t"
        "v_min_u32 v252, v252, v248\n\t"
        "s_nop 0\n\t"
        "v_readfirstlane_b32 s70, v252\n\t"
        "s_cmp_gt_i32 s70, s74\n\t"
        "s_cbranch_scc0 .Lmk_lsleep\n\t"
        "s_setpc_b64 s[72:73]\n"
        ".Lmk_lgive:\n\t"
        "s_endpgm\n\t"
        ".subsection 0\n\t"
        "MK_X 0, %[x0], %[x1], %[x2], %[x3], %[rx], %[sx]"
        : MK_ST
        : MK_IN, [x0] "v"(xv[0]), [x1] "v"(xv[1]), [x2] "v"(xv[2]), [x3] "v"(xv[3]), [rx] "s"(rx), [sx] "s"(sxr),
          [dn] "i"(L_DONE), [cap] "i"(L_CTL + 4 * C_CAP - L_DONE)
        : MK_CL);
    SCHED();
    *LP(unsigned, L_LAND + Lw * 256 + ln * 4) = 0u;
    raw_addr(ka, 1280, 2480);
    const int ok = Lw * 122880 + pj * 80 - 3072;
    const rsrc_t rk = __builtin_amdgcn_make_buffer_rsrc((void*)(wqkv + ok), 0, 640 * 3840 - ok, 0x00020000);
    asm volatile(
        "s_mov_b32 s82, %[sk]\n\t"
        "s_waitcnt lgkmcnt(0)\n"
        ".Lmk_blk:\n\t"
        "s_add_u32 m0, s85, %[kl]\n\t"
        "s_add_u32 s85, s85, 26624\n\t"
        "MK_RAW " STR(DEP_A) ", %[k0], %[k1], %[k2], %[k3], %[k4], %[rk], 245760\n\t"
        "MK_FLAG %[rt], 3\n\t"
        "s_sub_u32 s79, s85, 133120\n\t"
        "s_min_u32 s85, s85, s79\n\t"
        "s_add_u32 s84, s84, 3\n\t"
        "s_cmpk_eq_u32 s84, 30\n\t"
        "s_cbranch_scc1 .Lmk_blkend\n\t"
        "s_cmpk_lg_u32 s84, 3*" STR(XPRE) "\n\t"
        "s_cbranch_scc1 .Lmk_nobar\n\t"
        "s_barrier\n"
        ".Lmk_nobar:\n\t"
        "MK_HEAD s84\n\t"
        "MK_X " STR(DEP_A) ", %[x0], %[x1], %[x2], %[x3], %[rx], %[sx]\n\t"
        "s_branch .Lmk_blk\n"
        ".Lmk_blkend:\n\t"
        ".purgem MK_X4\n\t.purgem MK_X"
        : MK_ST
        : MK_IN, [x0] "v"(xv[0]), [x1] "v"(xv[1]), [x2] "v"(xv[2]), [x3] "v"(xv[3]), [rx] "s"(rx), [sx] "s"(sxr),
          [kl] "s"(16384 - Lw * 3072), [k0] "v"(ka[0]), [k1] "v"(ka[1]), [k2] "v"(ka[2]), [k3] "v"(ka[3]), [k4] "v"(ka[4]), [rk] "s"(rk), [sk] "s"(rot * 245760)
        : MK_CL);
    SCHED();
#pragma unroll
    for (int i = 0; i < 5; ++i) { qa[i] = ka[i] - 1280 + (hi_[i] ? 32 * 3840 - 80 - 1200 : 0); PIN(qa[i]); }
    const int oq = pq_ * 80 - 3072;
    const rsrc_t rw = __builtin_amdgcn_make_buffer_rsrc((void*)(wqkv + oq), 0, 640 * 3840 - oq, 0x00020000);
    char* ws2 = ws;
    asm volatile("" : "+s"(ws2));
    const rsrc_t rs = __builtin_amdgcn_make_buffer_rsrc((void*)ws2, 0, WS_BYTES, 0x00020000);
    asm volatile(
        "s_mov_b32 s82, %[sq]\n\t"
        "s_mov_b32 s85, s81\n\t"
        "s_movk_i32 s86, 30\n"
        ".Lmk_q:\n\t"
        "MK_HEAD s86\n\t"
        "s_mov_b32 m0, s85\n\t"
        "s_add_u32 s85, s85, 10240\n\t"
        "MK_RAW " STR(DEP_A) ", %[q0], %[q1], %[q2], %[q3], %[q4], %[rw], 491520\n\t"
        "MK_FLAG %[rt], 1\n\t"
        "s_add_u32 s84, s84, 1\n\t"
        "s_cmpk_ge_u32 s84, 32\n\t"
        "s_cselect_b32 s86, 33, 30\n\t"
        "s_cmpk_lg_u32 s84, 35\n\t"
        "s_cbranch_scc1 .Lmk_q\n\t"
        ".macro MK_SEG rot, base, len, end\n\t"
        "s_mov_b32 s80, \\rot\n\t"
        "s_mov_b32 s87, \\base\n\t"
        "s_mov_b32 s88, \\len\n\t"
        "s_movk_i32 s86, \\end\n\t"
        "s_call_b64 s[90:91], .Lmk_pk\n\t"
        ".endm\n\t"
        "MK_SEG %[rb], %[sb], 102400, 45\n\t"
        "MK_SEG %[rc1], %[sc1], 81920, 53\n\t"
        "MK_SEG %[rc2], %[sc2], 61440, 59\n\t"
        "MK_SEG %[rd1], %[sd1], 163840, 75\n\t"
        "MK_SEG %[rd2], %[sd2], 112640, 86\n\t"
        "s_waitcnt vmcnt(0)\n\t"
        "s_endpgm\n"
        ".Lmk_pk:\n\t"
        "MK_HEAD s84\n\t"
        "s_mov_b32 m0, s85\n\t"
        "s_add_u32 s77, s80, s87\n\t"
        "MK_LD " STR(DEP_P) ", %[l0], %[rs], s77 offen sc1\n\t"
        "s_add_u32 s80, s80, 10240\n\t"
        "MK_LD " STR(DEP_P) ", %[l0], %[rs], s77 offen offset:1024 sc1\n\t"
        "s_sub_u32 s79, s80, s88\n\t"
        "MK_LD " STR(DEP_P) ", %[l0], %[rs], s77 offen offset:2048 sc1\n\t"
        "s_min_u32 s80, s80, s79\n\t"
        "MK_LD " STR(DEP_P) ", %[l0], %[rs], s77 offen offset:3072 sc1\n\t"
        "s_add_u32 m0, m0, 4096\n\t"
        "s_add_u32 s85, s85, 10240\n\t"
        "MK_LD " STR(DEP_P) ", %[l1], %[rs], s77 offen sc1\n\t"
        "MK_FLAG %[rt], 1\n\t"
        "s_sub_u32 s79, s85, 133120\n\t"
        "s_min_u32 s85, s85, s79\n\t"
        "s_add_u32 s84, s84, 1\n\t"
        "s_cmp_lg_u32 s84, s86\n\t"
        "s_cbranch_scc1 .Lmk_pk\n\t"
        "s_setpc_b64 s[90:91]\n"
        ".Lmk_tab:\n\t"
        ".set mk_i, 0\n\t.rept 128\n\t.long mk_i\n\t.set mk_i, mk_i + 1\n\t.endr\n\t"
        ".purgem MK_FLAG\n\t.purgem MK_HEAD\n\t.purgem MK_RAW\n\t.purgem MK_SEG\n\t.purgem MK_LD"
        : MK_ST
        : MK_IN, [q0] "v"(qa[0]), [q1] "v"(qa[1]), [q2] "v"(qa[2]), [q3] "v"(qa[3]), [q4] "v"(qa[4]), [rw] "s"(rw), [sq] "s"(mod10(rot + Lw) * 245760),
          [l0] "v"(ln * 16), [l1] "v"(ln * 16 + 4096), [rs] "s"(rs),
          [sb] "s"(OFF_WB + cc * 102400), [rb] "s"(rotB * 10240 + lw),
          [sc1] "s"(OFF_WC + qj * 143360), [rc1] "s"(rotC * 4096 + lw),
          [sc2] "s"(OFF_WC + qj * 143360 + 81920), [rc2] "s"(rotC * 3072 + lw),
          [sd1] "s"(OFF_WD + cc * 276480), [rd1] "s"(rotD1 * 10240 + lw),
          [sd2] "s"(OFF_WD + cc * 276480 + 163840), [rd2] "s"(rotD2 * 10240 + lw)
        : MK_CL);
  }
  STAMP(0);
  *LP(v4u, L_DONE + lane * 16) = v4u{0u, 0u, 0u, 0u};
  *LP(unsigned, L_CTL + (lane & 31) * 4) = (lane & 31) == C_CAP ? CH_B : 0u;
  c.ws = (GAS char*)ws;
  c.rs = __builtin_amdgcn_make_buffer_rsrc((void*)ws, 0, WS_BYTES, 0x00020000);
  c.budget = 1u << 22;
  if (__builtin_expect(w >= 4, 0)) {
    __builtin_amdgcn_s_setprio(3);
    LGKM0();
    __builtin_amdgcn_s_barrier();
    CFENCE();
    const u64 e0 = __hip_atomic_load((GAS u64*)(c.ws + OFF_EPOCH), RLX_AGENT);
    {
      const unsigned lo = __builtin_amdgcn_readfirstlane((unsigned)e0), hi = __builtin_amdgcn_readfirstlane((unsigned)(e0 >> 32));
      c.ep = (((u64)hi << 32) | lo) + EPOCH_STEP;
    }
    if (w == 6) {
      const bool fast = ((0xa5 >> (pj & 7)) & 1) != 0;
      const int unit = (fast ? 0 : 128) + pi * 8 + (pj >> 1);
      const unsigned at = fast ? needp(RPK_F) : needp(RPK_S);
      if (unit < 40) repack<1>(c, wo, unit / 10, unit % 10, at);
      else if (unit < 148) repack<2>(c, w1, (unit - 40) / 27, (unit - 40) % 27, at);
      else if (unit < 238) repack<3>(c, w2, (unit - 148) / 10, (unit - 148) % 10, at);
      if (unit < 238) { DRAIN(); flag_set(c, FLR, unit); }
      STL(62);
      STAMP(22);
      const int g0 = gam0(qj);
      int idx = (lane & 3) * 10 + ((80 * cc + ((lane >> 2) & 1) * 79) >> 6);
      if (lane >= 8 && lane < 20) { const int cb = (g0 >> 2) + ((lane - 8) >> 2); if (cb < 27 && 4 * cb <= g0 + 6) idx = 40 + (lane & 3) * 27 + cb; }
      else if (lane >= 20 && lane < 38) { const int l = lane - 20; idx = 148 + (l >> 1) * 10 + ((80 * cc + (l & 1) * 79) >> 6); }
      ctl_wait<1>(c, C_RED, 1);
      flag_wait(c, FLR, idx, 8);
      if (lane == 0) *LP(volatile unsigned, L_CTL + 4 * C_CAP) = CH_C1;
      STAMP(20);
      flag_wait(c, FLR, idx, 20);
      if (lane == 0) *LP(volatile unsigned, L_CTL + 4 * C_CAP) = CH_D;
      if (lane < 28) {
        f32x4 s = {0.f, 0.f, 0.f, 0.f};
#pragma unroll
        for (int rb = 0; rb < 4; ++rb) s += __builtin_bit_cast(f32x4, ld128(c, lane * 16, OFF_CS + (rb * 1792 + 16 * g0) * 4));
        *LP(f32x4, L_CSUM + lane * 16) = s;
      }
      ctl_addc<C_CS>();
      flag_wait(c, FLR, idx, 38);
      if (lane == 0) *LP(volatile unsigned, L_CTL + 4 * C_CAP) = 1000u;
      DUMPL(20, 22);
      ENDPGM();
    }
    STL(62);
    STAMP(21);
    {
      unsigned t = 0;
      if (lane < 6) t = __builtin_amdgcn_raw_buffer_load_b32(c.rs, OFF_FLAGS + (lane * 256 + b) * 128, 0, SC1);
      TOUCH(t);
    }
#pragma unroll
    for (int i = 0; i < 4; ++i) *LP(v4u, L_RPK + i * 1024 + lane * 16) = v4u{0u, 0u, 0u, 0u};
    unsigned ra[4];
#pragma unroll
    for (int i = 0; i < 4; ++i) {
      const int it = min(i * 64 + lane, 209);
      const int z = it >= 200, he = z ? (it - 200) / 5 : it / 5, d4 = it - 5 * (it / 5);
      const int hd = z ? he : he / 20, d4g = 5 * hd + d4, e8 = z ? 0 : he + 8;
      ra[i] = KVSLOT * 10240 + ((d4g >> 2) * 3 + (e8 >> 4)) * 1024 + ((d4g & 3) * 16 + (e8 & 15)) * 16;
      PIN(ra[i]);
    }
    int kvo = OFF_KVP + (pj * 16 + pi) * KVP_STR + lane * 16;
    PIN(kvo);
    ctl_wait<1>(c, C_ARR1, 4);
    {
      f32x4 p[4][4];
#pragma unroll
      for (int i = 0; i < 4; ++i)
#pragma unroll
        for (int j = 0; j < 4; ++j) p[i][j] = *LP(f32x4, ra[i] + j * 10240);
      ctl_addc<C_RED>();
#pragma unroll
      for (int i = 0; i < 4; ++i) {
        const f32x4 v = (p[i][0] + p[i][1]) + (p[i][2] + p[i][3]);
        if (i < 3 || lane < 24) st128(c, __builtin_bit_cast(v4u, v), kvo, i * 1024);
      }
    }
    DRAIN();
    flag_set(c, FL1, pj * 16 + pi);
    STAMP(23);
    flag_wait(c, FL1, pq_ * 16 + n16, 16);
    ctl_addc<C_GO1>();
    const int mate = qi * 16 + n16;
    ctl_wait<1>(c, C_ARR2, 4);
    flag_set(c, FL2, pi * 16 + pj);
    STAMP(24);
    flag_wait(c, FL2, mate, 16);
    ctl_addc<C_GO2>();
    if (b == 0 && lane == 0) __hip_atomic_store((GAS u64*)(c.ws + OFF_EPOCH), c.ep, RLX_AGENT);
    ctl_wait<1>(c, C_ARR3, 4);
    flag_set(c, FL3, qi * 16 + qj);
    STAMP(25);
    flag_wait(c, FL3, mate, 16);
    ctl_addc<C_GO3>();
    ctl_wait<1>(c, C_ARR4A, 4);
    flag_set(c, FL4A, qi * 16 + qj);
    STAMP(26);
    flag_wait(c, FL4A, mate, 16);
    ctl_addc<C_GO4A>();
    STAMP(27);
    ctl_wait<1>(c, C_ARR4B, 4);
    flag_set(c, FL4B, qi * 16 + qj);
    STAMP(28);
    flag_wait(c, FL4B, mate, 16);
    ctl_addc<C_GO4B>();
    STAMP(29);
    DUMPL(21, 23, 24, 25, 26, 27, 28, 29);
    ENDPGM();
  }

  int xvoff;
  STL(0);
  const unsigned dn = L_LAND + 128 + lane * 4;
  c.dm = L_DONE + w * 256;
  const unsigned scr = L_SCR + w * 5120;

  {
    v4u xa[2][20];
    unsigned pf = 0u;
    {
      const unsigned oc = (sx * 16 + w * 4 + lane) * 128;
      if (lane < 4 && oc < clen) pf = *(const GAS unsigned*)(cpc + oc);
    }
    {
      const int t2 = tid < 240 ? tid : tid - 40;
      const int z = t2 >= 200, zi = t2 - 200, zg = zi / 10;
      const int it = z ? 200 + zi - 10 * zg : t2;
      const int he = z ? (it - 200) / 5 : it / 5, d4 = it - 5 * (it / 5);
      const int hd = z ? he : he / 20, e = he - 20 * hd, gi = e / 5, r5 = e - 5 * gi;
      const int R = z || r5 == 4, row = z ? 4 * zg + 1 : r5 == 4 ? 4 * gi : 4 * gi + r5;
      const int dd = d4 + hd;
      *LP(v2u, scr + 2048 + lane * 8) = v2u{(unsigned)it * 16, (unsigned)(L_RPK + (hd * 2 + R) * 1024 + ((dd & 3) * 16 + row) * 16 + (dd >> 2) * 8)};
      CFENCE();
    }
    const unsigned hv = n16 < 8 ? ONE2 : 0u;
    v4u ones = {hv, hv, hv, hv}, oneh = {hv ^ ONE2, hv ^ ONE2, hv ^ ONE2, hv ^ ONE2};
    PIN(ones); PIN(oneh);
    unsigned rawlane = (4 * g + ((lane >> 2) & 3)) * 160 + 8 * (lane & 3);
    const int oc = 32 * w, o4 = 128 + 5120 * (w & 1);
    LGKM0();
    __builtin_amdgcn_s_barrier();
    CFENCE();
    STAMP(30);
    {
      f32x4 acc[2][5];
      {
        unsigned br0 = w * 4096 + (n16 >> 3) * 1024 + (n16 & 7) * 128 + ((g ^ (n16 >> 1)) * 16), br1 = br0 ^ 64, wb = rawlane + 16384;
        PIN(br0); PIN(br1); PIN(wb);
        f32x4 sm, sq[2], ca;
        v4u wf[4], cf;
        v2u clo;
        unsigned la, cad;
        float q2;
        const auto addr = [&](int o) AI { unsigned r; asm volatile("v_add_u32 %0, %1, %2" : "=v"(r) : "s"(o), "v"(wb)); return r; };
        const auto stat = [&](auto qc, auto bc) AI {
          constexpr int q = decltype(qc)::v, m = q & 1, e = 2 * decltype(bc)::v + (q >> 2);
          if constexpr ((q & 2) == 0) mm<1, e == 0>(sq[m], xa[m][e], xa[m][e]);
          else mm<1, e == 0 && m == 0>(sm, m ? oneh : ones, xa[m][e]);
          SCHED();
        };
        ring_wait<0>(c);
        STAMP(31);
        SCHED();
        static_for<-4, 100>([&](auto ic) AI {
          constexpr int f = decltype(ic)::v, fn = f + 3;
          constexpr int s = f < 0 ? 0 : f / 5, t = f < 0 ? 0 : f % 5;
          constexpr int cb = f < 0 ? -1 : f / 10, i = (f + 10) % 10, nb = cb + 1;
          constexpr int off = fn < 0 ? 0 : bin(fn / 10) + ((fn % 10) / 5) * 5120 + 32 * (fn % 5);
          constexpr bool R = fn >= 0 && fn < 100, more = cb < 9, go = f >= 0, rel = cb != 7 && cb != 8;
          v2u lo;
          if constexpr (go) { mm<0, s == 0>(acc[0][t], wf[f % 4], xa[0][s]); SCHED(); }
          if constexpr (R) { lo = tr1(wb, off); SCHED(); }
          if constexpr (i == 4) { cad = addr(o4); SCHED(); }
          if constexpr (i == 8 && more) { cad = addr(oc); SCHED(); }
          if constexpr (i == 1 && more) bump<breg(nb) - breg(cb)>(br0);
          if constexpr (i == 2 && more) bump<breg(nb) - breg(cb)>(br1);
          if constexpr (i == 9 && go && rel) { release<3 * cb + 3>(c); SCHED(); }
          if constexpr (i == 6 && go && more) { ring_cmp<3 * nb>(la); SCHED(); }
          if constexpr (go) { mm<0, s == 0>(acc[1][t], wf[f % 4], xa[1][s]); SCHED(); }
          if constexpr (R) { const v2u hi = tr1(wb, off + 2560); wf[fn % 4] = v4u{lo.x, lo.y, hi.x, hi.y}; SCHED(); }
          if constexpr (i == 6 && go && more) { ring_br<3 * nb>(c); SCHED(); }
          if constexpr (go) {
            if constexpr (i == 1 || i == 4) { mm<1, f == 1>(ca, ones, cf); SCHED(); }
            else if constexpr (i == 7) { mm<1, false>(ca, oneh, cf); SCHED(); }
            else if constexpr (more) stat(IC<i - (i + 1) / 3>{}, IC<cb>{});
            else if constexpr (i < 4) stat(IC<i == 0 ? 0 : i == 2 ? 4 : 2>{}, IC<cb>{});
          }
          if constexpr (i == 0) xa[1][2 * cb + 1] = *LP(v4u, br1 + bin(cb) + 2048);
          if constexpr (i == 1) clo = tr1(cad, bin(cb) + 5120);
          if constexpr (i == 2) { const v2u hi = tr1(cad, bin(cb) + 7680); cf = v4u{clo.x, clo.y, hi.x, hi.y}; }
          if constexpr (i == 3 && more) la = *LP(unsigned, dn);
          if constexpr (i == 4) clo = tr1(cad, bin(cb));
          if constexpr (i == 5) { const v2u hi = tr1(cad, bin(cb) + 2560); cf = v4u{clo.x, clo.y, hi.x, hi.y}; }
          if constexpr (i == 6 && more) { xa[0][2 * nb] = *LP(v4u, br0 + bin(nb)); SCHED(); if constexpr (go) bump<breg(nb) - breg(cb)>(wb); }
          if constexpr (i == 7 && more) xa[1][2 * nb] = *LP(v4u, br0 + bin(nb) + 2048);
          if constexpr (i == 8 && more) clo = tr1(cad, bin(nb));
          if constexpr (i == 9 && more) { const v2u hi = tr1(cad, bin(nb) + 2560); cf = v4u{clo.x, clo.y, hi.x, hi.y}; }
          SCHED();
          if constexpr (i == 9 && go && more) stat(IC<7>{}, IC<cb>{});
          if constexpr (i == 9 && more) { xa[0][2 * nb + 1] = *LP(v4u, br1 + bin(nb)); SCHED(); }
          if constexpr (!more && i < 5) stat(IC<i == 0 ? 1 : i == 1 ? 3 : i == 2 ? 5 : i + 3>{}, IC<cb>{});
          if constexpr (f == 8) TOUCH(pf);
          if constexpr (f == 94) {
            const unsigned bw = scr + opaque(lane) * 16;
            *LP(f32x4, bw) = sq[0];
            *LP(f32x4, bw + 1024) = sq[1];
            CFENCE();
            SCHED();
          }
          if constexpr (f == 95) {
            const int l2 = opaque(lane), n2 = l2 & 15;
            q2 = *LP(float, scr + (l2 >> 5) * 1024 + ((n2 >> 2) * 16 + n2) * 16 + (n2 & 3) * 4);
            SCHED();
          }
          if constexpr (f == 97) {
            const int l2 = opaque(lane);
            PIN(sm);
            const float mean = sm[0] * (1.f / 640.f);
            const float a = __builtin_amdgcn_rsqf(fmaxf(q2 * (1.f / 640.f) - mean * mean, 0.f) + 1e-5f);
            *LP(float, scr + 4864 + l2 * 4) = (l2 & 16) ? -mean * a : a;
            CFENCE();
            SCHED();
          }
          if constexpr (f == 98) {
            const unsigned a = scr + 4096 + opaque(lane) * 4;
            asm volatile("s_nop 1" : "+v"(ca));
            *LP(float, a) = ca[0];
            ctl_addc<C_CSK>();
            SCHED();
          }
          if constexpr (f + 1 >= 0 && f + 1 < 100) {
            if constexpr (i == 5 && more) TOUCH(la);
            else if constexpr (i == 9) TOUCH(xa[1][2 * nb]);
            else TOUCH(wf[(f + 1) % 4]);
            SCHED();
          }
        });
        rawlane = wb - (16384 + breg(9));
        PIN(rawlane);
      }
      lane = opaque(lane); c.lane = lane; n16 = lane & 15; g = lane >> 4; xvoff = lane * 16;
      STAMP(4);
      f32x4 a4[2], b4[2];
#pragma unroll
      for (int m = 0; m < 2; ++m) { a4[m] = *LP(f32x4, scr + 4864 + m * 128 + g * 16); b4[m] = *LP(f32x4, scr + 4864 + m * 128 + 64 + g * 16); }
      SCHED();
      asm volatile("s_nop 3");
      SCHED();
      const float ks = n16 < 8 ? LOG2E : 1.f, kd = n16 < 8 ? PHID : 0.f;
#pragma unroll
      for (int m = 0; m < 2; ++m) {
        const f32x4 al = a4[m] * LOG2E;
        acc[m][0] = acc[m][0] * al + PHID; acc[m][1] = acc[m][1] * al + PHID;
        acc[m][2] = acc[m][2] * (a4[m] * ks) + kd;
        acc[m][3] = acc[m][3] * a4[m]; acc[m][4] = acc[m][4] * a4[m];
#pragma unroll
        for (int t = 0; t < 5; ++t) PIN(acc[m][t]);
      }
      ctl_wait(c, C_CSK, 4);
      STAMP(48);
      float cs[5];
      {
        const unsigned a = L_SCR + 4096 + n16 * 4;
#pragma unroll
        for (int t = 0; t < 4; ++t) cs[t] = *LP(float, a + t * 5120);
        cs[4] = *LP(float, a + 128) + *LP(float, a + 5248);
        cs[0] *= LOG2E; cs[1] *= LOG2E; cs[2] *= ks;
      }
      v4u pk[3], vv[3];
#pragma unroll
      for (int t = 4; t >= 0; --t) {
        const f32x4 t0 = b4[0] * cs[t] + acc[0][t], t1 = b4[1] * cs[t] + acc[1][t];
        if (t >= 2) vv[t - 2] = v4u{pk2(t0[0], t0[1]), pk2(t0[2], t0[3]), pk2(t1[0], t1[1]), pk2(t1[2], t1[3])};
        if (t <= 2) { const v2u p0 = phi4(t0), p1 = phi4(t1); pk[t] = v4u{p0.x, p0.y, p1.x, p1.y}; }
      }
      if (n16 < 8) vv[0] = ones;
      const unsigned kp = (KVSLOT + w) * 10240 + opaque(lane) * 16;
      const f32x4 z4 = {0.f, 0.f, 0.f, 0.f};
#pragma unroll
      for (int ta = 2; ta >= 0; --ta)
#pragma unroll
        for (int tb = 0; tb < 3; ++tb)
          if (ta != 0 || tb != 2) *LP(f32x4, kp + (ta * 3 + tb) * 1024) = mfma(pk[ta], vv[tb], z4);
      ctl_addc<C_ARR1>();
    }
    STAMP(3);
    v2u pq[2][3];
    {
      static_assert(region(CH_Q) == 0 && region(CH_B - 1) == 0, "q chunks");
      f32x4 aq[2][3], ca;
      v4u wf[4], cf[2];
      v2u clo;
      const unsigned wb = rawlane;
      unsigned c0 = rawlane + oc, c4 = rawlane + o4;
      PIN(c0); PIN(c4);
      float sa[2], sb[2];
      f32x4 cs[3];
      ring_wait<CH_B - 1>(c);
      STAMP(43);
      SCHED();
      constexpr auto qoff = [](int f) { return ((f % 20) / 2) * 5120 + (f & 1) * 80 + 32 * (f / 20); };
      constexpr auto coff = [](int j) { return (j / 3) * 10240 + (j % 3 == 1 ? 5120 : 0); };
      static_for<-3, 60>([&](auto ic) AI {
        constexpr int f = decltype(ic)::v, fn = f + 3, s = f < 0 ? 0 : f % 20, t = f < 0 ? 0 : f / 20, jn = f + 2;
        v2u lo;
        if constexpr (f >= 0) { mm<1, s == 0>(aq[0][t], wf[f % 4], xa[0][s]); SCHED(); }
        if constexpr (fn < 60) { lo = tr1(wb, qoff(fn)); SCHED(); }
        if constexpr (f >= 0) { mm<1, s == 0>(aq[1][t], wf[f % 4], xa[1][s]); SCHED(); }
        if constexpr (fn < 60) { const v2u hi = tr1(wb, qoff(fn) + 2560); wf[fn % 4] = v4u{lo.x, lo.y, hi.x, hi.y}; SCHED(); }
        if constexpr (f >= 0 && f < 15) { mm<1, f == 0>(ca, f % 3 == 2 ? oneh : ones, cf[f & 1]); SCHED(); }
        if constexpr (jn >= 0 && jn < 15) { cf[jn & 1] = tr2(jn % 3 == 2 ? c4 : c0, coff(jn), 2560); SCHED(); }
        if constexpr (f == 16) {
          const int l2 = opaque(lane);
          PIN(ca);
          if ((l2 & 16) == 0) *LP(float, L_SCR + 4608 + (l2 & 15) * 4 + w * 64 + (l2 >> 5) * 5120) = ca[0];
          ctl_addc<C_CSQ>();
#pragma unroll
          for (int m = 0; m < 2; ++m) { sa[m] = *LP(float, scr + 4864 + m * 128 + n16 * 4); sb[m] = *LP(float, scr + 4864 + m * 128 + 64 + n16 * 4); }
          SCHED();
        }
        if constexpr (f == 59) { release<CH_B>(c); SCHED(); }
      });
      STAMP(46);
      ctl_wait(c, C_CSQ, 4);
      STAMP(47);
      {
        const unsigned a = L_SCR + 4608 + g * 16;
        const unsigned h = g < 2 ? a + 224 : a + 5120 - 32;
        cs[0] = *LP(f32x4, a) + *LP(f32x4, a + 160);
        const f32x4 p1 = *LP(f32x4, a + 5120 + 64 - 32);
        cs[1] = *LP(f32x4, a + 64) + *LP(f32x4, h) + (g < 2 ? f32x4{0.f, 0.f, 0.f, 0.f} : p1);
        cs[2] = *LP(f32x4, a + 128) + *LP(f32x4, a + 5120 + 32) + *LP(f32x4, a + 5120 + 96);
      }
#pragma unroll
      for (int m = 0; m < 2; ++m) { sa[m] *= LOG2E; sb[m] *= LOG2E; }
#pragma unroll
      for (int t = 0; t < 3; ++t) {
#pragma unroll
        for (int m = 0; m < 2; ++m) {
          pq[m][t] = phi4(aq[m][t] * sa[m] + (cs[t] * sb[m] + PHID));
          if (t == 2 && g >= 2) pq[m][t] = v2u{0u, 0u};
        }
      }
    }
    v4u qb[2][2];
#pragma unroll
    for (int m = 0; m < 2; ++m)
#pragma unroll
      for (int hh = 0; hh < 2; ++hh) { qb[m][hh] = v4u{pq[m][hh].x, pq[m][hh].y, pq[m][hh + 1].x, pq[m][hh + 1].y}; PIN(qb[m][hh]); }
    v2u ga = *LP(v2u, scr + 2048 + opaque(lane) * 8);
    PIN(ga);
    const int gsrc = ga.x;
    const unsigned gdst = ga.y;
    const int mt0 = 8 * pi + 2 * w;
    int oa = OFF_O + (mt0 * 20 + pq_) * 1024 + xvoff;
    int ob = OFF_O + (mt0 * 20 + 16 + (pq_ >> 2)) * 1024 + (pq_ & 3) * 256 + g * 20480 + n16 * 16;
    unsigned fa = L_RPK + xvoff;
    PIN(oa); PIN(ob); PIN(fa);
    STAMP(5);
    ctl_wait(c, C_GO1, 1);
    STAMP(6);
    {
      v4u p[16];
#pragma unroll
      for (int i = 0; i < 16; ++i) p[i] = ld128(c, gsrc, OFF_KVP + (pq_ * 16 + i) * KVP_STR);
      f32x4 s = __builtin_bit_cast(f32x4, p[0]);
#pragma unroll
      for (int i = 1; i < 16; ++i) s += __builtin_bit_cast(f32x4, p[i]);
      *LP(v2u, gdst) = v2u{pk2(s[0], s[1]), pk2(s[2], s[3])};
    }
    ctl_addc<C_BAR1>();
    ctl_wait(c, C_BAR1, 4);
    STAMP(7);
    v4u ka_[4];
#pragma unroll
    for (int i = 0; i < 4; ++i) ka_[i] = *LP(v4u, fa + (i ^ 1) * 1024);
    f32x4 on[2][2][2];
    const f32x4 z4 = {0.f, 0.f, 0.f, 0.f};
#pragma unroll
    for (int i = 0; i < 4; ++i)
#pragma unroll
      for (int m = 0; m < 2; ++m) on[m][i >> 1][(i & 1) ^ 1] = mfma(ka_[i], qb[m][i >> 1], z4);
    unsigned tl[2];
#pragma unroll
    for (int m = 0; m < 2; ++m) {
      const float r0 = __builtin_amdgcn_rcpf(on[m][0][1][1] + 1e-6f), r1 = __builtin_amdgcn_rcpf(on[m][1][1][1] + 1e-6f);
      const v4u o = {pk2(on[m][0][0][0] * r0, on[m][0][0][1] * r0), pk2(on[m][0][0][2] * r0, on[m][0][0][3] * r0),
                     pk2(on[m][0][1][0] * r0, on[m][1][0][0] * r1), pk2(on[m][1][0][1] * r1, on[m][1][0][2] * r1)};
      tl[m] = pk2(on[m][1][0][3] * r1, on[m][1][1][0] * r1);
      st128(c, o, oa, m * 20480);
    }
    const v2u t0 = swap16(tl[0], tl[1]);
    const v2u u0 = swap32(t0.x, t0.x), u1 = swap32(t0.y, t0.y);
    if (lane < 32) st128(c, v4u{u0.x, u1.x, u0.y, u1.y}, ob, 0);
  }
  STAMP(8);
  DRAIN();
  ctl_addc<C_ARR2>();
  const int kb = w & 1;
  const int mtB = 4 * hp + w;
  unsigned hq[10];

  {
    f32x4 acc[2][5];
    {
      const char* xb0 = x + (size_t)(16 * mtB * 1280 + cc * 160);
      const unsigned rowoff = n16 * 1280 + g * 16;
      const v4u xr0 = *(const GAS v4u*)(xb0 + rowoff), xr1 = *(const GAS v4u*)(xb0 + rowoff + 64);
      const v2u xr2 = *(const GAS v2u*)(xb0 + (rowoff - g * 8 + 128));
      acc[0][0] = unpk4(xr0.x, xr0.y); acc[0][1] = unpk4(xr0.z, xr0.w);
      acc[0][2] = unpk4(xr1.x, xr1.y); acc[0][3] = unpk4(xr1.z, xr1.w);
      acc[0][4] = unpk4(xr2.x, xr2.y);
#pragma unroll
      for (int t = 0; t < 5; ++t) PIN(acc[0][t]);
    }
    const int sb = 5 * (cc >> 1) + 3 * (cc & 1);
    int ha = OFF_H + (mtB * 20 + sb) * 1024 + xvoff;
    int hb = OFF_H + (mtB * 20 + 5 * (cc >> 1) + 2) * 1024 + (cc & 1) * 512 + g * 128 + n16 * 16;
    PIN(ha); PIN(hb);
    ctl_wait(c, C_GO2, 1);
    STAMP(9);
    v4u xb[2][10];
    const int xs = OFF_O + mtB * 20480 + kb * 10240, xm = kb ? -20480 : 20480;
    xload<10, 0, 0, PRE_B, 10>(c, xb, xvoff, xs, xm, rotB);
    gemm_pass<5, 10, CH_B, 5, 10, 2>(c, dn, xvoff + kb * 5120, acc, xb, [&](auto ic) AI {
      constexpr int f = decltype(ic)::v;
      if constexpr (f >= 0 && f % 5 == 1 && f / 5 + PRE_B < 10) xload<10, 0, f / 5 + PRE_B, 1, 10>(c, xb, xvoff, xs, xm, rotB);
    });
    STAMP(1);
    pair_reduce(c, w, C_XB0, acc, hq);
    STAMP(49);
    st128(c, v4u{hq[0], hq[1], hq[2], hq[3]}, ha, 0);
    st128(c, v4u{hq[4], hq[5], hq[6], hq[7]}, ha, 1024);
    const v2u ta = swap16(hq[8], hq[8]), tb = swap16(hq[9], hq[9]);
    if ((g & 1) == 0) st128(c, v4u{ta.x, tb.x, ta.y, tb.y}, hb, 0);
    float s1 = 0.f, s2 = 0.f;
#pragma unroll
    for (int i = 0; i < 10; ++i) { s1 = dot2(hq[i], ONE2, s1); s2 = dot2(hq[i], hq[i], s2); }
    s1 = sum_g(s1); s2 = sum_g(s2);
    if (g == 0) st64(c, v2u{asu(s1), asu(s2)}, n16 * 8, OFF_HS + mtB * 1024 + cc * 128);
  }
  STAMP(10);
  DRAIN();
  ctl_addc<C_ARR3>();

  {
    const int mt0 = 8 * qi + 2 * w;
    ctl_wait(c, C_CS, 1);
    f32x4 acc[2][4];
#pragma unroll
    for (int t = 0; t < 4; ++t) { acc[0][t] = *LP(f32x4, L_CSUM + t * 64 + g * 16); PIN(acc[0][t]); }
    ctl_wait(c, C_GO3, 1);
    STAMP(11);
    v2u st[2][2];
#pragma unroll
    for (int m = 0; m < 2; ++m)
#pragma unroll
      for (int i = 0; i < 2; ++i) st[m][i] = ld64(c, lane * 8, OFF_HS + (mt0 + m) * 1024 + i * 512);
    v4u xh[2][20];
    xload<20, 0, 0, PRE_C, 20>(c, xh, xvoff, OFF_H + mt0 * 20480, 20480, rotC);
    float mneg[2], s2[2];
#pragma unroll
    for (int m = 0; m < 2; ++m) {
      mneg[m] = sum_g(asf(st[m][0].x) + asf(st[m][1].x)) * (-1.f / 640.f);
      s2[m] = asf(st[m][0].y) + asf(st[m][1].y);
    }
#pragma unroll
    for (int t = 0; t < 4; ++t) { acc[1][t] = acc[0][t] * mneg[1]; acc[0][t] = acc[0][t] * mneg[0]; PIN(acc[0][t]); PIN(acc[1][t]); }
    float sd[2], c2[2];
    gemm_pass<4, 20, CH_C1, 10, 20, 0>(c, dn, xvoff, acc, xh, [&](auto ic) AI {
      constexpr int f = decltype(ic)::v;
      if constexpr (f >= 0 && f % 4 == 1 && f / 4 + PRE_C < 20) xload<20, 0, f / 4 + PRE_C, 1, 20>(c, xh, xvoff, OFF_H + mt0 * 20480, 20480, rotC);
      if constexpr (f == 2 || f == 6) {
        constexpr int m = f == 6;
        const float var = fmaxf(sum_g(s2[m]) * (1.f / 640.f) - mneg[m] * mneg[m], 0.f) + 1e-5f;
        const float rs = __builtin_amdgcn_rsqf(var);
        sd[m] = var * rs;
        c2[m] = -1.4426950408889634f * rs;
        PIN(sd[m]); PIN(c2[m]);
      }
    });
    STAMP(12);
    const auto silu2 = [&](int m, const f32x4& a) AI {
      float y[4];
#pragma unroll
      for (int i = 0; i < 4; ++i) {
        const float e = __builtin_amdgcn_exp2f(a[i] * c2[m]);
        y[i] = a[i] * __builtin_amdgcn_rcpf(fmaf(e, sd[m], sd[m]));
      }
      return v2u{pk2(y[0], y[1]), pk2(y[2], y[3])};
    };
    const int fb = OFF_F + mt0 * 55296;
    f32x4 ac2[2][3];
#pragma unroll
    for (int t = 0; t < 3; ++t) ac2[0][t] = *LP(f32x4, L_CSUM + (4 + t) * 64 + g * 16);
#pragma unroll
    for (int m = 0; m < 2; ++m)
#pragma unroll
      for (int v = 0; v < 2; ++v) {
        const v2u a = silu2(m, acc[m][2 * v]), d = silu2(m, acc[m][2 * v + 1]);
        st128(c, v4u{a.x, a.y, d.x, d.y}, xvoff, fb + m * 55296 + (2 * qj + v) * 1024);
      }
#pragma unroll
    for (int t = 0; t < 3; ++t) { ac2[1][t] = ac2[0][t] * mneg[1]; ac2[0][t] = ac2[0][t] * mneg[0]; PIN(ac2[0][t]); PIN(ac2[1][t]); }
    STAMP(2);
    gemm_pass<3, 20, CH_C2, 10, 20, 0>(c, dn, xvoff, ac2, xh, [&](auto ic) AI {
      if constexpr (decltype(ic)::v == 18) { DRAIN(); ctl_addc<C_ARR4A>(); }
    });
    STAMP(13);
#pragma unroll
    for (int m = 0; m < 2; ++m) {
      const v2u a = silu2(m, ac2[m][0]), d = silu2(m, ac2[m][1]);
      st128(c, v4u{a.x, a.y, d.x, d.y}, xvoff, fb + m * 55296 + (32 + qj) * 1024);
    }
    if ((qj & 3) != 3) {
      const v2u a = silu2(0, ac2[0][2]), d = silu2(1, ac2[1][2]);
      const v2u ta = swap16(a.x, d.x), tb = swap16(a.y, d.y);
      const int l = 3 * (qj >> 2) + (qj & 3);
      st128(c, v4u{ta.x, tb.x, ta.y, tb.y}, (g & 1) * 55296 + (g >> 1) * 256 + n16 * 16, fb + (48 + (l >> 1)) * 1024 + (l & 1) * 512);
    }
  }
  STAMP(14);

  {
    const int xs = OFF_F + mtB * 55296, xm = kb ? -55296 : 55296;
    const bool early = ctl_read(C_GO4A) != 0u;
    CFENCE();
    if (!early) { DRAIN(); ctl_addc<C_ARR4B>(); ctl_wait(c, C_GO4A, 1); }
    v4u xf[2][20];
    xload<20, 0, 0, PRE_D, 16>(c, xf, xvoff, xs + kb * 16384, xm, rotD1);
    f32x4 acc[2][5];
#pragma unroll
    for (int t = 0; t < 5; ++t) { acc[0][t] = unpk4(hq[2 * t], hq[2 * t + 1]); PIN(acc[0][t]); }
    if (early) { asm volatile("s_waitcnt vmcnt(%0)" :: "i"(2 * PRE_D) : "memory"); ctl_addc<C_ARR4B>(); }
    STAMP(16);
    gemm_pass<5, 27, CH_D, 5, 20, 2>(c, dn, xvoff + kb * 5120, acc, xf, [&](auto ic) AI {
      constexpr int f = decltype(ic)::v, sn = f / 5 + PRE_D;
      if constexpr (f >= 0 && f % 5 == 1 && sn < 27) {
        if constexpr (sn == 16) {
          STAMP(17);
          ctl_wait(c, C_GO4B, 1);
          STAMP(18);
        }
        if constexpr (sn < 16) xload<20, 0, sn, 1, 16>(c, xf, xvoff, xs + kb * 16384, xm, rotD1);
        else xload<20, 16, sn - 16, 1, 11>(c, xf, xvoff, xs + 32768 + kb * 11264, xm, rotD2);
      }
    });
    STAMP(19);
    unsigned y[10];
    pair_reduce(c, w, C_XD0, acc, y);
    const int l2 = opaque(lane);
    char* ob = out + (size_t)(16 * mtB * 1280 + cc * 160);
    const unsigned ro = (l2 & 15) * 1280 + (l2 >> 4) * 16;
    *(GAS v4u*)(ob + ro) = v4u{y[0], y[1], y[2], y[3]};
    *(GAS v4u*)(ob + ro + 64) = v4u{y[4], y[5], y[6], y[7]};
    *(GAS v2u*)(ob + (ro - (l2 >> 4) * 8 + 128)) = v2u{y[8], y[9]};
  }
  STAMP(15);
}

extern "C" void kernel_launch(void* const* d_in, const int* in_sizes, int n_in, void* d_out, int out_size, void* d_ws, size_t ws_size,
                              hipStream_t stream) {
  (void)hipFuncSetAttribute((const void*)mk_kernel, hipFuncAttributeMaxDynamicSharedMemorySize, LDS_BYTES);
  mk_kernel<<<256, 512, LDS_BYTES, stream>>>((const char*)d_in[0], (const char*)d_in[1], (const char*)d_in[2], (const char*)d_in[4],
                                             (const char*)d_in[5], (char*)d_out, (char*)d_ws);
}
```

```cpp
#include <hip/hip_runtime.h>
#include <stdint.h>

typedef unsigned v4u __attribute__((ext_vector_type(4)));
typedef unsigned v2u __attribute__((ext_vector_type(2)));
typedef short v4s __attribute__((ext_vector_type(4)));
typedef float f32x4 __attribute__((ext_vector_type(4)));
typedef float f32x2 __attribute__((ext_vector_type(2)));
typedef __bf16 bf16x2 __attribute__((ext_vector_type(2)));
typedef __bf16 bf16x8 __attribute__((ext_vector_type(8)));
typedef unsigned long long u64;
typedef __amdgpu_buffer_rsrc_t rsrc_t;
#define LAS __attribute__((address_space(3)))
#define GAS __attribute__((address_space(1)))
#define DI __device__ __forceinline__
#define AI __attribute__((always_inline))
#define RLX_AGENT __ATOMIC_RELAXED, __HIP_MEMORY_SCOPE_AGENT
#define SCHED() __builtin_amdgcn_sched_barrier(0)
#define CFENCE() asm volatile("" ::: "memory")
#define DRAIN() asm volatile("s_waitcnt vmcnt(0)" ::: "memory")
#define VMWAIT(n) asm volatile("s_waitcnt vmcnt(" #n ")" ::: "memory")
#define LGKM0() asm volatile("s_waitcnt lgkmcnt(0)" ::: "memory")
#define ENDPGM() asm volatile("s_endpgm" ::: "memory")
#define PIN(v) asm volatile("" : "+v"(v))
#define LP(T, a) ((LAS T*)(uintptr_t)(unsigned)(a))
constexpr int SC1 = 16;
constexpr unsigned ONE2 = 0x3f803f80u;

#define DEP_A 12
#define DEP_P 10
#define LPRIO 0
#define REL_B 5
#define REL_C 3
#define F4A 18
#define RPK_F 12
#define RPK_S 35
#define RPK_PUB 2
#define PQX 0
#define CMAP 1
#define UMAX 216
#define CAPL 1
#define TCH 7
#define TCH_T 0
#define LATE_L 1
#define LATE_S 35
#define QUIET3 2
#define WGF 1
#define Q3N 45
#define BMAP 0
#define ROTS 0
#define PW 12
#define HOLD 0
#define QUIET 1
#define PSL 0
#define PD 0
#define XPRE 1
#define ROTA 1
#define STR_(x) #x
#define STR(x) STR_(x)

#define ROT 1
#define PRE_B 6
#define PRE_C 8
#define PRE_D 8

constexpr int OFF_EPOCH = 0;
constexpr int OFF_FLAGS = 4096;
constexpr int OFF_CS = 655360;
constexpr int OFF_HS = 819200;
constexpr int OFF_KVP = 1 << 20;
constexpr int KVP_STR = 1920;
constexpr int OFF_WB = 2 << 20;
constexpr int OFF_WC = OFF_WB + 819200;
constexpr int OFF_WD = OFF_WC + 2293760;
constexpr int OFF_O = OFF_WD + 2211840;
constexpr int OFF_H = OFF_O + 2621440;
constexpr int OFF_F = OFF_H + 2621440;
constexpr int WS_BYTES = OFF_F + 7077888;
constexpr int OFF_PROF = 32 << 20;
enum { FLR = 0, FL1 = 256, FL2 = 512, FL3 = 1536, FL4A = 2560, FL4B = 3584, FL_N = 4608 };
constexpr unsigned EPOCH_STEP = 0x9E3779B6u;

constexpr int RCH = 13;
constexpr int L_SCR = RCH * 10240;
constexpr int L_RPK = L_SCR + 20480;
constexpr int L_CSUM = L_RPK + 4096;
constexpr int L_DONE = L_CSUM + 512;
constexpr int L_LAND = L_DONE + 1024;
constexpr int L_FLG = L_LAND + 512;
constexpr int L_STA = L_FLG + 1024;
constexpr int L_CK = L_STA + 1024;
constexpr int L_CQ = L_CK + 1024;
constexpr int L_CTL = L_CQ + 512;
constexpr int LDS_BYTES = L_CTL + 128;
static_assert(LDS_BYTES <= 163840, "lds");
enum { C_CAP = 0, C_CSK, C_CSQ, C_PUB, C_CS, C_ARR1, C_GO1, C_XB0, C_XB1, C_XD0, C_XD1, C_ARR2, C_ARR3, C_ARR4A, C_GO4A, C_ARR4B, C_GO4B };

constexpr int CH_Q = 30, CH_B = 35, CH_C1 = 45, CH_C2 = 53, CH_D = 59, CH_END = 86;
constexpr int BLK = 26624;
static_assert(5 * BLK == RCH * 10240, "ring");
constexpr int needp(int ch) { return ch + 1; }
constexpr int slot(int ch) { return (ch - CH_Q) % RCH; }
constexpr int KVSLOT = 9;

struct Ctx {
  rsrc_t rs, rt;
  unsigned dm;
  GAS char* ws;
  unsigned ep;
  int lane;
  unsigned budget;
};

DI f32x4 mfma(v4u a, v4u b, f32x4 c) {
  return __builtin_amdgcn_mfma_f32_16x16x32_bf16(__builtin_bit_cast(bf16x8, a), __builtin_bit_cast(bf16x8, b), c, 0, 0, 0);
}
DI float asf(unsigned u) { return __builtin_bit_cast(float, u); }
DI unsigned asu(float f) { return __builtin_bit_cast(unsigned, f); }
DI unsigned pk2(float a, float b) { f32x2 f = {a, b}; return __builtin_bit_cast(unsigned, __builtin_convertvector(f, bf16x2)); }
DI float bflo(unsigned u) { return asf(u << 16); }
DI float bfhi(unsigned u) { return asf(u & 0xffff0000u); }
DI f32x4 unpk4(unsigned a, unsigned b) { return f32x4{bflo(a), bfhi(a), bflo(b), bfhi(b)}; }
DI float dot2(unsigned a, unsigned b, float c) {
  return __builtin_amdgcn_fdot2_f32_bf16(__builtin_bit_cast(bf16x2, a), __builtin_bit_cast(bf16x2, b), c, false);
}
DI f32x4 add4(f32x4 a, f32x4 b) {
  f32x4 r;
#pragma unroll
  for (int i = 0; i < 4; ++i) asm("v_add_f32 %0, %1, %2" : "=v"(r[i]) : "v"(a[i]), "v"(b[i]));
  return r;
}
DI v4u ld128(const Ctx& c, int voff, int soff) { return __builtin_amdgcn_raw_buffer_load_b128(c.rs, voff, soff, SC1); }
DI v2u ld64(const Ctx& c, int voff, int soff) { return __builtin_amdgcn_raw_buffer_load_b64(c.rs, voff, soff, SC1); }
DI void st128(const Ctx& c, v4u v, int voff, int soff) { __builtin_amdgcn_raw_buffer_store_b128(v, c.rs, voff + soff, 0, SC1); }
DI void st64(const Ctx& c, v2u v, int voff, int soff) { __builtin_amdgcn_raw_buffer_store_b64(v, c.rs, voff, soff, SC1); }
DI void st32(const Ctx& c, unsigned v, int voff, int soff) { __builtin_amdgcn_raw_buffer_store_b32(v, c.rs, voff, soff, SC1); }
DI u64 now() { return __builtin_amdgcn_s_memrealtime(); }

DI void ctl_add(int i) {
  asm volatile("v_mov_b32 v254, %0\n\tv_mov_b32 v255, %1\n\ts_mov_b64 exec, 1\n\tds_add_u32 v254, v255\n\ts_mov_b64 exec, -1"
               :: "s"(L_CTL + 4 * i), "i"(L_CTL) : "memory", "v254", "v255");
}
template <int I> DI void ctl_addc() {
  asm volatile("v_mov_b32 v254, %0\n\ts_mov_b64 exec, 1\n\tds_add_u32 v254, v254 offset:%1\n\ts_mov_b64 exec, -1"
               :: "i"(L_CTL), "i"(4 * I) : "memory", "v254");
}
template <int SLP = 0> DI void ctl_wait(Ctx& c, int i, int n) {
  unsigned t;
  asm volatile(
      "v_mov_b32 v254, %2\n"
      ".Lw%=:\n\t"
      ".if %4\n\ts_sleep %4\n\t.endif\n\t"
      "ds_read_b32 v255, v254\n\t"
      "s_sub_u32 %0, %0, 1\n\t"
      "s_max_i32 %0, %0, 0\n\t"
      "s_waitcnt lgkmcnt(0)\n\t"
      "v_readfirstlane_b32 %1, v255\n\t"
      "s_cmp_lt_u32 %1, %3\n\t"
      "s_cselect_b32 %1, %0, 0\n\t"
      "s_cmp_lg_u32 %1, 0\n\t"
      "s_cbranch_scc1 .Lw%="
      : "+{s71}"(c.budget), "=&s"(t) : "s"(L_CTL + 4 * i), "s"(n * L_CTL), "i"(SLP) : "memory", "scc", "v254", "v255");
}
DI unsigned ctl_read(int i) { return __builtin_amdgcn_readfirstlane(*LP(volatile unsigned, L_CTL + 4 * i)); }
DI void flag_set(const Ctx& c, int idx) {
  asm volatile("v_mov_b32 v255, %1\n\ts_mov_b64 exec, 1\n\tbuffer_store_dword v255, off, %0, %2 sc1\n\ts_mov_b64 exec, -1"
               :: "s"(c.rs), "s"(c.ep), "s"(OFF_FLAGS + idx * 128) : "memory", "v255");
}
template <int I> DI bool arrive(const Ctx& c, int idx) {
  unsigned n;
  asm volatile("v_mov_b32 v254, %1\n\ts_mov_b64 exec, 1\n\tds_add_rtn_u32 v255, v254, v254 offset:%2\n\ts_mov_b64 exec, -1\n\ts_waitcnt lgkmcnt(0)\n\tv_readfirstlane_b32 %0, v255"
               : "=s"(n) : "i"(L_CTL), "i"(4 * I) : "memory", "v254", "v255");
  if (n == 3 * L_CTL) flag_set(c, idx);
  return n == 3 * L_CTL;
}
DI bool flag_test(const Ctx& c, int idx, int nl) {
  unsigned v = c.ep;
  if (c.lane < nl) v = __hip_atomic_load((GAS unsigned*)(c.ws + OFF_FLAGS + idx * 128), RLX_AGENT);
  const bool ok = !__any(v != c.ep);
  CFENCE();
  return ok;
}
DI void flag_wait(const Ctx& c, int off, int nl) {
  unsigned n = 1u << 17;
  while (!flag_test(c, off, nl) && --n != 0) {}
}
DI void flag_copy(Ctx& c, int row, int idx0, u64 mask) {
  asm volatile(
      "s_mov_b32 m0, %3\n\t"
      "v_lshl_add_u32 v254, %5, 2, %3\n\t"
      "s_mov_b64 exec, %6\n"
      ".Lf%=:\n\t"
      ".if " STR(PSL) "\n\ts_sleep " STR(PSL) "\n\t.endif\n\t"
      "buffer_load_dword %7, %1, %4 offen sc1 lds\n\t"
      "s_waitcnt vmcnt(" STR(PD) ")\n\t"
      "ds_read_b32 v255, v254\n\t"
      "s_sub_u32 %0, %0, 64\n\t"
      "s_cbranch_scc1 .Lg%=\n\t"
      "s_waitcnt lgkmcnt(0)\n\t"
      "v_cmp_ne_u32 vcc, %2, v255\n\t"
      "s_and_b64 exec, exec, vcc\n\t"
      "s_cbranch_scc1 .Lf%=\n\t"
      "s_branch .Le%=\n"
      ".Lg%=:\n\t"
      "s_mov_b32 %0, 0\n"
      ".Le%=:\n\t"
      "s_mov_b64 exec, -1\n\t"
      "s_waitcnt vmcnt(0) lgkmcnt(0)"
      : "+s"(c.budget) : "s"(c.rs), "s"(c.ep), "s"(L_FLG + row * 256), "s"(OFF_FLAGS + idx0 * 128), "v"(c.lane), "s"(mask), "v"(c.lane * 128)
      : "memory", "scc", "vcc", "v254", "v255");
}
template <int ROW> DI void flag_seen(Ctx& c, unsigned dn, u64 mask) {
  asm volatile(
      ".Lf%=:\n\t"
      "ds_read_b32 v255, %1 offset:%4\n\t"
      "s_sub_u32 %0, %0, 1\n\t"
      "s_cbranch_scc1 .Lg%=\n\t"
      "s_waitcnt lgkmcnt(0)\n\t"
      "v_cmp_ne_u32 vcc, %2, v255\n\t"
      "s_and_b64 vcc, vcc, %3\n\t"
      "s_cbranch_scc1 .Lf%=\n\t"
      ".subsection 1\n"
      ".Lg%=:\n\t"
      "s_mov_b32 %0, 0\n\t"
      "s_waitcnt lgkmcnt(0)\n\t"
      "s_branch .Le%=\n\t"
      ".subsection 0\n"
      ".Le%=:"
      : "+{s71}"(c.budget) : "v"(dn), "s"(c.ep), "s"(mask), "i"(L_FLG + ROW * 256 - L_LAND - 128) : "memory", "scc", "vcc", "v255");
}
DI void done_wait(Ctx& c, unsigned n) {
  unsigned k = 0;
  while (__any(*LP(volatile unsigned, L_DONE + c.lane * 16) < n) && ++k < (1u << 18)) __builtin_amdgcn_s_sleep(2);
  CFENCE();
}
DI void touch(const char* p, int bytes, int off, int n, int pitch) {
  const rsrc_t r = __builtin_amdgcn_make_buffer_rsrc((void*)p, 0, bytes, 0x00020000);
  asm volatile(".Lt%=:\n\t"
               "s_buffer_load_dwordx2 s[98:99], %2, %0 offset:60\n\t"
               "s_add_u32 %0, %0, %3\n\t"
               "s_sub_u32 %1, %1, 1\n\t"
               "s_cmp_gt_i32 %1, 0\n\t"
               "s_cbranch_scc1 .Lt%=\n\t"
               "s_waitcnt lgkmcnt(0)"
               : "+s"(off), "+s"(n) : "s"(r), "s"(pitch) : "memory", "scc", "s98", "s99");
}
DI void land_wait(const Ctx& c, unsigned n) {
  unsigned k = 0;
  while (__any(*LP(volatile unsigned, L_LAND + 128 + c.lane * 4) < n) && ++k < (1u << 16)) __builtin_amdgcn_s_sleep(4);
  CFENCE();
}
#define STAMP(i) do {} while (0)
#define STL(i) do {} while (0)
#define DUMPL(...) do {} while (0)

DI v2u swap16(unsigned a, unsigned b) { return __builtin_amdgcn_permlane16_swap(a, b, false, false); }
DI v2u swap32(unsigned a, unsigned b) { return __builtin_amdgcn_permlane32_swap(a, b, false, false); }
DI float sum_g(float v) {
  v2u r = swap16(asu(v), asu(v));
  v = asf(r.x) + asf(r.y);
  r = swap32(asu(v), asu(v));
  return asf(r.x) + asf(r.y);
}
DI v2u tr1(unsigned a, int off) { return __builtin_bit_cast(v2u, __builtin_amdgcn_ds_read_tr16_b64_v4i16(LP(v4s, a + off))); }
DI v4u tr2(unsigned a, int off, int hioff) {
  const v2u lo = tr1(a, off), hi = tr1(a, off + hioff);
  return v4u{lo.x, lo.y, hi.x, hi.y};
}

DI int gam0(int j) { return 27 * (j >> 2) + 7 * (j & 3); }

template <int CH> DI void ring_wait(Ctx& c) {
  asm volatile("s_movk_i32 s70, %1\n\ts_call_b64 s[72:73], .Lmk_ring"
               : "+{s71}"(c.budget) : "i"(needp(CH)) : "memory", "scc", "vcc", "s70", "s72", "s73", "v254", "v255");
}
template <int CH> DI void ring_cmp(unsigned la) { asm volatile("v_cmp_gt_u32 vcc, %1, %0" :: "v"(la), "i"(needp(CH)) : "vcc"); }
template <int CH> DI void ring_br(Ctx& c) {
  asm volatile("s_cbranch_vccnz .Lrs%=\n\t.subsection 1\n.Lrs%=:\n\ts_movk_i32 s70, %1\n\ts_call_b64 s[72:73], .Lmk_ring\n\ts_branch .Lrb%=\n\t.subsection 0\n.Lrb%=:"
               : "+{s71}"(c.budget) : "i"(needp(CH)) : "memory", "scc", "vcc", "s70", "s72", "s73", "v254", "v255");
}
#define TOUCH(x) asm volatile("" :: "v"(x))
template <int N> DI void release(const Ctx& c) {
  asm volatile("s_mov_b32 m0, %1\n\ts_movk_i32 s70, %2\n\tbuffer_load_dword off, %0, s70 lds" :: "s"(c.rt), "s"(c.dm), "i"(4 * N) : "memory", "s70");
}
constexpr int upto(int ch) {
  if (ch == CH_Q) return CH_B - 1;
  if (ch == CH_B) return CH_C1 - 1;
  if (ch == CH_C1) return CH_C2 - 1;
  if (ch == CH_C2) return CH_D - 2;
  if (ch == CH_D - 1) return ch;
  if (ch == CH_D) return CH_D + 6;
  if (ch == CH_D + 7) return CH_D + 12;
  if (ch >= CH_D + 13 && (ch - CH_D - 13) % 2 == 0) return ch + 1;
  return -1;
}
constexpr bool released(int n) {
  if (n + RCH > CH_END + 1) return false;
  if (n <= CH_C1) return n == CH_B + REL_B;
  if (n <= CH_D) return n >= CH_C1 + REL_C;
  return (n - CH_B) % 2 == 0;
}
template <int I> struct IC { static constexpr int v = I; };
template <int B, int E, class Fn> DI void static_for(Fn&& fn) {
  if constexpr (B < E) { fn(IC<B>{}); static_for<B + 1, E>(fn); }
}
template <int D> DI void bump(unsigned& b) { if constexpr (D != 0) asm volatile("v_add_u32 %0, %1, %0" : "+v"(b) : "i"(D)); }
constexpr int breg(int b) { return ((b % 5) / 2) * 2 * BLK; }
constexpr int bin(int b) { return ((b % 5) % 2) * BLK; }
constexpr int region(int ch) { return (slot(ch) / 6) * 61440; }
constexpr int inreg(int ch) { return (slot(ch) % 6) * 10240; }

template <int TL, bool Z> DI void mm(f32x4& acc, const v4u& wf, const v4u& x) {
  if constexpr (Z && TL) asm volatile("v_mfma_f32_16x16x32_bf16 %0, %1, %2, 0" : "=&v"(acc) : "v"(wf), "v"(x));
  else if constexpr (Z) asm volatile("v_mfma_f32_16x16x32_bf16 %0, %2, %1, 0" : "=&v"(acc) : "v"(wf), "v"(x));
  else if constexpr (TL) asm volatile("v_mfma_f32_16x16x32_bf16 %0, %1, %2, %0" : "+v"(acc) : "v"(wf), "v"(x));
  else asm volatile("v_mfma_f32_16x16x32_bf16 %0, %2, %1, %0" : "+v"(acc) : "v"(wf), "v"(x));
}
template <int NT, int NKS, int CH0, int FPC, int XW, int ZM, bool RAW, class Hook>
DI void gemm_pass(Ctx& c, unsigned dn, unsigned wb, f32x4 (&acc)[2][NT], v4u (&x)[2][XW], Hook&& hook) {
  constexpr int LA = 3, WBN = LA + 1, NF = NKS * NT;
  v4u wf[WBN];
  unsigned la;
  bump<region(CH0)>(wb);
  SCHED();
  static_for<-LA, NF>([&](auto ic) AI {
    constexpr int f = decltype(ic)::v, fn = f + LA;
    constexpr int s = f < 0 ? 0 : f / NT, t = f < 0 ? 0 : f % NT;
    constexpr int ch = CH0 + fn / FPC, off = inreg(ch) + (fn % FPC) * (RAW ? 32 : 1024);
    v2u lo;
    constexpr auto chk = [](int i) { return i > 0 && i < NF && i % FPC == 0 && upto(CH0 + i / FPC) >= 0; };
    constexpr bool rel = f >= 0 && (f + 1) % FPC == 0 && released(CH0 + (f + 1) / FPC);
    if constexpr (f >= 0) { mm<1, (ZM & 1) && s == 0>(acc[0][t], wf[f % WBN], x[0][s % XW]); SCHED(); }
    if constexpr (fn < NF) {
      if constexpr (fn == 0) ring_wait<upto(ch)>(c);
      if constexpr (chk(fn)) ring_br<upto(ch)>(c);
      if constexpr (RAW) lo = tr1(wb, off);
      else wf[fn % WBN] = *LP(v4u, wb + off);
      SCHED();
    }
    if constexpr (f >= 0) { mm<1, (ZM & 2) && s == 0>(acc[1][t], wf[f % WBN], x[1][s % XW]); SCHED(); }
    if constexpr (RAW && fn < NF) { const v2u hi = tr1(wb, off + 2560); wf[fn % WBN] = v4u{lo.x, lo.y, hi.x, hi.y}; SCHED(); }
    if constexpr (fn + 1 < NF && (fn + 1) % FPC == 0) bump<region(ch + 1) - region(ch)>(wb);
    if constexpr (rel) release<CH0 + (f + 1) / FPC>(c);
    if constexpr (chk(fn + 4)) la = *LP(unsigned, dn);
    hook(ic);
    SCHED();
    if constexpr (chk(fn + 1)) { ring_cmp<upto(CH0 + (fn + 1) / FPC)>(la); SCHED(); }
  });
  asm volatile("s_nop 7");
  SCHED();
}
template <int XW, int S0, int S1, int N, int LEN> DI void xload(const Ctx& c, v4u (&x)[2][XW], int xvoff, int xsoff, int xmt, int rot) {
#pragma unroll
  for (int s = S1; s < S1 + N; ++s) {
    const int o = xsoff + (rot + s >= LEN ? rot + s - LEN : rot + s) * 1024;
    x[0][(S0 + s) % XW] = ld128(c, xvoff, o);
    x[1][(S0 + s) % XW] = ld128(c, xvoff, o + xmt);
  }
  SCHED();
}
DI int opaque(int v) { asm volatile("" : "+v"(v)); return v; }
constexpr float LOG2E = 1.4426950408889634f, PHID = 1.4426950408889634e-6f;
DI v2u phi4(f32x4 u) {
  f32x4 e, m;
#pragma unroll
  for (int i = 0; i < 4; ++i) { e[i] = __builtin_amdgcn_fmed3f(__builtin_amdgcn_exp2f(u[i]), 0.f, 1.f); m[i] = fmaxf(u[i], PHID); }
  const f32x4 y = m * 0.6931471805599453f + e;
  return v2u{pk2(y[0], y[1]), pk2(y[2], y[3])};
}

DI void pair_reduce(Ctx& c, int w, int cx, int n0, f32x4 (&acc)[2][5], unsigned (&y)[10]) {
  const unsigned lo = opaque(c.lane) * 16 + L_SCR;
  LAS f32x4* mine = LP(f32x4, lo + w * 5120);
  LAS f32x4* other = LP(f32x4, lo + (w ^ 1) * 5120);
#pragma unroll
  for (int t = 0; t < 5; ++t) mine[t * 64] = acc[1][t];
  ctl_add(cx + (w >> 1));
  ctl_wait(c, cx + (w >> 1), n0 + 2);
#pragma unroll
  for (int t = 0; t < 5; ++t) {
    const f32x4 o = acc[0][t] + other[t * 64];
    y[2 * t] = pk2(o[0], o[1]); y[2 * t + 1] = pk2(o[2], o[3]);
  }
}
DI int mod10(int v) { return v >= 20 ? v - 20 : v >= 10 ? v - 10 : v; }

__global__ void __launch_bounds__(512, 2)
mk_kernel(const char* __restrict__ x, const char* __restrict__ wqkv, const char* __restrict__ wo, const char* __restrict__ w1,
          const char* __restrict__ w2, char* __restrict__ out, char* ws) {
  u64 cpc; unsigned clen, toff;
  asm volatile(
      "s_getpc_b64 %0\n"
      ".Lpc%=:\n\t"
      "s_mov_b32 %1, .Lend%=-.Lpc%=-4\n\t"
      "s_mov_b32 %2, .Lmk_tab-.Lpc%=\n\t"
      ".subsection 1\n"
      ".Lmk_ring:\n\t"
      "v_mov_b32 v254, %3\n\t"
      "ds_read2_b32 v[254:255], v254 offset1:64\n\t"
      "s_sub_u32 s71, s71, 1\n\t"
      "s_cbranch_scc1 .Lmk_give\n\t"
      "s_waitcnt lgkmcnt(0)\n\t"
      "v_min_u32 v254, v254, v255\n\t"
      "v_cmp_gt_u32 vcc, s70, v254\n\t"
      "s_cbranch_vccnz .Lmk_ring\n\t"
      "s_setpc_b64 s[72:73]\n"
      ".Lmk_give:\n\t"
      "s_waitcnt lgkmcnt(0)\n\t"
      "s_mov_b32 s71, 0\n\t"
      "s_setpc_b64 s[72:73]\n\t"
      ".subsection 2\n"
      ".Lend%=:\n\t"
      ".fill 128, 4, 0xBF800000\n\t"
      ".subsection 0"
      : "=s"(cpc), "=s"(clen), "=s"(toff) : "i"(L_LAND));
  asm volatile("" :: "s"(x), "s"(wqkv), "s"(wo), "s"(w1), "s"(w2), "s"(out), "s"(ws));
  const int tid = threadIdx.x, w = __builtin_amdgcn_readfirstlane(tid >> 6);
  int lane = tid & 63;
  const int b = blockIdx.x, xc = b & 7, sx = b >> 3;
  const int pi = 4 * (xc >> 1) + (sx >> 3), pj = (xc & 1) * 8 + (sx & 7);
  const int pq_ = pj ^ PQX;
  const int hp = BMAP ? 8 * (xc >> 1) + (sx >> 2) : 4 * xc + (sx >> 3), cc = BMAP ? 4 * (xc & 1) + (sx & 3) : sx & 7;
  const int qi = CMAP ? pi : 2 * xc + (sx >> 4), qj = CMAP ? pj : sx & 15;
  const int r8 = BMAP ? sx >> 2 : sx & 7, r4 = BMAP ? sx & 3 : sx >> 3;
  const int rotB = mod10(((5 * r8) >> 2) + (BMAP || (ROTS & 1) ? (5 * r4) >> 1 : 0)), rotC = 5 * ((qj + ((ROTS & 2) ? sx >> 3 : 0)) & 3), rotD1 = (2 * r8 + 5 * r4) & 15;
  const int rd2 = ((11 * r8) >> 3) + 3 * r4, rotD2 = rd2 >= 11 ? rd2 - 11 : rd2;
  int n16 = lane & 15, g = lane >> 4;
  Ctx c;
  c.lane = lane;
  c.rt = __builtin_amdgcn_make_buffer_rsrc((void*)(cpc + toff), 0, 512, 0x00020000);
  if (__builtin_expect((w >> 1) == 2, 1)) {
    __builtin_amdgcn_s_setprio(LPRIO);
    const int rot = ROTA == 0 ? 0 : ROTA == 2 ? 5 * (sx & 1) : mod10((sx & 7) + ((5 * (sx >> 3)) >> 1) + (xc >> 1));
    const int Lw = w - 4;
    const int ln = opaque(lane), xr = ln >> 3;
    int xv[4], ka[5], qa[5];
    bool hi_[5];
    xv[0] = xr * 1280 + (((ln & 7) ^ (xr >> 1)) * 16) + 3072; xv[1] = (xv[0] ^ 64) + 10240 - 1024; xv[2] = xv[0] - 2048; xv[3] = xv[1] - 2048;
    const auto raw_addr = [&](int (&a)[5], int lo, int hi) AI {
#pragma unroll
      for (int i = 0; i < 5; ++i) {
        const unsigned m = ln * 205 + 64 * 205 * i, rho = m >> 11;
        const int ch16 = ln * 16 + 1024 * i - 160 * rho;
        const int k = ((m >> 10) & 24) | ((m >> 13) & 4) | (rho & 3);
        hi_[i] = ch16 >= 80;
        a[i] = k * 3840 + ch16 + 3072 - (i & 3) * 1024 + (ch16 < 80 ? lo : hi);
        PIN(a[i]);
      }
    };
#pragma unroll
    for (int i = 0; i < 4; ++i) PIN(xv[i]);
    unsigned lim = RCH, bud = 1u << 20, key = 0;
    const unsigned lw = Lw * 5120;
    int sxr = (128 * pi + 64 * Lw) * 1280, sr = rot * 128, sl = Lw * 8192;
    asm volatile("" : "+s"(sxr), "+s"(sr), "+s"(sl));
    SCHED();
    const rsrc_t rx = __builtin_amdgcn_make_buffer_rsrc((void*)(x - 3072), 0, 2048 * 1280 + 3072, 0x00020000);
#define MK_ST "+{s70}"(lim), "+{s71}"(bud), "+{s84}"(key), "+{s80}"(sr), "+{s85}"(sl)
#define MK_IN "{s81}"(lw), "{s76}"(L_LAND + Lw * 256), [rt] "s"(c.rt)
#define MK_CL "memory", "scc", "s72", "s73", "s74", "s75", "s77", "s78", "s79", "s82", "s83", "s86", "s87", "s88", "s90", "s91", "v248", "v252", "v253", "v254", "v255"
    asm volatile(
        ".macro MK_LD dep, ops:vararg\n\t"
        ".if \\dep\n\ts_waitcnt vmcnt(\\dep)\n\t.endif\n\t"
        "buffer_load_dwordx4 \\ops lds\n\t"
        ".endm\n\t"
        ".macro MK_FLAG rt, add\n\t"
        "s_mov_b32 m0, s76\n\t"
        "s_lshl2_add_u32 s83, s84, 4*\\add\n\t"
        "buffer_load_dword off, \\rt, s83 lds\n\t"
        ".endm\n\t"
        ".macro MK_HEAD key\n\t"
        "s_cmp_gt_i32 s70, \\key\n\t"
        "s_cbranch_scc1 1f\n\t"
        "s_mov_b32 s74, \\key\n\t"
        "s_call_b64 s[72:73], .Lmk_lslow\n"
        "1:\n\t"
        ".endm\n\t"
        ".macro MK_RAW dep, a0, a1, a2, a3, a4, rs, step\n\t"
        "MK_LD \\dep, \\a0, \\rs, s82 offen\n\t"
        "s_add_u32 s78, s82, \\step\n\t"
        "MK_LD \\dep, \\a1, \\rs, s82 offen offset:1024\n\t"
        "s_sub_u32 s79, s78, 2457600\n\t"
        "MK_LD \\dep, \\a2, \\rs, s82 offen offset:2048\n\t"
        "MK_LD \\dep, \\a3, \\rs, s82 offen offset:3072\n\t"
        "s_add_u32 m0, m0, 4096\n\t"
        "s_min_u32 s78, s78, s79\n\t"
        "MK_LD \\dep, \\a4, \\rs, s82 offen\n\t"
        "s_mov_b32 s82, s78\n\t"
        ".endm\n\t"
        ".macro MK_X4 dep, a0, a1, a2, a3, rs\n\t"
        "MK_LD \\dep, \\a0, \\rs, s77 offen\n\t"
        "s_add_u32 s78, s77, 20480\n\t"
        "MK_LD \\dep, \\a1, \\rs, s77 offen offset:1024\n\t"
        "MK_LD \\dep, \\a2, \\rs, s78 offen offset:2048\n\t"
        "s_add_u32 s77, s77, 40960\n\t"
        "MK_LD \\dep, \\a3, \\rs, s78 offen offset:3072\n\t"
        ".endm\n\t"
        ".macro MK_X dep, a0, a1, a2, a3, rs, sx\n\t"
        "s_mov_b32 m0, s85\n\t"
        "s_add_u32 s77, s80, \\sx\n\t"
        "MK_X4 \\dep, \\a0, \\a1, \\a2, \\a3, \\rs\n\t"
        "s_add_u32 m0, s85, 4096\n\t"
        "s_add_u32 s80, s80, 128\n\t"
        "MK_X4 \\dep, \\a0, \\a1, \\a2, \\a3, \\rs\n\t"
        "s_sub_u32 s79, s80, 1280\n\t"
        "s_min_u32 s80, s80, s79\n\t"
        ".endm\n\t"
        ".subsection 1\n"
        ".Lmk_lsleep:\n\t"
        "s_sleep 1\n"
        ".Lmk_lslow:\n\t"
        "v_mov_b32 v248, %[dn]\n\t"
        "ds_read2st64_b32 v[252:253], v248 offset1:1\n\t"
        "ds_read2st64_b32 v[254:255], v248 offset0:2 offset1:3\n\t"
        "ds_read_b32 v248, v248 offset:%[cap]\n\t"
        "s_sub_u32 s71, s71, 1\n\t"
        "s_cbranch_scc1 .Lmk_lgive\n\t"
        "s_waitcnt lgkmcnt(0)\n\t"
        "v_min3_u32 v252, v252, v253, v254\n\t"
        "v_min_u32 v252, v252, v255\n\t"
        "v_readfirstlane_b32 s75, v248\n\t"
        "v_readfirstlane_b32 s70, v252\n\t"
        "s_add_u32 s70, s70, 13\n\t"
        "s_min_u32 s70, s70, s75\n\t"
        "s_cmp_gt_i32 s70, s74\n\t"
        "s_cbranch_scc0 .Lmk_lsleep\n\t"
        "s_setpc_b64 s[72:73]\n"
        ".Lmk_lgive:\n\t"
        "s_endpgm\n\t"
        ".subsection 0\n\t"
        "MK_X 0, %[x0], %[x1], %[x2], %[x3], %[rx], %[sx]"
        : MK_ST
        : MK_IN, [x0] "v"(xv[0]), [x1] "v"(xv[1]), [x2] "v"(xv[2]), [x3] "v"(xv[3]), [rx] "s"(rx), [sx] "s"(sxr),
          [dn] "i"(L_DONE), [cap] "i"(L_CTL + 4 * C_CAP - L_DONE)
        : MK_CL);
    SCHED();
    *LP(unsigned, L_LAND + Lw * 256 + ln * 4) = 0u;
    raw_addr(ka, 1280, 2480);
    const int ok = Lw * 122880 + pj * 80 - 3072;
    const rsrc_t rk = __builtin_amdgcn_make_buffer_rsrc((void*)(wqkv + ok), 0, 640 * 3840 - ok, 0x00020000);
    asm volatile(
        "s_mov_b32 s82, %[sk]\n\t"
        "s_waitcnt lgkmcnt(0)\n"
        ".Lmk_blk:\n\t"
        "s_add_u32 m0, s85, %[kl]\n\t"
        "s_add_u32 s85, s85, 26624\n\t"
        "MK_RAW " STR(DEP_A) ", %[k0], %[k1], %[k2], %[k3], %[k4], %[rk], 245760\n\t"
        "MK_FLAG %[rt], 3\n\t"
        "s_sub_u32 s79, s85, 133120\n\t"
        "s_min_u32 s85, s85, s79\n\t"
        "s_add_u32 s84, s84, 3\n\t"
        "s_cmpk_eq_u32 s84, 30\n\t"
        "s_cbranch_scc1 .Lmk_blkend\n\t"
        "s_cmpk_lg_u32 s84, 3*" STR(XPRE) "\n\t"
        "s_cbranch_scc1 .Lmk_nobar\n\t"
        "s_barrier\n"
        ".Lmk_nobar:\n\t"
        "MK_HEAD s84\n\t"
        "MK_X " STR(DEP_A) ", %[x0], %[x1], %[x2], %[x3], %[rx], %[sx]\n\t"
        "s_branch .Lmk_blk\n"
        ".Lmk_blkend:\n\t"
        ".purgem MK_X4\n\t.purgem MK_X"
        : MK_ST
        : MK_IN, [x0] "v"(xv[0]), [x1] "v"(xv[1]), [x2] "v"(xv[2]), [x3] "v"(xv[3]), [rx] "s"(rx), [sx] "s"(sxr),
          [kl] "s"(16384 - Lw * 3072), [k0] "v"(ka[0]), [k1] "v"(ka[1]), [k2] "v"(ka[2]), [k3] "v"(ka[3]), [k4] "v"(ka[4]), [rk] "s"(rk), [sk] "s"(rot * 245760)
        : MK_CL);
    SCHED();
#pragma unroll
    for (int i = 0; i < 5; ++i) { qa[i] = ka[i] - 1280 + (hi_[i] ? 32 * 3840 - 80 - 1200 : 0); PIN(qa[i]); }
    const int oq = pq_ * 80 - 3072;
    const rsrc_t rw = __builtin_amdgcn_make_buffer_rsrc((void*)(wqkv + oq), 0, 640 * 3840 - oq, 0x00020000);
    char* ws2 = ws;
    asm volatile("" : "+s"(ws2));
    const rsrc_t rs = __builtin_amdgcn_make_buffer_rsrc((void*)ws2, 0, WS_BYTES, 0x00020000);
    asm volatile(
        "s_mov_b32 s82, %[sq]\n\t"
        "s_mov_b32 s85, s81\n\t"
        "s_movk_i32 s86, 30\n"
        ".Lmk_q:\n\t"
        "MK_HEAD s86\n\t"
        "s_mov_b32 m0, s85\n\t"
        "s_add_u32 s85, s85, 10240\n\t"
        "MK_RAW " STR(DEP_A) ", %[q0], %[q1], %[q2], %[q3], %[q4], %[rw], 491520\n\t"
        "MK_FLAG %[rt], 1\n\t"
        "s_add_u32 s84, s84, 1\n\t"
        "s_cmpk_ge_u32 s84, 32\n\t"
        "s_cselect_b32 s86, 33, 30\n\t"
        "s_cmpk_lg_u32 s84, 35\n\t"
        "s_cbranch_scc1 .Lmk_q"
        : MK_ST
        : MK_IN, [q0] "v"(qa[0]), [q1] "v"(qa[1]), [q2] "v"(qa[2]), [q3] "v"(qa[3]), [q4] "v"(qa[4]), [rw] "s"(rw), [sq] "s"(mod10(rot + Lw) * 245760)
        : MK_CL);
    SCHED();
#pragma unroll
    for (int i = 0; i < 5; ++i) {
      const unsigned rho = (ln * 205 + 64 * 205 * i) >> 11;
      const int o = ln * 16 + 1024 * i - 160 * rho + 3072 - (i & 3) * 1024, gg = (rho >> 2) & 3, j = ((rho >> 2) & 4) | (rho & 3);
      ka[i] = (5 * gg + (j >= 5 ? 15 + j : j)) * 1280 + o;
      qa[i] = (40 * gg + 23 + 5 * (j >> 1) + (j & 1)) * 1280 + o;
      PIN(ka[i]); PIN(qa[i]);
    }
    const rsrc_t ro = __builtin_amdgcn_make_buffer_rsrc((void*)(wo + (cc * 160 - 3072)), 0, 819200 - (cc * 160 - 3072), 0x00020000);
    asm volatile(
        "s_mov_b32 s80, %[rb]\n"
        ".Lmk_b:\n\t"
        "s_max_u32 s86, s84, 40\n\t"
        "s_cmp_lt_u32 s84, 39\n\t"
        "s_cselect_b32 s86, 39, s86\n\t"
        "s_cmp_lt_u32 s84, 37\n\t"
        "s_cselect_b32 s86, 36, s86\n\t"
        "MK_HEAD s86\n\t"
        "s_mov_b32 m0, s85\n\t"
        "s_add_u32 s85, s85, 10240\n\t"
        "s_cmp_ge_u32 s80, 16\n\t"
        "s_cbranch_scc1 .Lmk_b1\n\t"
        "s_mul_i32 s82, s80, 51200\n\t"
        "MK_RAW " STR(DEP_A) ", %[k0], %[k1], %[k2], %[k3], %[k4], %[ro], 0\n\t"
        "s_branch .Lmk_b2\n"
        ".Lmk_b1:\n\t"
        "s_mul_i32 s82, s80, 204800\n\t"
        "s_sub_u32 s82, s82, 3276800\n\t"
        "MK_RAW " STR(DEP_A) ", %[q0], %[q1], %[q2], %[q3], %[q4], %[ro], 0\n"
        ".Lmk_b2:\n\t"
        "MK_FLAG %[rt], 1\n\t"
        "s_sub_u32 s79, s85, 133120\n\t"
        "s_min_u32 s85, s85, s79\n\t"
        "s_add_u32 s80, s80, 1\n\t"
        "s_cmp_eq_u32 s80, %[be]\n\t"
        "s_cselect_b32 s79, 10, 0\n\t"
        "s_sub_u32 s80, s80, s79\n\t"
        "s_add_u32 s84, s84, 1\n\t"
        "s_cmpk_lg_u32 s84, 45\n\t"
        "s_cbranch_scc1 .Lmk_b\n\t"
        ".macro MK_SEG rot, base, len, end\n\t"
        "s_mov_b32 s80, \\rot\n\t"
        "s_mov_b32 s87, \\base\n\t"
        "s_mov_b32 s88, \\len\n\t"
        "s_movk_i32 s86, \\end\n\t"
        "s_call_b64 s[90:91], .Lmk_pk\n\t"
        ".endm\n\t"
        "MK_SEG %[rc1], %[sc1], 81920, 53\n\t"
        "s_add_u32 s87, s87, 81920\n\t"
        "MK_SEG %[rc2], s87, 61440, 59\n\t"
        "MK_SEG %[rd1], %[sd1], 163840, 75\n\t"
        "s_add_u32 s87, s87, 163840\n\t"
        "MK_SEG %[rd2], s87, 112640, 86\n\t"
        "s_waitcnt vmcnt(0)\n\t"
        "s_endpgm\n"
        ".Lmk_pk:\n\t"
        "MK_HEAD s84\n\t"
        "s_mov_b32 m0, s85\n\t"
        "s_add_u32 s77, s80, s87\n\t"
        "MK_LD " STR(DEP_P) ", %[l0], %[rs], s77 offen sc1\n\t"
        "s_add_u32 s80, s80, 10240\n\t"
        "MK_LD " STR(DEP_P) ", %[l0], %[rs], s77 offen offset:1024 sc1\n\t"
        "s_sub_u32 s79, s80, s88\n\t"
        "MK_LD " STR(DEP_P) ", %[l0], %[rs], s77 offen offset:2048 sc1\n\t"
        "s_min_u32 s80, s80, s79\n\t"
        "MK_LD " STR(DEP_P) ", %[l0], %[rs], s77 offen offset:3072 sc1\n\t"
        "s_add_u32 m0, m0, 4096\n\t"
        "s_add_u32 s85, s85, 10240\n\t"
        "MK_LD " STR(DEP_P) ", %[l1], %[rs], s77 offen sc1\n\t"
        "MK_FLAG %[rt], 1\n\t"
        "s_sub_u32 s79, s85, 133120\n\t"
        "s_min_u32 s85, s85, s79\n\t"
        "s_add_u32 s84, s84, 1\n\t"
        "s_cmp_lg_u32 s84, s86\n\t"
        "s_cbranch_scc1 .Lmk_pk\n\t"
        "s_setpc_b64 s[90:91]\n"
        ".Lmk_tab:\n\t"
        ".set mk_i, 0\n\t.rept 128\n\t.long mk_i\n\t.set mk_i, mk_i + 1\n\t.endr\n\t"
        ".purgem MK_FLAG\n\t.purgem MK_HEAD\n\t.purgem MK_RAW\n\t.purgem MK_SEG\n\t.purgem MK_LD"
        : MK_ST
        : MK_IN, [q0] "v"(qa[0]), [q1] "v"(qa[1]), [q2] "v"(qa[2]), [q3] "v"(qa[3]), [q4] "v"(qa[4]),
          [k0] "v"(ka[0]), [k1] "v"(ka[1]), [k2] "v"(ka[2]), [k3] "v"(ka[3]), [k4] "v"(ka[4]), [ro] "s"(ro),
          [l0] "v"(ln * 16), [l1] "v"(ln * 16 + 4096), [rs] "s"(rs),
          [rb] "s"(rotB + 10 * Lw), [be] "s"(10 + 10 * Lw),
          [sc1] "s"(OFF_WC + qj * 143360), [rc1] "s"(rotC * 4096 + lw),
          [rc2] "s"(rotC * 3072 + lw),
          [sd1] "s"(OFF_WD + cc * 276480), [rd1] "s"(rotD1 * 10240 + lw),
          [rd2] "s"(rotD2 * 10240 + lw)
        : MK_CL);
  }
  STAMP(0);
  c.ws = (GAS char*)ws;
  c.rs = __builtin_amdgcn_make_buffer_rsrc((void*)ws, 0, WS_BYTES, 0x00020000);
  c.budget = 1u << 22;
  unsigned e0 = __hip_atomic_load((GAS unsigned*)(c.ws + OFF_EPOCH), RLX_AGENT);
  const auto epoch = [&]() AI { SCHED(); PIN(e0); c.ep = (__builtin_amdgcn_readfirstlane(e0) + EPOCH_STEP) | 1u; };
  unsigned pf = 0u, pg = 0u;
  {
    if (w == 7) {
      const v4u z = {0u, 0u, 0u, 0u};
      *LP(v4u, L_DONE + lane * 16) = z;
      *LP(v4u, L_FLG + lane * 16) = z;
      *LP(unsigned, L_CTL + (lane & 31) * 4) = (lane & 31) == C_CAP ? 40 : 0u;
#pragma unroll
      for (int i = 0; i < 4; ++i) *LP(v4u, L_RPK + i * 1024 + lane * 16) = z;
      pf = __builtin_amdgcn_raw_buffer_load_b32(c.rs, OFF_FLAGS + (b * 18 + min(lane, 17)) * 128, 0, SC1);
      pg = *(const GAS unsigned*)(cpc + min((sx * 16 + n16) * 128u, clen));
      LGKM0();
    }
    __builtin_amdgcn_s_barrier();
    CFENCE();
    STAMP(30);
  }
  if (__builtin_expect(w >= 4, 0)) {
    __builtin_amdgcn_s_setprio(3);
    const int hw = w - 6;
    STL(62);
    const bool fast = (0xa5 >> (pj & 7)) & 1;
    if (hw == 0) {
      const int u = pi * 8 + (pj >> 1) + (fast ? 0 : 128), wi = min(lane >> 2, 4), e = lane & 3;
      const bool k2 = u < 108;
      int cb, s, A, B = 4, C = 8;
      if (k2) {
        cb = u >> 2; s = 5 * (u & 3) + wi;
        A = 160 * (u & 3) + 32 * wi - (wi > 2 ? 16 : 0);
        if (wi == 2) { B = 8; C = 80; }
      } else {
        const int k = 5 * (u - 108) + wi;
        cb = k / 54; s = k - 54 * cb;
        if (s < 48) A = 16 * (s < 32 ? gam0(s >> 1) + 2 * (s & 1) : gam0(s - 32) + 4);
        else { const int l = 2 * (s - 48), a0 = gam0(l + l / 3), a1 = gam0(l + 1 + (l + 1) / 3); A = 16 * a0 + 96; B = 8; C = 16 * (a1 - a0); }
      }
      const int pitch = k2 ? 3456 : 1280;
      int so = (A + B * (e & 1) + C * (e >> 1)) * pitch + cb * 128;
      const int gam = 4 * cb + e, grp = gam / 27, rr = gam - 27 * grp, jj = min(rr / 7, 3), t = rr - 7 * jj, c5 = gam / 5;
      int db = k2 ? OFF_WC + (4 * grp + jj) * 143360 + (t < 4 ? 4 * s + t : 76 + 3 * s + t) * 1024
                  : OFF_WD + c5 * 276480 + ((s < 16 ? s * 10 : s < 32 ? (s - 16) * 10 + 5 : s < 43 ? (s - 16) * 10 : (s - 27) * 10 + 5) + gam - 5 * c5) * 1024;
      PIN(so); PIN(db); PIN(B);
      const rsrc_t r0 = __builtin_amdgcn_make_buffer_rsrc((void*)(k2 ? w1 : w2), 0, 2211840, 0x00020000);
      unsigned t0 = L_SCR + g * 1024 + ((lane >> 2) & 3) * 128 + 8 * e;
      PIN(t0);
      int va[5], dd[5][4];
#pragma unroll
      for (int i = 0; i < 5; ++i) {
        va[i] = ((lane >> 3) + (lane >= 32 && __builtin_amdgcn_readlane(B, 4 * i) == 4 ? 12 : 0)) * pitch + (lane & 7) * 16;
        PIN(va[i]);
#pragma unroll
        for (int e2 = 0; e2 < 4; ++e2) { dd[i][e2] = lane * 16 + __builtin_amdgcn_readlane(db, 4 * i + e2); PIN(dd[i][e2]); }
      }
      const int g0 = gam0(qj), c0 = (80 * cc) >> 6;
      int dep = lane < 12 ? min(4 * (g0 >> 2) + lane, 3 + 4 * ((g0 + 5) >> 2) + ((qj & 3) != 3 && ((g0 + 6) & 3) == 0 ? 4 : 0)) : min(108 + (54 * c0) / 5 + lane - 12, 108 + (54 * c0 + 107) / 5);
      dep += FLR;
      PIN(dep);
      STAMP(36);
      epoch();
      asm volatile("" :: "s"(c.ep));
      ctl_addc<C_ARR1>();
      v4u v[5][4];
      const auto fetch = [&]() AI {
#pragma unroll
        for (int i = 0; i < 20; ++i)
          asm volatile("s_mov_b32 m0, %0\n\tv_readlane_b32 s70, %3, %4\n\ts_nop 4\n\tbuffer_load_dwordx4 %1, %2, s70 offen lds"
                       :: "i"(L_SCR + i * 1024), "v"(va[i >> 2]), "s"(r0), "v"(so), "i"(i) : "memory", "s70");
      };
      const auto turn = [&]() AI {
        DRAIN();
#pragma unroll
        for (int i = 0; i < 5; ++i)
#pragma unroll
          for (int e2 = 0; e2 < 4; ++e2) v[i][e2] = tr2(t0, i * 4096 + 32 * e2, 512);
      };
      const auto put = [&]() AI {
#pragma unroll
        for (int i = 0; i < 5; ++i)
#pragma unroll
          for (int e2 = 0; e2 < 4; ++e2) st128(c, v[i][e2], dd[i][e2], 0);
      };
      if (k2) land_wait(c, RPK_F);
      STAMP(21);
      if (k2) {
        fetch();
        turn();
        put();
        float cs[4];
#pragma unroll
        for (int e2 = 0; e2 < 4; ++e2) {
          cs[e2] = 0.f;
#pragma unroll
          for (int i = 0; i < 5; ++i) cs[e2] = dot2(v[i][e2].x, ONE2, dot2(v[i][e2].y, ONE2, dot2(v[i][e2].z, ONE2, dot2(v[i][e2].w, ONE2, cs[e2]))));
        }
        v2u q0 = swap32(asu(cs[0]), asu(cs[2])), q1 = swap32(asu(cs[1]), asu(cs[3]));
        q0 = swap16(asu(asf(q0.x) + asf(q0.y)), asu(asf(q1.x) + asf(q1.y)));
        st32(c, asu(asf(q0.x) + asf(q0.y)), lane * 4, OFF_CS + ((u & 3) * 1792 + 64 * (u >> 2)) * 4);
      }
      STAMP(32);
      const bool late = !k2 && u < UMAX;
      if (!late) { ctl_addc<C_XB0>(); ctl_addc<C_XB1>(); }
      if (k2) { DRAIN(); flag_set(c, FLR + u); }
      STAMP(39);
      if ((TCH & 4) && !k2) touch(w2, 2211840, (5632 + 79 * (u - 108)) * 128, 79, 128);
      STAMP(37);
      ctl_wait<1>(c, C_PUB, LATE_L);
      if (late) fetch();
      flag_wait(c, dep, 12);
      if (lane == 0) *LP(volatile unsigned, L_CTL + 4 * C_CAP) = CH_D;
      STAMP(40);
      if (late) { turn(); ctl_addc<C_XB0>(); ctl_addc<C_XB1>(); }
      STAMP(41);
      done_wait(c, LATE_S);
      {
        f32x4 cs[4];
#pragma unroll
        for (int rb = 0; rb < 4; ++rb) cs[rb] = __builtin_bit_cast(f32x4, ld128(c, min(lane, 27) * 16, OFF_CS + (rb * 1792 + 16 * g0) * 4));
        SCHED();
        if (late) put();
        if (lane < 28) *LP(f32x4, L_CSUM + lane * 16) = add4(add4(cs[0], cs[1]), add4(cs[2], cs[3]));
      }
      ctl_addc<C_CS>();
      DRAIN();
      if (!k2) flag_set(c, FLR + u);
      STAMP(22);
      ctl_wait<2>(c, C_XB0, 3);
      ctl_wait<2>(c, C_XB1, 3);
      __builtin_amdgcn_s_sleep(8);
      STAMP(20);
      flag_wait(c, dep, 36);
      if (lane == 0) *LP(volatile unsigned, L_CTL + 4 * C_CAP) = 127;
      STAMP(34);
      DUMPL(20, 21, 22, 32, 34, 36, 37, 39, 40, 41);
      ENDPGM();
    }
    STL(62);
    unsigned ra[4], gd[4];
#pragma unroll
    for (int i = 0; i < 4; ++i) {
      {
        const int gr = min(lane + 32 * (i & 2), 119), hd = i & 1, z = gr >= 100, d4 = z ? gr % 5 : gr / 20;
        const int d4g = 5 * hd + d4, e8 = z ? 0 : 20 * hd + gr - 20 * d4 + 8;
        ra[i] = KVSLOT * 10240 + ((d4g >> 2) * 3 + (e8 >> 4)) * 1024 + ((d4g & 3) * 16 + (e8 & 15)) * 16;
      }
      {
        const int gr = 32 * i + 16 * (g >> 1) + n16, hd = g & 1, z = gr >= 100, d4 = z ? gr % 5 : gr / 20;
        const int e = gr - 20 * d4, gi = e / 5, r5 = e - 5 * gi;
        const int R = z || r5 == 4, row = z ? 4 * ((gr - 100) / 5) + 1 : r5 == 4 ? 4 * gi : 4 * gi + r5;
        const int dd = d4 + hd;
        gd[i] = gr < 120 ? L_RPK + (hd * 2 + R) * 1024 + ((dd & 3) * 16 + row) * 16 + (dd >> 2) * 8 : L_CK;
      }
      PIN(ra[i]); PIN(gd[i]);
    }
    int gs = g * KVP_STR + n16 * 16, gs7 = g * KVP_STR + (n16 & 7) * 16, kvo = OFF_KVP + (pj * 16 + pi) * KVP_STR + lane * 16;
    v4u sel[2];
#pragma unroll
    for (int i = 0; i < 4; ++i) {
      const unsigned o = ((n16 >> 1) & 3) == i ? (n16 & 1 ? 0x3f800000u : 0x3f80u) : 0u;
      sel[0][i] = n16 < 8 ? o : 0u; sel[1][i] = n16 < 8 ? 0u : o;
    }
    PIN(gs); PIN(gs7); PIN(kvo); PIN(sel[0]); PIN(sel[1]);
    if (TCH & 2) touch(wo, 819200, b * 3200, 25, 128);
    if (TCH & 4) touch(w2, 2211840, b * 2816, 22, 128);
    STAMP(38);
    epoch();
    TOUCH(pf); TOUCH(pg);
    ctl_wait<1>(c, C_ARR1, 5);
    STAMP(26);
    {
      f32x4 p[4][4];
#pragma unroll
      for (int i = 0; i < 4; ++i)
#pragma unroll
        for (int j = 0; j < 4; ++j) p[i][j] = *LP(f32x4, ra[i] + j * 10240);
      if (!CAPL) { CFENCE(); if (lane == 0) *LP(volatile unsigned, L_CTL + 4 * C_CAP) = CH_C1; }
#pragma unroll
      for (int h = 0; h < 2; ++h) {
        const f32x4 u = add4(add4(p[2 * h][0], p[2 * h][1]), add4(p[2 * h][2], p[2 * h][3])), v = add4(add4(p[2 * h + 1][0], p[2 * h + 1][1]), add4(p[2 * h + 1][2], p[2 * h + 1][3]));
        if (h == 0 || lane < 56) st128(c, v4u{pk2(u[0], u[1]), pk2(u[2], u[3]), pk2(v[0], v[1]), pk2(v[2], v[3])}, kvo, h * 1024);
      }
    }
    if (CAPL) { CFENCE(); if (lane == 0) *LP(volatile unsigned, L_CTL + 4 * C_CAP) = CH_C1; }
    STAMP(27);
    DRAIN();
    flag_set(c, FL1 + pj * 16 + pi);
    ctl_addc<C_PUB>();
    STAMP(23);
    flag_wait(c, FL1 + pq_ * 16 + n16, 16);
    STAMP(24);
    {
      v4u p[4][8];
#pragma unroll
      for (int t = 0; t < 4; ++t)
#pragma unroll
        for (int i = 0; i < 8; ++i) p[t][i] = ld128(c, (i == 7 ? gs7 : gs) + i * 256, OFF_KVP + (pq_ * 16 + 4 * t) * KVP_STR);
      ctl_addc<C_PUB>();
      SCHED(); STAMP(35); SCHED();
      f32x4 u[4];
#pragma unroll
      for (int i = 0; i < 4; ++i) u[i] = f32x4{0.f, 0.f, 0.f, 0.f};
#pragma unroll
      for (int t = 0; t < 4; ++t)
#pragma unroll
        for (int i = 0; i < 8; ++i) u[i >> 1] = mfma(sel[i & 1], p[t][i], u[i >> 1]);
#pragma unroll
      for (int i = 0; i < 4; ++i) *LP(v2u, gd[i]) = v2u{pk2(u[i][0], u[i][1]), pk2(u[i][2], u[i][3])};
    }
    ctl_addc<C_GO1>();
    LGKM0();
    STAMP(25);
    STL(25);
    ctl_wait<2>(c, C_XB0, 3);
    if (b == 0 && lane == 0) __hip_atomic_store((GAS unsigned*)(c.ws + OFF_EPOCH), c.ep, RLX_AGENT);
    ctl_wait<1>(c, C_ARR4A, 4);
    flag_set(c, FL4A + qi * 16 + qj);
    STAMP(42);
    flag_wait(c, FL4A + (hp >> 1) * 16 + n16, 16);
    ctl_addc<C_GO4A>();
    STAMP(28);
    ctl_wait<1>(c, C_ARR4B, 4);
    flag_set(c, FL4B + qi * 16 + qj);
    STAMP(44);
    flag_wait(c, FL4B + (hp >> 1) * 16 + n16, 16);
    ctl_addc<C_GO4B>();
    STAMP(29);
    DUMPL(23, 24, 25, 26, 27, 28, 29, 35, 38, 42, 44);
    ENDPGM();
  }

  int xvoff;
  STL(30);
  const unsigned dn = L_LAND + 128 + lane * 4;
  c.dm = L_DONE + w * 256;
  const int kb = w & 1;
  const int mtB = 4 * hp + w;
  v2u xr[5];

  {
    v4u xa[2][20];
    const unsigned hv = n16 < 8 ? ONE2 : 0u;
    v4u ones = {hv, hv, hv, hv}, oneh = {hv ^ ONE2, hv ^ ONE2, hv ^ ONE2, hv ^ ONE2};
    PIN(ones); PIN(oneh);
    unsigned rawlane = (4 * g + ((lane >> 2) & 3)) * 160 + 8 * (lane & 3);
    const int oc = 32 * w, o4 = 128 + 5120 * (w & 1);
    {
      f32x4 acc[2][5];
      {
        unsigned br0 = w * 4096 + (n16 >> 3) * 1024 + (n16 & 7) * 128 + ((g ^ (n16 >> 1)) * 16), br1 = br0 ^ 64, wb = rawlane + 16384;
        PIN(br0); PIN(br1); PIN(wb);
        f32x4 sm, sq[2], ca;
        v4u wf[4], cf;
        v2u clo;
        unsigned la, cad;
        float q2;
        const auto addr = [&](int o) AI { unsigned r; asm volatile("v_add_u32 %0, %1, %2" : "=v"(r) : "s"(o), "v"(wb)); return r; };
        const auto stat = [&](auto qc, auto bc) AI {
          constexpr int q = decltype(qc)::v, m = q & 1, e = 2 * decltype(bc)::v + (q >> 2);
          if constexpr ((q & 2) == 0) mm<1, e == 0>(sq[m], xa[m][e], xa[m][e]);
          else mm<1, e == 0 && m == 0>(sm, m ? oneh : ones, xa[m][e]);
          SCHED();
        };
        epoch();
        ring_wait<0>(c);
        STAMP(31);
        SCHED();
        static_for<-4, 100>([&](auto ic) AI {
          constexpr int f = decltype(ic)::v, fn = f + 3;
          constexpr int s = f < 0 ? 0 : f / 5, t = f < 0 ? 0 : f % 5;
          constexpr int cb = f < 0 ? -1 : f / 10, i = (f + 10) % 10, nb = cb + 1;
          constexpr int off = fn < 0 ? 0 : bin(fn / 10) + ((fn % 10) / 5) * 5120 + 32 * (fn % 5);
          constexpr bool R = fn >= 0 && fn < 100, more = cb < 9, go = f >= 0, rel = true;
          v2u lo;
          if constexpr (go) { mm<0, s == 0>(acc[0][t], wf[f % 4], xa[0][s]); SCHED(); }
          if constexpr (R) { lo = tr1(wb, off); SCHED(); }
          if constexpr (i == 4) { cad = addr(o4); SCHED(); }
          if constexpr (i == 8 && more) { cad = addr(oc); SCHED(); }
          if constexpr (i == 1 && more) bump<breg(nb) - breg(cb)>(br0);
          if constexpr (i == 2 && more) bump<breg(nb) - breg(cb)>(br1);
          if constexpr (i == 9 && go && rel) { release<3 * cb + 3>(c); SCHED(); }
          if constexpr (i == 6 && go && more) { ring_cmp<3 * nb>(la); SCHED(); }
          if constexpr (go) { mm<0, s == 0>(acc[1][t], wf[f % 4], xa[1][s]); SCHED(); }
          if constexpr (R) { const v2u hi = tr1(wb, off + 2560); wf[fn % 4] = v4u{lo.x, lo.y, hi.x, hi.y}; SCHED(); }
          if constexpr (i == 6 && go && more) { ring_br<3 * nb>(c); SCHED(); }
          if constexpr (go) {
            if constexpr (i == 1 || i == 4) { mm<1, f == 1>(ca, ones, cf); SCHED(); }
            else if constexpr (i == 7) { mm<1, false>(ca, oneh, cf); SCHED(); }
            else if constexpr (more) stat(IC<i - (i + 1) / 3>{}, IC<cb>{});
            else if constexpr (i < 4) stat(IC<i == 0 ? 0 : i == 2 ? 4 : 2>{}, IC<cb>{});
          }
          if constexpr (i == 0) xa[1][2 * cb + 1] = *LP(v4u, br1 + bin(cb) + 2048);
          if constexpr (i == 1) clo = tr1(cad, bin(cb) + 5120);
          if constexpr (i == 2) { const v2u hi = tr1(cad, bin(cb) + 7680); cf = v4u{clo.x, clo.y, hi.x, hi.y}; }
          if constexpr (i == 3 && more) la = *LP(unsigned, dn);
          if constexpr (i == 4) clo = tr1(cad, bin(cb));
          if constexpr (i == 5) { const v2u hi = tr1(cad, bin(cb) + 2560); cf = v4u{clo.x, clo.y, hi.x, hi.y}; }
          if constexpr (i == 6 && more) { xa[0][2 * nb] = *LP(v4u, br0 + bin(nb)); SCHED(); if constexpr (go) bump<breg(nb) - breg(cb)>(wb); }
          if constexpr (i == 7 && more) xa[1][2 * nb] = *LP(v4u, br0 + bin(nb) + 2048);
          if constexpr (i == 8 && more) clo = tr1(cad, bin(nb));
          if constexpr (i == 9 && more) { const v2u hi = tr1(cad, bin(nb) + 2560); cf = v4u{clo.x, clo.y, hi.x, hi.y}; }
          SCHED();
          if constexpr (i == 9 && go && more) stat(IC<7>{}, IC<cb>{});
          if constexpr (i == 9 && more) { xa[0][2 * nb + 1] = *LP(v4u, br1 + bin(nb)); SCHED(); }
          if constexpr (!more && i < 5) stat(IC<i == 0 ? 1 : i == 1 ? 3 : i == 2 ? 5 : i + 3>{}, IC<cb>{});
          if constexpr (f == 94) {
            const unsigned bw = 4 * BLK + w * 4096 + opaque(lane) * 16;
            *LP(f32x4, bw) = sq[0];
            *LP(f32x4, bw + 1024) = sq[1];
            CFENCE();
            SCHED();
          }
          if constexpr (f == 95) {
            const int l2 = opaque(lane), n2 = l2 & 15;
            q2 = *LP(float, 4 * BLK + w * 4096 + (l2 >> 5) * 1024 + ((n2 >> 2) * 16 + n2) * 16 + (n2 & 3) * 4);
            SCHED();
          }
          if constexpr (f == 97) {
            const int l2 = opaque(lane);
            PIN(sm);
            const float mean = sm[0] * (1.f / 640.f);
            const float a = __builtin_amdgcn_rsqf(fmaxf(q2 * (1.f / 640.f) - mean * mean, 0.f) + 1e-5f);
            *LP(float, L_STA + w * 256 + l2 * 4) = (l2 & 16) ? -mean * a : a;
            CFENCE();
            SCHED();
          }
          if constexpr (f == 98) {
            const unsigned a = L_CK + w * 256 + opaque(lane) * 4;
            asm volatile("s_nop 1" : "+v"(ca));
            *LP(float, a) = ca[0];
            ctl_addc<C_CSK>();
            SCHED();
          }
          if constexpr (f + 1 >= 0 && f + 1 < 100) {
            if constexpr (i == 5 && more) TOUCH(la);
            else if constexpr (i == 9) TOUCH(xa[1][2 * nb]);
            else TOUCH(wf[(f + 1) % 4]);
            SCHED();
          }
        });
        rawlane = wb - (16384 + breg(9));
        PIN(rawlane);
      }
      lane = opaque(lane); c.lane = lane; n16 = lane & 15; g = lane >> 4; xvoff = lane * 16;
      STAMP(4);
      f32x4 a4[2], b4[2];
#pragma unroll
      for (int m = 0; m < 2; ++m) { a4[m] = *LP(f32x4, L_STA + w * 256 + m * 128 + g * 16); b4[m] = *LP(f32x4, L_STA + w * 256 + m * 128 + 64 + g * 16); }
      SCHED();
      asm volatile("s_nop 3");
      SCHED();
      const float ks = n16 < 8 ? LOG2E : 1.f, kd = n16 < 8 ? PHID : 0.f;
#pragma unroll
      for (int m = 0; m < 2; ++m) {
        const f32x4 al = a4[m] * LOG2E;
        acc[m][0] = acc[m][0] * al + PHID; acc[m][1] = acc[m][1] * al + PHID;
        acc[m][2] = acc[m][2] * (a4[m] * ks) + kd;
        acc[m][3] = acc[m][3] * a4[m]; acc[m][4] = acc[m][4] * a4[m];
#pragma unroll
        for (int t = 0; t < 5; ++t) PIN(acc[m][t]);
      }
      ctl_wait(c, C_CSK, 4);
      STAMP(48);
      float cs[5];
      {
        const unsigned a = L_CK + n16 * 4;
#pragma unroll
        for (int t = 0; t < 4; ++t) cs[t] = *LP(float, a + t * 256);
        cs[4] = *LP(float, a + 128) + *LP(float, a + 384);
        cs[0] *= LOG2E; cs[1] *= LOG2E; cs[2] *= ks;
      }
      v4u pk[3], vv[3];
#pragma unroll
      for (int t = 4; t >= 0; --t) {
        const f32x4 t0 = b4[0] * cs[t] + acc[0][t], t1 = b4[1] * cs[t] + acc[1][t];
        if (t >= 2) vv[t - 2] = v4u{pk2(t0[0], t0[1]), pk2(t0[2], t0[3]), pk2(t1[0], t1[1]), pk2(t1[2], t1[3])};
        if (t <= 2) { const v2u p0 = phi4(t0), p1 = phi4(t1); pk[t] = v4u{p0.x, p0.y, p1.x, p1.y}; }
      }
      if (n16 < 8) vv[0] = ones;
      const unsigned kp = (KVSLOT + w) * 10240 + opaque(lane) * 16;
      const f32x4 z4 = {0.f, 0.f, 0.f, 0.f};
#pragma unroll
      for (int ta = 2; ta >= 0; --ta)
#pragma unroll
        for (int tb = 0; tb < 3; ++tb)
          if (ta != 0 || tb != 2) *LP(f32x4, kp + (ta * 3 + tb) * 1024) = mfma(pk[ta], vv[tb], z4);
      ctl_addc<C_ARR1>();
    }
    STAMP(3);
    v2u pq[2][3];
    {
      static_assert(region(CH_Q) == 0 && region(CH_B - 1) == 0, "q chunks");
      f32x4 aq[2][3], ca;
      v4u wf[4], cf[2];
      v2u clo;
      const unsigned wb = rawlane;
      unsigned c0 = rawlane + oc, c4 = rawlane + o4;
      PIN(c0); PIN(c4);
      float sa[2], sb[2];
      f32x4 cs[3];
      ring_wait<CH_B - 1>(c);
      STAMP(43);
      SCHED();
      constexpr auto qoff = [](int f) { return ((f % 20) / 2) * 5120 + (f & 1) * 80 + 32 * (f / 20); };
      constexpr auto coff = [](int j) { return (j / 3) * 10240 + (j % 3 == 1 ? 5120 : 0); };
      static_for<-3, 60>([&](auto ic) AI {
        constexpr int f = decltype(ic)::v, fn = f + 3, s = f < 0 ? 0 : f % 20, t = f < 0 ? 0 : f / 20, jn = f + 2;
        v2u lo;
        if constexpr (f >= 0) { mm<1, s == 0>(aq[0][t], wf[f % 4], xa[0][s]); SCHED(); }
        if constexpr (fn < 60) { lo = tr1(wb, qoff(fn)); SCHED(); }
        if constexpr (f >= 0) { mm<1, s == 0>(aq[1][t], wf[f % 4], xa[1][s]); SCHED(); }
        if constexpr (fn < 60) { const v2u hi = tr1(wb, qoff(fn) + 2560); wf[fn % 4] = v4u{lo.x, lo.y, hi.x, hi.y}; SCHED(); }
        if constexpr (f >= 0 && f < 15) { mm<1, f == 0>(ca, f % 3 == 2 ? oneh : ones, cf[f & 1]); SCHED(); }
        if constexpr (jn >= 0 && jn < 15) { cf[jn & 1] = tr2(jn % 3 == 2 ? c4 : c0, coff(jn), 2560); SCHED(); }
        if constexpr (f == 16) {
          const int l2 = opaque(lane);
          PIN(ca);
          if ((l2 & 16) == 0) *LP(float, L_CQ + (l2 & 15) * 4 + w * 64 + (l2 >> 5) * 256) = ca[0];
          ctl_addc<C_CSQ>();
#pragma unroll
          for (int m = 0; m < 2; ++m) { sa[m] = *LP(float, L_STA + w * 256 + m * 128 + n16 * 4); sb[m] = *LP(float, L_STA + w * 256 + m * 128 + 64 + n16 * 4); }
          SCHED();
        }
      });
      STAMP(46);
      release<32>(c);
      {
        const char* xb0 = x + (size_t)(16 * mtB * 1280 + cc * 160);
        const unsigned rowoff = n16 * 1280 + g * 8;
#pragma unroll
        for (int t = 0; t < 5; ++t) xr[t] = *(const GAS v2u*)(xb0 + rowoff + 32 * t);
        SCHED();
      }
      ctl_wait(c, C_CSQ, 4);
      STAMP(47);
      {
        const unsigned a = L_CQ + g * 16;
        cs[0] = *LP(f32x4, a) + *LP(f32x4, a + 160);
        const f32x4 p1 = *LP(f32x4, a + 288);
        cs[1] = *LP(f32x4, a + 64) + *LP(f32x4, a + 224) + (g < 2 ? f32x4{0.f, 0.f, 0.f, 0.f} : p1);
        cs[2] = *LP(f32x4, a + 128) + p1 + *LP(f32x4, a + 352);
      }
#pragma unroll
      for (int m = 0; m < 2; ++m) { sa[m] *= LOG2E; sb[m] *= LOG2E; }
#pragma unroll
      for (int t = 0; t < 3; ++t) {
#pragma unroll
        for (int m = 0; m < 2; ++m) {
          pq[m][t] = phi4(aq[m][t] * sa[m] + (cs[t] * sb[m] + PHID));
          if (t == 2 && g >= 2) pq[m][t] = v2u{0u, 0u};
        }
      }
    }
    v4u qb[2][2];
#pragma unroll
    for (int m = 0; m < 2; ++m)
#pragma unroll
      for (int hh = 0; hh < 2; ++hh) { qb[m][hh] = v4u{pq[m][hh].x, pq[m][hh].y, pq[m][hh + 1].x, pq[m][hh + 1].y}; PIN(qb[m][hh]); }
    const int mt0 = 8 * pi + 2 * w;
    int oa = OFF_O + (mt0 * 20 + pq_) * 1024 + xvoff;
    int ob = OFF_O + (mt0 * 20 + 16 + (pq_ >> 2)) * 1024 + (pq_ & 3) * 256 + g * 20480 + n16 * 16;
    unsigned fa = L_RPK + xvoff;
    PIN(oa); PIN(ob); PIN(fa);
#pragma unroll
    for (int t = 0; t < 5; ++t) PIN(xr[t]);
    STAMP(5);
    ctl_wait(c, C_GO1, 1);
    if (!QUIET) release<CH_B>(c);
    STAMP(7);
    v4u ka_[4];
#pragma unroll
    for (int i = 0; i < 4; ++i) ka_[i] = *LP(v4u, fa + (i ^ 1) * 1024);
    f32x4 on[2][2][2];
    const f32x4 z4 = {0.f, 0.f, 0.f, 0.f};
#pragma unroll
    for (int i = 0; i < 4; ++i)
#pragma unroll
      for (int m = 0; m < 2; ++m) on[m][i >> 1][(i & 1) ^ 1] = mfma(ka_[i], qb[m][i >> 1], z4);
    unsigned tl[2];
#pragma unroll
    for (int m = 0; m < 2; ++m) {
      const float r0 = __builtin_amdgcn_rcpf(on[m][0][1][1] + 1e-6f), r1 = __builtin_amdgcn_rcpf(on[m][1][1][1] + 1e-6f);
      const v4u o = {pk2(on[m][0][0][0] * r0, on[m][0][0][1] * r0), pk2(on[m][0][0][2] * r0, on[m][0][0][3] * r0),
                     pk2(on[m][0][1][0] * r0, on[m][1][0][0] * r1), pk2(on[m][1][0][1] * r1, on[m][1][0][2] * r1)};
      tl[m] = pk2(on[m][1][0][3] * r1, on[m][1][1][0] * r1);
      st128(c, o, oa, m * 20480);
    }
    const v2u t0 = swap16(tl[0], tl[1]);
    const v2u u0 = swap32(t0.x, t0.x), u1 = swap32(t0.y, t0.y);
    if (lane < 32) st128(c, v4u{u0.x, u1.x, u0.y, u1.y}, ob, 0);
  }
  STAMP(8);
  unsigned hq[10];

  {
    f32x4 acc[2][5];
    {
#pragma unroll
      for (int t = 0; t < 5; ++t) { acc[0][t] = unpk4(xr[t].x, xr[t].y); PIN(acc[0][t]); }
    }
    const int sb = 5 * (cc >> 1) + 3 * (cc & 1);
    int ha = OFF_H + (mtB * 20 + sb) * 1024 + xvoff;
    int hb = OFF_H + (mtB * 20 + 5 * (cc >> 1) + 2) * 1024 + (cc & 1) * 512 + g * 128 + n16 * 16;
    unsigned wraw = (4 * g + ((lane >> 2) & 3)) * 160 + 8 * (lane & 3) + kb * 5120;
    PIN(ha); PIN(hb); PIN(wraw);
    DRAIN();
    const bool last = arrive<C_ARR2>(c, FL2 + pi * 16 + pq_);
    STAMP(6);
    if (PW & 1) ctl_wait(c, C_ARR2, 4);
    if (!(PW & 4)) flag_wait(c, FL2 + (hp >> 1) * 16 + n16, kb ? 16 : 10);
    else if (last) { flag_wait(c, FL2 + (hp >> 1) * 16 + n16, 16); ctl_addc<C_ARR2>(); }
    else ctl_wait(c, C_ARR2, 5);
    STAMP(9);
    v4u xb[2][10];
    const int xs = OFF_O + mtB * 20480 + kb * 10240, xm = kb ? -20480 : 20480;
    xload<10, 0, 0, PRE_B, 10>(c, xb, xvoff, xs, xm, rotB);
    if (QUIET) release<CH_B>(c);
    gemm_pass<5, 10, CH_B, 5, 10, 2, true>(c, dn, wraw, acc, xb, [&](auto ic) AI {
      constexpr int f = decltype(ic)::v;
      if constexpr (f >= 0 && f % 5 == 1 && f / 5 + PRE_B < 10) xload<10, 0, f / 5 + PRE_B, 1, 10>(c, xb, xvoff, xs, xm, rotB);
    });
    STAMP(1);
    ctl_wait(c, C_XB0 + (w >> 1), 1);
    pair_reduce(c, w, C_XB0, 1, acc, hq);
    STAMP(49);
    st128(c, v4u{hq[0], hq[1], hq[2], hq[3]}, ha, 0);
    st128(c, v4u{hq[4], hq[5], hq[6], hq[7]}, ha, 1024);
    const v2u ta = swap16(hq[8], hq[8]), tb = swap16(hq[9], hq[9]);
    if ((g & 1) == 0) st128(c, v4u{ta.x, tb.x, ta.y, tb.y}, hb, 0);
    float s1 = 0.f, s2 = 0.f;
#pragma unroll
    for (int i = 0; i < 10; ++i) { s1 = dot2(hq[i], ONE2, s1); s2 = dot2(hq[i], hq[i], s2); }
    s1 = sum_g(s1); s2 = sum_g(s2);
    if (g == 0) st64(c, v2u{asu(s1), asu(s2)}, n16 * 8, OFF_HS + mtB * 1024 + cc * 128);
  }
  if (!QUIET3) release<CH_C1>(c);
  STAMP(10);
  if (QUIET3) DRAIN(); else VMWAIT(1);
  const bool last3 = arrive<C_ARR3>(c, FL3 + hp * 8 + cc);
  if (QUIET3 == 2) release<Q3N>(c);
  ctl_wait(c, C_CS, 1);
  STAMP(33);
  if (PW & 2) ctl_wait(c, C_ARR3, 4);

  {
    const int mt0 = 8 * qi + 2 * w;
    f32x4 acc[2][4];
#pragma unroll
    for (int t = 0; t < 4; ++t) { acc[0][t] = *LP(f32x4, L_CSUM + t * 64 + g * 16); PIN(acc[0][t]); }
    if (!(PW & 8)) flag_wait(c, FL3 + qi * 16 + (w >> 1) * 8 + n16, 8);
    else if (last3) { flag_wait(c, FL3 + qi * 16 + n16, 16); ctl_addc<C_ARR3>(); }
    else ctl_wait(c, C_ARR3, 5);
    if (QUIET3 == 1 || (QUIET3 == 2 && Q3N < 45)) release<CH_C1>(c);
    STAMP(11);
    v2u st[2][2];
#pragma unroll
    for (int m = 0; m < 2; ++m)
#pragma unroll
      for (int i = 0; i < 2; ++i) st[m][i] = ld64(c, lane * 8, OFF_HS + (mt0 + m) * 1024 + i * 512);
    v4u xh[2][20];
    xload<20, 0, 0, PRE_C, 20>(c, xh, xvoff, OFF_H + mt0 * 20480, 20480, rotC);
    float mneg[2], s2[2];
#pragma unroll
    for (int m = 0; m < 2; ++m) {
      mneg[m] = sum_g(asf(st[m][0].x) + asf(st[m][1].x)) * (-1.f / 640.f);
      s2[m] = asf(st[m][0].y) + asf(st[m][1].y);
    }
#pragma unroll
    for (int t = 0; t < 4; ++t) { acc[1][t] = acc[0][t] * mneg[1]; acc[0][t] = acc[0][t] * mneg[0]; PIN(acc[0][t]); PIN(acc[1][t]); }
    float sd[2], c2[2];
    gemm_pass<4, 20, CH_C1, 10, 20, 0, false>(c, dn, xvoff, acc, xh, [&](auto ic) AI {
      constexpr int f = decltype(ic)::v;
      if constexpr (f >= 0 && f % 4 == 1 && f / 4 + PRE_C < 20) xload<20, 0, f / 4 + PRE_C, 1, 20>(c, xh, xvoff, OFF_H + mt0 * 20480, 20480, rotC);
      if constexpr (f == 2 || f == 6) {
        constexpr int m = f == 6;
        const float var = fmaxf(sum_g(s2[m]) * (1.f / 640.f) - mneg[m] * mneg[m], 0.f) + 1e-5f;
        const float rs = __builtin_amdgcn_rsqf(var);
        sd[m] = var * rs;
        c2[m] = -1.4426950408889634f * rs;
        PIN(sd[m]); PIN(c2[m]);
      }
    });
    STAMP(12);
    const auto silu2 = [&](int m, const f32x4& a) AI {
      float y[4];
#pragma unroll
      for (int i = 0; i < 4; ++i) {
        const float e = __builtin_amdgcn_exp2f(a[i] * c2[m]);
        y[i] = a[i] * __builtin_amdgcn_rcpf(fmaf(e, sd[m], sd[m]));
      }
      return v2u{pk2(y[0], y[1]), pk2(y[2], y[3])};
    };
    const int fb = OFF_F + mt0 * 55296;
    f32x4 ac2[2][3];
#pragma unroll
    for (int t = 0; t < 3; ++t) ac2[0][t] = *LP(f32x4, L_CSUM + (4 + t) * 64 + g * 16);
#pragma unroll
    for (int m = 0; m < 2; ++m)
#pragma unroll
      for (int v = 0; v < 2; ++v) {
        const v2u a = silu2(m, acc[m][2 * v]), d = silu2(m, acc[m][2 * v + 1]);
        st128(c, v4u{a.x, a.y, d.x, d.y}, xvoff, fb + m * 55296 + (2 * qj + v) * 1024);
      }
#pragma unroll
    for (int t = 0; t < 3; ++t) { ac2[1][t] = ac2[0][t] * mneg[1]; ac2[0][t] = ac2[0][t] * mneg[0]; PIN(ac2[0][t]); PIN(ac2[1][t]); }
    STAMP(2);
    gemm_pass<3, 20, CH_C2, 10, 20, 0, false>(c, dn, xvoff, ac2, xh, [&](auto ic) AI {
      if constexpr (decltype(ic)::v == F4A) { asm volatile("s_waitcnt vmcnt(%0)" :: "i"((F4A + 1) / 10) : "memory"); ctl_addc<C_ARR4A>(); }
    });
    STAMP(13);
#pragma unroll
    for (int m = 0; m < 2; ++m) {
      const v2u a = silu2(m, ac2[m][0]), d = silu2(m, ac2[m][1]);
      st128(c, v4u{a.x, a.y, d.x, d.y}, xvoff, fb + m * 55296 + (32 + qj) * 1024);
    }
    if ((qj & 3) != 3) {
      const v2u a = silu2(0, ac2[0][2]), d = silu2(1, ac2[1][2]);
      const v2u ta = swap16(a.x, d.x), tb = swap16(a.y, d.y);
      const int l = 3 * (qj >> 2) + (qj & 3);
      st128(c, v4u{ta.x, tb.x, ta.y, tb.y}, (g & 1) * 55296 + (g >> 1) * 256 + n16 * 16, fb + (48 + (l >> 1)) * 1024 + (l & 1) * 512);
    }
  }
  STAMP(14);

  {
    const int xs = OFF_F + mtB * 55296, xm = kb ? -55296 : 55296;
    const bool early = ctl_read(C_GO4A) != 0u;
    CFENCE();
    if (!early) { DRAIN(); ctl_addc<C_ARR4B>(); ctl_wait(c, C_GO4A, 1); }
    v4u xf[2][20];
    xload<20, 0, 0, PRE_D, 16>(c, xf, xvoff, xs + kb * 16384, xm, rotD1);
    f32x4 acc[2][5];
#pragma unroll
    for (int t = 0; t < 5; ++t) { acc[0][t] = unpk4(hq[2 * t], hq[2 * t + 1]); PIN(acc[0][t]); }
    if (early) { asm volatile("s_waitcnt vmcnt(%0)" :: "i"(2 * PRE_D) : "memory"); ctl_addc<C_ARR4B>(); }
    STAMP(16);
    gemm_pass<5, 27, CH_D, 5, 20, 2, false>(c, dn, xvoff + kb * 5120, acc, xf, [&](auto ic) AI {
      constexpr int f = decltype(ic)::v, sn = f / 5 + PRE_D;
      if constexpr (f >= 0 && f % 5 == 1 && sn < 27) {
        if constexpr (sn == 16) {
          STAMP(17);
          ctl_wait(c, C_GO4B, 1);
          STAMP(18);
        }
        if constexpr (sn < 16) xload<20, 0, sn, 1, 16>(c, xf, xvoff, xs + kb * 16384, xm, rotD1);
        else xload<20, 16, sn - 16, 1, 11>(c, xf, xvoff, xs + 32768 + kb * 11264, xm, rotD2);
      }
    });
    STAMP(19);
    unsigned y[10];
    pair_reduce(c, w, C_XD0, 0, acc, y);
    const int l2 = opaque(lane);
    char* ob = out + (size_t)(16 * mtB * 1280 + cc * 160);
    const unsigned ro = (l2 & 15) * 1280 + (l2 & 16) * 2 + (l2 & 32) / 2;
#pragma unroll
    for (int u = 0; u < 2; ++u) {
      const v2u a = swap16(y[4 * u], y[4 * u + 2]), d = swap16(y[4 * u + 1], y[4 * u + 3]);
      *(GAS v4u*)(ob + ro + 64 * u) = v4u{a.x, d.x, a.y, d.y};
    }
    *(GAS v2u*)(ob + ((l2 & 15) * 1280 + (l2 >> 4) * 8 + 128)) = v2u{y[8], y[9]};
  }
  STAMP(15);
}

extern "C" void kernel_launch(void* const* d_in, const int* in_sizes, int n_in, void* d_out, int out_size, void* d_ws, size_t ws_size,
                              hipStream_t stream) {
  (void)hipFuncSetAttribute((const void*)mk_kernel, hipFuncAttributeMaxDynamicSharedMemorySize, LDS_BYTES);
  mk_kernel<<<256, 512, LDS_BYTES, stream>>>((const char*)d_in[0], (const char*)d_in[1], (const char*)d_in[2], (const char*)d_in[4],
                                             (const char*)d_in[5], (char*)d_out, (char*)d_ws);
}
```

```cpp
#include <hip/hip_runtime.h>
#include <stdint.h>

typedef unsigned v4u __attribute__((ext_vector_type(4)));
typedef unsigned v2u __attribute__((ext_vector_type(2)));
typedef short v4s __attribute__((ext_vector_type(4)));
typedef float f32x4 __attribute__((ext_vector_type(4)));
typedef float f32x2 __attribute__((ext_vector_type(2)));
typedef __bf16 bf16x2 __attribute__((ext_vector_type(2)));
typedef __bf16 bf16x8 __attribute__((ext_vector_type(8)));
typedef unsigned long long u64;
typedef __amdgpu_buffer_rsrc_t rsrc_t;
#define LAS __attribute__((address_space(3)))
#define GAS __attribute__((address_space(1)))
#define DI __device__ __forceinline__
#define AI __attribute__((always_inline))
#define RLX_AGENT __ATOMIC_RELAXED, __HIP_MEMORY_SCOPE_AGENT
#define SCHED() __builtin_amdgcn_sched_barrier(0)
#define CFENCE() asm volatile("" ::: "memory")
#define DRAIN() asm volatile("s_waitcnt vmcnt(0)" ::: "memory")
#define VMWAIT(n) asm volatile("s_waitcnt vmcnt(" #n ")" ::: "memory")
#define LGKM0() asm volatile("s_waitcnt lgkmcnt(0)" ::: "memory")
#define ENDPGM() asm volatile("s_endpgm" ::: "memory")
#define PIN(v) asm volatile("" : "+v"(v))
#define LP(T, a) ((LAS T*)(uintptr_t)(unsigned)(a))
constexpr int SC1 = 16;
constexpr unsigned ONE2 = 0x3f803f80u;

#define DEP_A 12
#define DEP_P 10
#define LPRIO 0
#define REL_B 5
#define REL_C 3
#define F4A 18
#define RPK_F 21
#define RPK_S 35
#define RPK_PUB 2
#define PQX 0
#define CMAP 1
#define UMAX 216
#define CAPL 1
#define TCH 7
#define TCH_T 0
#define LATE_L 1
#define LATE_S 35
#define QUIET3 2
#define WGF 1
#define Q3N 45
#define BMAP 0
#define ROTS 0
#define PW 12
#define ROTT 0x10876532u
#define PADT 1
#define HOLD 0
#define QUIET 1
#define PSL 0
#define PD 0
#define XPRE 1
#define ROTA 1
#define STR_(x) #x
#define STR(x) STR_(x)

#define ROT 1
#define PRE_B 6
#define PRE_C 8
#define PRE_D 8

constexpr int OFF_EPOCH = 0;
constexpr int OFF_FLAGS = 4096;
constexpr int OFF_CS = 655360;
constexpr int OFF_HS = 819200;
constexpr int OFF_KVP = 1 << 20;
constexpr int KVP_STR = 1920;
constexpr int OFF_WB = 2 << 20;
constexpr int OFF_WC = OFF_WB + 819200;
constexpr int OFF_WD = OFF_WC + 2293760;
constexpr int OFF_O = OFF_WD + 2211840;
constexpr int OFF_H = OFF_O + 2621440;
constexpr int OFF_F = OFF_H + 2621440;
constexpr int WS_BYTES = OFF_F + 7077888;
constexpr int OFF_PROF = 32 << 20;
enum { FLR = 0, FL1 = 256, FL2 = 512, FL3 = 768, FL4A = 1024, FL4B = 1280, FL_N = 1536 };
constexpr unsigned EPOCH_STEP = 0x9E3779B6u;

constexpr int RCH = 13;
constexpr int L_SCR = RCH * 10240;
constexpr int L_RPK = L_SCR + 20480;
constexpr int L_CSUM = L_RPK + 4096;
constexpr int L_DONE = L_CSUM + 512;
constexpr int L_LAND = L_DONE + 1024;
constexpr int L_FLG = L_LAND + 512;
constexpr int L_STA = L_FLG + 1024;
constexpr int L_CK = L_STA + 1024;
constexpr int L_CQ = L_CK + 1024;
constexpr int L_CTL = L_CQ + 512;
constexpr int LDS_BYTES = L_CTL + 128;
static_assert(LDS_BYTES <= 163840, "lds");
enum { C_CAP = 0, C_CSK, C_CSQ, C_PUB, C_CS, C_ARR1, C_GO1, C_XB0, C_XB1, C_XD0, C_XD1, C_ARR2, C_ARR3, C_ARR4A, C_GO4A, C_ARR4B, C_GO4B };

constexpr int CH_Q = 30, CH_B = 35, CH_C1 = 45, CH_C2 = 53, CH_D = 59, CH_END = 86;
constexpr int BLK = 26624;
static_assert(5 * BLK == RCH * 10240, "ring");
constexpr int needp(int ch) { return ch + 1; }
constexpr int slot(int ch) { return (ch - CH_Q) % RCH; }
constexpr int KVSLOT = 9;

struct Ctx {
  rsrc_t rs, rt;
  unsigned dm;
  GAS char* ws;
  unsigned ep;
  int lane;
  unsigned budget;
};

DI f32x4 mfma(v4u a, v4u b, f32x4 c) {
  return __builtin_amdgcn_mfma_f32_16x16x32_bf16(__builtin_bit_cast(bf16x8, a), __builtin_bit_cast(bf16x8, b), c, 0, 0, 0);
}
DI float asf(unsigned u) { return __builtin_bit_cast(float, u); }
DI unsigned asu(float f) { return __builtin_bit_cast(unsigned, f); }
DI unsigned pk2(float a, float b) { f32x2 f = {a, b}; return __builtin_bit_cast(unsigned, __builtin_convertvector(f, bf16x2)); }
DI float bflo(unsigned u) { return asf(u << 16); }
DI float bfhi(unsigned u) { return asf(u & 0xffff0000u); }
DI f32x4 unpk4(unsigned a, unsigned b) { return f32x4{bflo(a), bfhi(a), bflo(b), bfhi(b)}; }
DI float dot2(unsigned a, unsigned b, float c) {
  return __builtin_amdgcn_fdot2_f32_bf16(__builtin_bit_cast(bf16x2, a), __builtin_bit_cast(bf16x2, b), c, false);
}
DI f32x4 add4(f32x4 a, f32x4 b) {
  f32x4 r;
#pragma unroll
  for (int i = 0; i < 4; ++i) asm("v_add_f32 %0, %1, %2" : "=v"(r[i]) : "v"(a[i]), "v"(b[i]));
  return r;
}
DI v4u ld128(const Ctx& c, int voff, int soff) { return __builtin_amdgcn_raw_buffer_load_b128(c.rs, voff, soff, SC1); }
DI v2u ld64(const Ctx& c, int voff, int soff) { return __builtin_amdgcn_raw_buffer_load_b64(c.rs, voff, soff, SC1); }
DI void st128(const Ctx& c, v4u v, int voff, int soff) { __builtin_amdgcn_raw_buffer_store_b128(v, c.rs, voff + soff, 0, SC1); }
DI void st64(const Ctx& c, v2u v, int voff, int soff) { __builtin_amdgcn_raw_buffer_store_b64(v, c.rs, voff, soff, SC1); }
DI void st32(const Ctx& c, unsigned v, int voff, int soff) { __builtin_amdgcn_raw_buffer_store_b32(v, c.rs, voff, soff, SC1); }
DI u64 now() { return __builtin_amdgcn_s_memrealtime(); }

DI void ctl_add(int i) {
  asm volatile("v_mov_b32 v254, %0\n\tv_mov_b32 v255, %1\n\ts_mov_b64 exec, 1\n\tds_add_u32 v254, v255\n\ts_mov_b64 exec, -1"
               :: "s"(L_CTL + 4 * i), "i"(L_CTL) : "memory", "v254", "v255");
}
template <int I> DI void ctl_addc() {
  asm volatile("v_mov_b32 v254, %0\n\ts_mov_b64 exec, 1\n\tds_add_u32 v254, v254 offset:%1\n\ts_mov_b64 exec, -1"
               :: "i"(L_CTL), "i"(4 * I) : "memory", "v254");
}
template <int SLP = 0> DI void ctl_wait(Ctx& c, int i, int n) {
  unsigned t;
  asm volatile(
      "v_mov_b32 v254, %2\n"
      ".Lw%=:\n\t"
      ".if %4\n\ts_sleep %4\n\t.endif\n\t"
      "ds_read_b32 v255, v254\n\t"
      "s_sub_u32 %0, %0, 1\n\t"
      "s_max_i32 %0, %0, 0\n\t"
      "s_waitcnt lgkmcnt(0)\n\t"
      "v_readfirstlane_b32 %1, v255\n\t"
      "s_cmp_lt_u32 %1, %3\n\t"
      "s_cselect_b32 %1, %0, 0\n\t"
      "s_cmp_lg_u32 %1, 0\n\t"
      "s_cbranch_scc1 .Lw%="
      : "+{s71}"(c.budget), "=&s"(t) : "s"(L_CTL + 4 * i), "s"(n * L_CTL), "i"(SLP) : "memory", "scc", "v254", "v255");
}
DI unsigned ctl_read(int i) { return __builtin_amdgcn_readfirstlane(*LP(volatile unsigned, L_CTL + 4 * i)); }
DI void flag_set(const Ctx& c, int idx) {
  asm volatile("v_mov_b32 v255, %1\n\ts_mov_b64 exec, 1\n\tbuffer_store_dword v255, off, %0, %2 sc1\n\ts_mov_b64 exec, -1"
               :: "s"(c.rs), "s"(c.ep), "s"(OFF_FLAGS + idx * 128) : "memory", "v255");
}
template <int I> DI bool arrive(const Ctx& c, int idx) {
  unsigned n;
  asm volatile("v_mov_b32 v254, %1\n\ts_mov_b64 exec, 1\n\tds_add_rtn_u32 v255, v254, v254 offset:%2\n\ts_mov_b64 exec, -1\n\ts_waitcnt lgkmcnt(0)\n\tv_readfirstlane_b32 %0, v255"
               : "=s"(n) : "i"(L_CTL), "i"(4 * I) : "memory", "v254", "v255");
  if (n == 3 * L_CTL) flag_set(c, idx);
  return n == 3 * L_CTL;
}
DI bool flag_test(const Ctx& c, int idx, int nl) {
  unsigned v = c.ep;
  if (c.lane < nl) v = __hip_atomic_load((GAS unsigned*)(c.ws + OFF_FLAGS + idx * 128), RLX_AGENT);
  const bool ok = !__any(v != c.ep);
  CFENCE();
  return ok;
}
DI void flag_wait(const Ctx& c, int off, int nl) {
  unsigned n = 1u << 17;
  while (!flag_test(c, off, nl) && --n != 0) {}
}
DI void flag_copy(Ctx& c, int row, int idx0, u64 mask) {
  asm volatile(
      "s_mov_b32 m0, %3\n\t"
      "v_lshl_add_u32 v254, %5, 2, %3\n\t"
      "s_mov_b64 exec, %6\n"
      ".Lf%=:\n\t"
      ".if " STR(PSL) "\n\ts_sleep " STR(PSL) "\n\t.endif\n\t"
      "buffer_load_dword %7, %1, %4 offen sc1 lds\n\t"
      "s_waitcnt vmcnt(" STR(PD) ")\n\t"
      "ds_read_b32 v255, v254\n\t"
      "s_sub_u32 %0, %0, 64\n\t"
      "s_cbranch_scc1 .Lg%=\n\t"
      "s_waitcnt lgkmcnt(0)\n\t"
      "v_cmp_ne_u32 vcc, %2, v255\n\t"
      "s_and_b64 exec, exec, vcc\n\t"
      "s_cbranch_scc1 .Lf%=\n\t"
      "s_branch .Le%=\n"
      ".Lg%=:\n\t"
      "s_mov_b32 %0, 0\n"
      ".Le%=:\n\t"
      "s_mov_b64 exec, -1\n\t"
      "s_waitcnt vmcnt(0) lgkmcnt(0)"
      : "+s"(c.budget) : "s"(c.rs), "s"(c.ep), "s"(L_FLG + row * 256), "s"(OFF_FLAGS + idx0 * 128), "v"(c.lane), "s"(mask), "v"(c.lane * 128)
      : "memory", "scc", "vcc", "v254", "v255");
}
template <int ROW> DI void flag_seen(Ctx& c, unsigned dn, u64 mask) {
  asm volatile(
      ".Lf%=:\n\t"
      "ds_read_b32 v255, %1 offset:%4\n\t"
      "s_sub_u32 %0, %0, 1\n\t"
      "s_cbranch_scc1 .Lg%=\n\t"
      "s_waitcnt lgkmcnt(0)\n\t"
      "v_cmp_ne_u32 vcc, %2, v255\n\t"
      "s_and_b64 vcc, vcc, %3\n\t"
      "s_cbranch_scc1 .Lf%=\n\t"
      ".subsection 1\n"
      ".Lg%=:\n\t"
      "s_mov_b32 %0, 0\n\t"
      "s_waitcnt lgkmcnt(0)\n\t"
      "s_branch .Le%=\n\t"
      ".subsection 0\n"
      ".Le%=:"
      : "+{s71}"(c.budget) : "v"(dn), "s"(c.ep), "s"(mask), "i"(L_FLG + ROW * 256 - L_LAND - 128) : "memory", "scc", "vcc", "v255");
}
DI void done_wait(Ctx& c, unsigned n) {
  unsigned k = 0;
  while (__any(*LP(volatile unsigned, L_DONE + c.lane * 16) < n) && ++k < (1u << 18)) __builtin_amdgcn_s_sleep(2);
  CFENCE();
}
DI void touch(const char* p, int bytes, int off, int n, int pitch) {
  const rsrc_t r = __builtin_amdgcn_make_buffer_rsrc((void*)p, 0, bytes, 0x00020000);
  asm volatile(".Lt%=:\n\t"
               "s_buffer_load_dwordx2 s[98:99], %2, %0 offset:60\n\t"
               "s_add_u32 %0, %0, %3\n\t"
               "s_sub_u32 %1, %1, 1\n\t"
               "s_cmp_gt_i32 %1, 0\n\t"
               "s_cbranch_scc1 .Lt%=\n\t"
               "s_waitcnt lgkmcnt(0)"
               : "+s"(off), "+s"(n) : "s"(r), "s"(pitch) : "memory", "scc", "s98", "s99");
}
DI void land_wait(const Ctx& c, unsigned n) {
  unsigned k = 0;
  while (__any(*LP(volatile unsigned, L_LAND + 128 + c.lane * 4) < n) && ++k < (1u << 16)) __builtin_amdgcn_s_sleep(4);
  CFENCE();
}
#define STAMP(i) do {} while (0)
#define STL(i) do {} while (0)
#define DUMPL(...) do {} while (0)

DI v2u swap16(unsigned a, unsigned b) { return __builtin_amdgcn_permlane16_swap(a, b, false, false); }
DI v2u swap32(unsigned a, unsigned b) { return __builtin_amdgcn_permlane32_swap(a, b, false, false); }
DI float sum_g(float v) {
  v2u r = swap16(asu(v), asu(v));
  v = asf(r.x) + asf(r.y);
  r = swap32(asu(v), asu(v));
  return asf(r.x) + asf(r.y);
}
DI v2u tr1(unsigned a, int off) { return __builtin_bit_cast(v2u, __builtin_amdgcn_ds_read_tr16_b64_v4i16(LP(v4s, a + off))); }
DI v4u tr2(unsigned a, int off, int hioff) {
  const v2u lo = tr1(a, off), hi = tr1(a, off + hioff);
  return v4u{lo.x, lo.y, hi.x, hi.y};
}

DI int gam0(int j) { return 27 * (j >> 2) + 7 * (j & 3); }

template <int CH> DI void ring_wait(Ctx& c) {
  asm volatile("s_movk_i32 s70, %1\n\ts_call_b64 s[72:73], .Lmk_ring"
               : "+{s71}"(c.budget) : "i"(needp(CH)) : "memory", "scc", "vcc", "s70", "s72", "s73", "v254", "v255");
}
template <int CH> DI void ring_cmp(unsigned la) { asm volatile("v_cmp_gt_u32 vcc, %1, %0" :: "v"(la), "i"(needp(CH)) : "vcc"); }
template <int CH> DI void ring_br(Ctx& c) {
  asm volatile("s_cbranch_vccnz .Lrs%=\n\t.subsection 1\n.Lrs%=:\n\ts_movk_i32 s70, %1\n\ts_call_b64 s[72:73], .Lmk_ring\n\ts_branch .Lrb%=\n\t.subsection 0\n.Lrb%=:"
               : "+{s71}"(c.budget) : "i"(needp(CH)) : "memory", "scc", "vcc", "s70", "s72", "s73", "v254", "v255");
}
#define TOUCH(x) asm volatile("" :: "v"(x))
template <int N> DI void release(const Ctx& c) {
  asm volatile("s_mov_b32 m0, %1\n\ts_movk_i32 s70, %2\n\tbuffer_load_dword off, %0, s70 lds" :: "s"(c.rt), "s"(c.dm), "i"(4 * N) : "memory", "s70");
}
constexpr int upto(int ch) {
  if (ch == CH_Q) return CH_B - 1;
  if (ch == CH_B) return CH_C1 - 1;
  if (ch == CH_C1) return CH_C2 - 1;
  if (ch == CH_C2) return CH_D - 2;
  if (ch == CH_D - 1) return ch;
  if (ch == CH_D) return CH_D + 6;
  if (ch == CH_D + 7) return CH_D + 12;
  if (ch >= CH_D + 13 && (ch - CH_D - 13) % 2 == 0) return ch + 1;
  return -1;
}
constexpr bool released(int n) {
  if (n + RCH > CH_END + 1) return false;
  if (n <= CH_C1) return n == CH_B + REL_B;
  if (n <= CH_D) return n >= CH_C1 + REL_C;
  return (n - CH_B) % 2 == 0;
}
template <int I> struct IC { static constexpr int v = I; };
template <int B, int E, class Fn> DI void static_for(Fn&& fn) {
  if constexpr (B < E) { fn(IC<B>{}); static_for<B + 1, E>(fn); }
}
template <int D> DI void bump(unsigned& b) { if constexpr (D != 0) asm volatile("v_add_u32 %0, %1, %0" : "+v"(b) : "i"(D)); }
constexpr int breg(int b) { return ((b % 5) / 2) * 2 * BLK; }
constexpr int bin(int b) { return ((b % 5) % 2) * BLK; }
constexpr int region(int ch) { return (slot(ch) / 6) * 61440; }
constexpr int inreg(int ch) { return (slot(ch) % 6) * 10240; }

template <int TL, bool Z> DI void mm(f32x4& acc, const v4u& wf, const v4u& x) {
  if constexpr (Z && TL) asm volatile("v_mfma_f32_16x16x32_bf16 %0, %1, %2, 0" : "=&v"(acc) : "v"(wf), "v"(x));
  else if constexpr (Z) asm volatile("v_mfma_f32_16x16x32_bf16 %0, %2, %1, 0" : "=&v"(acc) : "v"(wf), "v"(x));
  else if constexpr (TL) asm volatile("v_mfma_f32_16x16x32_bf16 %0, %1, %2, %0" : "+v"(acc) : "v"(wf), "v"(x));
  else asm volatile("v_mfma_f32_16x16x32_bf16 %0, %2, %1, %0" : "+v"(acc) : "v"(wf), "v"(x));
}
template <int NT, int NKS, int CH0, int FPC, int XW, int ZM, bool RAW, class Hook>
DI void gemm_pass(Ctx& c, unsigned dn, unsigned wb, f32x4 (&acc)[2][NT], v4u (&x)[2][XW], Hook&& hook) {
  constexpr int LA = 3, WBN = LA + 1, NF = NKS * NT;
  v4u wf[WBN];
  unsigned la;
  bump<region(CH0)>(wb);
  SCHED();
  static_for<-LA, NF>([&](auto ic) AI {
    constexpr int f = decltype(ic)::v, fn = f + LA;
    constexpr int s = f < 0 ? 0 : f / NT, t = f < 0 ? 0 : f % NT;
    constexpr int ch = CH0 + fn / FPC, off = inreg(ch) + (fn % FPC) * (RAW ? 32 : 1024);
    v2u lo;
    constexpr auto chk = [](int i) { return i > 0 && i < NF && i % FPC == 0 && upto(CH0 + i / FPC) >= 0; };
    constexpr bool rel = f >= 0 && (f + 1) % FPC == 0 && released(CH0 + (f + 1) / FPC);
    if constexpr (f >= 0) { mm<1, (ZM & 1) && s == 0>(acc[0][t], wf[f % WBN], x[0][s % XW]); SCHED(); }
    if constexpr (fn < NF) {
      if constexpr (fn == 0) ring_wait<upto(ch)>(c);
      if constexpr (chk(fn)) ring_br<upto(ch)>(c);
      if constexpr (RAW) lo = tr1(wb, off);
      else wf[fn % WBN] = *LP(v4u, wb + off);
      SCHED();
    }
    if constexpr (f >= 0) { mm<1, (ZM & 2) && s == 0>(acc[1][t], wf[f % WBN], x[1][s % XW]); SCHED(); }
    if constexpr (RAW && fn < NF) { const v2u hi = tr1(wb, off + 2560); wf[fn % WBN] = v4u{lo.x, lo.y, hi.x, hi.y}; SCHED(); }
    if constexpr (fn + 1 < NF && (fn + 1) % FPC == 0) bump<region(ch + 1) - region(ch)>(wb);
    if constexpr (rel) release<CH0 + (f + 1) / FPC>(c);
    if constexpr (chk(fn + 4)) la = *LP(unsigned, dn);
    hook(ic);
    SCHED();
    if constexpr (chk(fn + 1)) { ring_cmp<upto(CH0 + (fn + 1) / FPC)>(la); SCHED(); }
  });
  asm volatile("s_nop 7");
  SCHED();
}
template <int XW, int S0, int S1, int N, int LEN> DI void xload(const Ctx& c, v4u (&x)[2][XW], int xvoff, int xsoff, int xmt, int rot) {
#pragma unroll
  for (int s = S1; s < S1 + N; ++s) {
    const int o = xsoff + (rot + s >= LEN ? rot + s - LEN : rot + s) * 1024;
    x[0][(S0 + s) % XW] = ld128(c, xvoff, o);
    x[1][(S0 + s) % XW] = ld128(c, xvoff, o + xmt);
  }
  SCHED();
}
DI int opaque(int v) { asm volatile("" : "+v"(v)); return v; }
constexpr float LOG2E = 1.4426950408889634f, PHID = 1.4426950408889634e-6f;
DI v2u phi4(f32x4 u) {
  f32x4 e, m;
#pragma unroll
  for (int i = 0; i < 4; ++i) { e[i] = __builtin_amdgcn_fmed3f(__builtin_amdgcn_exp2f(u[i]), 0.f, 1.f); m[i] = fmaxf(u[i], PHID); }
  const f32x4 y = m * 0.6931471805599453f + e;
  return v2u{pk2(y[0], y[1]), pk2(y[2], y[3])};
}

DI void pair_reduce(Ctx& c, int w, int cx, int n0, f32x4 (&acc)[2][5], unsigned (&y)[10]) {
  const unsigned lo = opaque(c.lane) * 16 + L_SCR;
  LAS f32x4* mine = LP(f32x4, lo + w * 5120);
  LAS f32x4* other = LP(f32x4, lo + (w ^ 1) * 5120);
#pragma unroll
  for (int t = 0; t < 5; ++t) mine[t * 64] = acc[1][t];
  ctl_add(cx + (w >> 1));
  ctl_wait(c, cx + (w >> 1), n0 + 2);
#pragma unroll
  for (int t = 0; t < 5; ++t) {
    const f32x4 o = acc[0][t] + other[t * 64];
    y[2 * t] = pk2(o[0], o[1]); y[2 * t + 1] = pk2(o[2], o[3]);
  }
}
DI int mod10(int v) { return v >= 20 ? v - 20 : v >= 10 ? v - 10 : v; }

__global__ void __launch_bounds__(512, 2)
mk_kernel(const char* __restrict__ x, const char* __restrict__ wqkv, const char* __restrict__ wo, const char* __restrict__ w1,
          const char* __restrict__ w2, char* __restrict__ out, char* ws) {
  u64 cpc; unsigned clen, toff;
  asm volatile(
      "s_getpc_b64 %0\n"
      ".Lpc%=:\n\t"
      "s_mov_b32 %1, .Lend%=-.Lpc%=-4\n\t"
      "s_mov_b32 %2, .Lmk_tab-.Lpc%=\n\t"
      ".subsection 1\n"
      ".Lmk_ring:\n\t"
      "v_mov_b32 v254, %3\n\t"
      "ds_read2_b32 v[254:255], v254 offset1:64\n\t"
      "s_sub_u32 s71, s71, 1\n\t"
      "s_cbranch_scc1 .Lmk_give\n\t"
      "s_waitcnt lgkmcnt(0)\n\t"
      "v_min_u32 v254, v254, v255\n\t"
      "v_cmp_gt_u32 vcc, s70, v254\n\t"
      "s_cbranch_vccnz .Lmk_ring\n\t"
      "s_setpc_b64 s[72:73]\n"
      ".Lmk_give:\n\t"
      "s_waitcnt lgkmcnt(0)\n\t"
      "s_mov_b32 s71, 0\n\t"
      "s_setpc_b64 s[72:73]\n\t"
      ".subsection 2\n"
      ".Lend%=:\n\t"
      ".fill 128, 4, 0xBF800000\n\t"
      ".subsection 0"
      : "=s"(cpc), "=s"(clen), "=s"(toff) : "i"(L_LAND));
  asm volatile("" :: "s"(x), "s"(wqkv), "s"(wo), "s"(w1), "s"(w2), "s"(out), "s"(ws));
  const int tid = threadIdx.x, w = __builtin_amdgcn_readfirstlane(tid >> 6);
  int lane = tid & 63;
  const int b = blockIdx.x, xc = b & 7, sx = b >> 3;
  const int pi = 4 * (xc >> 1) + (sx >> 3), pj = (xc & 1) * 8 + (sx & 7);
  const int pq_ = pj ^ PQX;
  const int hp = BMAP ? 8 * (xc >> 1) + (sx >> 2) : 4 * xc + (sx >> 3), cc = BMAP ? 4 * (xc & 1) + (sx & 3) : sx & 7;
  const int qi = CMAP ? pi : 2 * xc + (sx >> 4), qj = CMAP ? pj : sx & 15;
  const int r8 = BMAP ? sx >> 2 : sx & 7, r4 = BMAP ? sx & 3 : sx >> 3;
  const int rotB = mod10(((5 * r8) >> 2) + (BMAP || (ROTS & 1) ? (5 * r4) >> 1 : 0)), rotC = 5 * ((qj + ((ROTS & 2) ? sx >> 3 : 0)) & 3), rotD1 = (2 * r8 + 5 * r4) & 15;
  const int rd2 = ((11 * r8) >> 3) + 3 * r4, rotD2 = rd2 >= 11 ? rd2 - 11 : rd2;
  int n16 = lane & 15, g = lane >> 4;
  Ctx c;
  c.lane = lane;
  c.rt = __builtin_amdgcn_make_buffer_rsrc((void*)(cpc + toff), 0, 512, 0x00020000);
  if (__builtin_expect((w >> 1) == 2, 1)) {
    __builtin_amdgcn_s_setprio(LPRIO);
    const int rot = ROTA == 0 ? 0 : ROTA == 2 ? 5 * (sx & 1) : mod10(((ROTT >> (4 * (sx & 7))) & 15) + ((5 * (sx >> 3)) >> 1) + (xc >> 1));
    const int Lw = w - 4;
    const int ln = opaque(lane), xr = ln >> 3;
    int xv[4], ka[5], qa[5];
    bool hi_[5];
    xv[0] = xr * 1280 + (((ln & 7) ^ (xr >> 1)) * 16) + 3072; xv[1] = (xv[0] ^ 64) + 10240 - 1024; xv[2] = xv[0] - 2048; xv[3] = xv[1] - 2048;
    const auto raw_addr = [&](int (&a)[5], int lo, int hi) AI {
#pragma unroll
      for (int i = 0; i < 5; ++i) {
        const unsigned m = ln * 205 + 64 * 205 * i, rho = m >> 11;
        const int ch16 = ln * 16 + 1024 * i - 160 * rho;
        const int k = ((m >> 10) & 24) | ((m >> 13) & 4) | (rho & 3);
        hi_[i] = ch16 >= 80;
        a[i] = k * 3840 + ch16 + 3072 - (i & 3) * 1024 + (ch16 < 80 ? lo : hi);
        PIN(a[i]);
      }
    };
#pragma unroll
    for (int i = 0; i < 4; ++i) PIN(xv[i]);
    unsigned lim = RCH, bud = 1u << 20, key = 0;
    const unsigned lw = Lw * 5120;
    int sxr = (128 * pi + 64 * Lw) * 1280, sr = rot * 128, sl = Lw * 8192;
    asm volatile("" : "+s"(sxr), "+s"(sr), "+s"(sl));
    SCHED();
    const rsrc_t rx = __builtin_amdgcn_make_buffer_rsrc((void*)(x - 3072), 0, 2048 * 1280 + 3072, 0x00020000);
#define MK_ST "+{s70}"(lim), "+{s71}"(bud), "+{s84}"(key), "+{s80}"(sr), "+{s85}"(sl)
#define MK_IN "{s81}"(lw), "{s76}"(L_LAND + Lw * 256), [rt] "s"(c.rt)
#define MK_CL "memory", "scc", "s72", "s73", "s74", "s75", "s77", "s78", "s79", "s82", "s83", "s86", "s87", "s88", "s90", "s91", "v248", "v252", "v253", "v254", "v255"
    asm volatile(
        ".macro MK_LD dep, ops:vararg\n\t"
        ".if \\dep\n\ts_waitcnt vmcnt(\\dep)\n\t.endif\n\t"
        "buffer_load_dwordx4 \\ops lds\n\t"
        ".endm\n\t"
        ".macro MK_FLAG rt, add\n\t"
        "s_mov_b32 m0, s76\n\t"
        "s_lshl2_add_u32 s83, s84, 4*\\add\n\t"
        "buffer_load_dword off, \\rt, s83 lds\n\t"
        ".endm\n\t"
        ".macro MK_HEAD key\n\t"
        "s_cmp_gt_i32 s70, \\key\n\t"
        "s_cbranch_scc1 1f\n\t"
        "s_mov_b32 s74, \\key\n\t"
        "s_call_b64 s[72:73], .Lmk_lslow\n"
        "1:\n\t"
        ".endm\n\t"
        ".macro MK_RAW dep, a0, a1, a2, a3, a4, rs, step\n\t"
        "MK_LD \\dep, \\a0, \\rs, s82 offen\n\t"
        "s_add_u32 s78, s82, \\step\n\t"
        "MK_LD \\dep, \\a1, \\rs, s82 offen offset:1024\n\t"
        "s_sub_u32 s79, s78, 2457600\n\t"
        "MK_LD \\dep, \\a2, \\rs, s82 offen offset:2048\n\t"
        "MK_LD \\dep, \\a3, \\rs, s82 offen offset:3072\n\t"
        "s_add_u32 m0, m0, 4096\n\t"
        "s_min_u32 s78, s78, s79\n\t"
        "MK_LD \\dep, \\a4, \\rs, s82 offen\n\t"
        "s_mov_b32 s82, s78\n\t"
        ".endm\n\t"
        ".macro MK_X4 dep, a0, a1, a2, a3, rs\n\t"
        "MK_LD \\dep, \\a0, \\rs, s77 offen\n\t"
        "s_add_u32 s78, s77, 20480\n\t"
        "MK_LD \\dep, \\a1, \\rs, s77 offen offset:1024\n\t"
        "MK_LD \\dep, \\a2, \\rs, s78 offen offset:2048\n\t"
        "s_add_u32 s77, s77, 40960\n\t"
        "MK_LD \\dep, \\a3, \\rs, s78 offen offset:3072\n\t"
        ".endm\n\t"
        ".macro MK_X dep, a0, a1, a2, a3, rs, sx\n\t"
        "s_mov_b32 m0, s85\n\t"
        "s_add_u32 s77, s80, \\sx\n\t"
        "MK_X4 \\dep, \\a0, \\a1, \\a2, \\a3, \\rs\n\t"
        "s_add_u32 m0, s85, 4096\n\t"
        "s_add_u32 s80, s80, 128\n\t"
        "MK_X4 \\dep, \\a0, \\a1, \\a2, \\a3, \\rs\n\t"
        "s_sub_u32 s79, s80, 1280\n\t"
        "s_min_u32 s80, s80, s79\n\t"
        ".endm\n\t"
        ".subsection 1\n"
        ".Lmk_lsleep:\n\t"
        "s_sleep 1\n"
        ".Lmk_lslow:\n\t"
        "v_mov_b32 v248, %[dn]\n\t"
        "ds_read2st64_b32 v[252:253], v248 offset1:1\n\t"
        "ds_read2st64_b32 v[254:255], v248 offset0:2 offset1:3\n\t"
        "ds_read_b32 v248, v248 offset:%[cap]\n\t"
        "s_sub_u32 s71, s71, 1\n\t"
        "s_cbranch_scc1 .Lmk_lgive\n\t"
        "s_waitcnt lgkmcnt(0)\n\t"
        "v_min3_u32 v252, v252, v253, v254\n\t"
        "v_min_u32 v252, v252, v255\n\t"
        "v_readfirstlane_b32 s75, v248\n\t"
        "v_readfirstlane_b32 s70, v252\n\t"
        "s_add_u32 s70, s70, 13\n\t"
        "s_min_u32 s70, s70, s75\n\t"
        "s_cmp_gt_i32 s70, s74\n\t"
        "s_cbranch_scc0 .Lmk_lsleep\n\t"
        "s_setpc_b64 s[72:73]\n"
        ".Lmk_lgive:\n\t"
        "s_endpgm\n\t"
        ".subsection 0\n\t"
        "MK_X 0, %[x0], %[x1], %[x2], %[x3], %[rx], %[sx]"
        : MK_ST
        : MK_IN, [x0] "v"(xv[0]), [x1] "v"(xv[1]), [x2] "v"(xv[2]), [x3] "v"(xv[3]), [rx] "s"(rx), [sx] "s"(sxr),
          [dn] "i"(L_DONE), [cap] "i"(L_CTL + 4 * C_CAP - L_DONE)
        : MK_CL);
    SCHED();
    *LP(unsigned, L_LAND + Lw * 256 + ln * 4) = 0u;
    raw_addr(ka, 1280, 2480);
    const int ok = Lw * 122880 + pj * 80 - 3072;
    const rsrc_t rk = __builtin_amdgcn_make_buffer_rsrc((void*)(wqkv + ok), 0, 640 * 3840 - ok, 0x00020000);
    asm volatile(
        "s_mov_b32 s82, %[sk]\n\t"
        "s_waitcnt lgkmcnt(0)\n"
        ".Lmk_blk:\n\t"
        "s_add_u32 m0, s85, %[kl]\n\t"
        "s_add_u32 s85, s85, 26624\n\t"
        "MK_RAW " STR(DEP_A) ", %[k0], %[k1], %[k2], %[k3], %[k4], %[rk], 245760\n\t"
        "MK_FLAG %[rt], 3\n\t"
        "s_sub_u32 s79, s85, 133120\n\t"
        "s_min_u32 s85, s85, s79\n\t"
        "s_add_u32 s84, s84, 3\n\t"
        "s_cmpk_eq_u32 s84, 30\n\t"
        "s_cbranch_scc1 .Lmk_blkend\n\t"
        "s_cmpk_lg_u32 s84, 3*" STR(XPRE) "\n\t"
        "s_cbranch_scc1 .Lmk_nobar\n\t"
        "s_barrier\n"
        ".Lmk_nobar:\n\t"
        "MK_HEAD s84\n\t"
        "MK_X " STR(DEP_A) ", %[x0], %[x1], %[x2], %[x3], %[rx], %[sx]\n\t"
        "s_branch .Lmk_blk\n"
        ".Lmk_blkend:\n\t"
        ".purgem MK_X4\n\t.purgem MK_X"
        : MK_ST
        : MK_IN, [x0] "v"(xv[0]), [x1] "v"(xv[1]), [x2] "v"(xv[2]), [x3] "v"(xv[3]), [rx] "s"(rx), [sx] "s"(sxr),
          [kl] "s"(16384 - Lw * 3072), [k0] "v"(ka[0]), [k1] "v"(ka[1]), [k2] "v"(ka[2]), [k3] "v"(ka[3]), [k4] "v"(ka[4]), [rk] "s"(rk), [sk] "s"(rot * 245760)
        : MK_CL);
    SCHED();
#pragma unroll
    for (int i = 0; i < 5; ++i) { qa[i] = ka[i] - 1280 + (hi_[i] ? 32 * 3840 - 80 - 1200 : 0); PIN(qa[i]); }
    const int oq = pq_ * 80 - 3072;
    const rsrc_t rw = __builtin_amdgcn_make_buffer_rsrc((void*)(wqkv + oq), 0, 640 * 3840 - oq, 0x00020000);
    char* ws2 = ws;
    asm volatile("" : "+s"(ws2));
    const rsrc_t rs = __builtin_amdgcn_make_buffer_rsrc((void*)ws2, 0, WS_BYTES, 0x00020000);
    asm volatile(
        "s_mov_b32 s82, %[sq]\n\t"
        "s_mov_b32 s85, s81\n\t"
        "s_movk_i32 s86, 30\n"
        ".Lmk_q:\n\t"
        "MK_HEAD s86\n\t"
        "s_mov_b32 m0, s85\n\t"
        "s_add_u32 s85, s85, 10240\n\t"
        "MK_RAW " STR(DEP_A) ", %[q0], %[q1], %[q2], %[q3], %[q4], %[rw], 491520\n\t"
        "MK_FLAG %[rt], 1\n\t"
        "s_add_u32 s84, s84, 1\n\t"
        "s_cmpk_ge_u32 s84, 32\n\t"
        "s_cselect_b32 s86, 33, 30\n\t"
        "s_cmpk_lg_u32 s84, 35\n\t"
        "s_cbranch_scc1 .Lmk_q"
        : MK_ST
        : MK_IN, [q0] "v"(qa[0]), [q1] "v"(qa[1]), [q2] "v"(qa[2]), [q3] "v"(qa[3]), [q4] "v"(qa[4]), [rw] "s"(rw), [sq] "s"(mod10(rot + Lw) * 245760)
        : MK_CL);
    SCHED();
#pragma unroll
    for (int i = 0; i < 5; ++i) {
      const unsigned rho = (ln * 205 + 64 * 205 * i) >> 11;
      const int o = ln * 16 + 1024 * i - 160 * rho + 3072 - (i & 3) * 1024, gg = (rho >> 2) & 3, j = ((rho >> 2) & 4) | (rho & 3);
      ka[i] = (5 * gg + (j >= 5 ? 15 + j : j)) * 1280 + o;
      qa[i] = (40 * gg + 23 + 5 * (j >> 1) + (j & 1)) * 1280 + o;
      PIN(ka[i]); PIN(qa[i]);
    }
    const rsrc_t ro = __builtin_amdgcn_make_buffer_rsrc((void*)(wo + (cc * 160 - 3072)), 0, 819200 - (cc * 160 - 3072), 0x00020000);
    asm volatile(
        "s_mov_b32 s80, %[rb]\n"
        ".Lmk_b:\n\t"
        "s_max_u32 s86, s84, 40\n\t"
        "s_cmp_lt_u32 s84, 39\n\t"
        "s_cselect_b32 s86, 39, s86\n\t"
        "s_cmp_lt_u32 s84, 37\n\t"
        "s_cselect_b32 s86, 36, s86\n\t"
        "MK_HEAD s86\n\t"
        "s_mov_b32 m0, s85\n\t"
        "s_add_u32 s85, s85, 10240\n\t"
        "s_cmp_ge_u32 s80, 16\n\t"
        "s_cbranch_scc1 .Lmk_b1\n\t"
        "s_mul_i32 s82, s80, 51200\n\t"
        "MK_RAW " STR(DEP_A) ", %[k0], %[k1], %[k2], %[k3], %[k4], %[ro], 0\n\t"
        "s_branch .Lmk_b2\n"
        ".Lmk_b1:\n\t"
        "s_mul_i32 s82, s80, 204800\n\t"
        "s_sub_u32 s82, s82, 3276800\n\t"
        "MK_RAW " STR(DEP_A) ", %[q0], %[q1], %[q2], %[q3], %[q4], %[ro], 0\n"
        ".Lmk_b2:\n\t"
        "MK_FLAG %[rt], 1\n\t"
        "s_sub_u32 s79, s85, 133120\n\t"
        "s_min_u32 s85, s85, s79\n\t"
        "s_add_u32 s80, s80, 1\n\t"
        "s_cmp_eq_u32 s80, %[be]\n\t"
        "s_cselect_b32 s79, 10, 0\n\t"
        "s_sub_u32 s80, s80, s79\n\t"
        "s_add_u32 s84, s84, 1\n\t"
        "s_cmpk_lg_u32 s84, 45\n\t"
        "s_cbranch_scc1 .Lmk_b\n\t"
        ".macro MK_SEG rot, base, len, end\n\t"
        "s_mov_b32 s80, \\rot\n\t"
        "s_mov_b32 s87, \\base\n\t"
        "s_mov_b32 s88, \\len\n\t"
        "s_movk_i32 s86, \\end\n\t"
        "s_call_b64 s[90:91], .Lmk_pk\n\t"
        ".endm\n\t"
        "MK_SEG %[rc1], %[sc1], 81920, 53\n\t"
        "s_add_u32 s87, s87, 81920\n\t"
        "MK_SEG %[rc2], s87, 61440, 59\n\t"
        "MK_SEG %[rd1], %[sd1], 163840, 75\n\t"
        "s_add_u32 s87, s87, 163840\n\t"
        "MK_SEG %[rd2], s87, 112640, 86\n\t"
        "s_waitcnt vmcnt(0)\n\t"
        "s_endpgm\n"
        ".Lmk_pk:\n\t"
        "MK_HEAD s84\n\t"
        "s_mov_b32 m0, s85\n\t"
        "s_add_u32 s77, s80, s87\n\t"
        "MK_LD " STR(DEP_P) ", %[l0], %[rs], s77 offen sc1\n\t"
        "s_add_u32 s80, s80, 10240\n\t"
        "MK_LD " STR(DEP_P) ", %[l0], %[rs], s77 offen offset:1024 sc1\n\t"
        "s_sub_u32 s79, s80, s88\n\t"
        "MK_LD " STR(DEP_P) ", %[l0], %[rs], s77 offen offset:2048 sc1\n\t"
        "s_min_u32 s80, s80, s79\n\t"
        "MK_LD " STR(DEP_P) ", %[l0], %[rs], s77 offen offset:3072 sc1\n\t"
        "s_add_u32 m0, m0, 4096\n\t"
        "s_add_u32 s85, s85, 10240\n\t"
        "MK_LD " STR(DEP_P) ", %[l1], %[rs], s77 offen sc1\n\t"
        "MK_FLAG %[rt], 1\n\t"
        "s_sub_u32 s79, s85, 133120\n\t"
        "s_min_u32 s85, s85, s79\n\t"
        "s_add_u32 s84, s84, 1\n\t"
        "s_cmp_lg_u32 s84, s86\n\t"
        "s_cbranch_scc1 .Lmk_pk\n\t"
        "s_setpc_b64 s[90:91]\n"
        ".Lmk_tab:\n\t"
        ".set mk_i, 0\n\t.rept 128\n\t.long mk_i\n\t.set mk_i, mk_i + 1\n\t.endr\n\t"
        ".purgem MK_FLAG\n\t.purgem MK_HEAD\n\t.purgem MK_RAW\n\t.purgem MK_SEG\n\t.purgem MK_LD"
        : MK_ST
        : MK_IN, [q0] "v"(qa[0]), [q1] "v"(qa[1]), [q2] "v"(qa[2]), [q3] "v"(qa[3]), [q4] "v"(qa[4]),
          [k0] "v"(ka[0]), [k1] "v"(ka[1]), [k2] "v"(ka[2]), [k3] "v"(ka[3]), [k4] "v"(ka[4]), [ro] "s"(ro),
          [l0] "v"(ln * 16), [l1] "v"(ln * 16 + 4096), [rs] "s"(rs),
          [rb] "s"(rotB + 10 * Lw), [be] "s"(10 + 10 * Lw),
          [sc1] "s"(OFF_WC + qj * 143360), [rc1] "s"(rotC * 4096 + lw),
          [rc2] "s"(rotC * 3072 + lw),
          [sd1] "s"(OFF_WD + cc * 276480), [rd1] "s"(rotD1 * 10240 + lw),
          [rd2] "s"(rotD2 * 10240 + lw)
        : MK_CL);
  }
  STAMP(0);
  c.ws = (GAS char*)ws;
  c.rs = __builtin_amdgcn_make_buffer_rsrc((void*)ws, 0, WS_BYTES, 0x00020000);
  c.budget = 1u << 22;
  unsigned e0 = __hip_atomic_load((GAS unsigned*)(c.ws + OFF_EPOCH), RLX_AGENT);
  const auto epoch = [&]() AI { SCHED(); PIN(e0); c.ep = (__builtin_amdgcn_readfirstlane(e0) + EPOCH_STEP) | 1u; };
  unsigned pf = 0u, pg = 0u;
  {
    if (w == 7) {
      const v4u z = {0u, 0u, 0u, 0u};
      *LP(v4u, L_DONE + lane * 16) = z;
      *LP(v4u, L_FLG + lane * 16) = z;
      *LP(unsigned, L_CTL + (lane & 31) * 4) = (lane & 31) == C_CAP ? 40 : 0u;
#pragma unroll
      for (int i = 0; i < 4; ++i) *LP(v4u, L_RPK + i * 1024 + lane * 16) = z;
      const int l = min(PADT ? b * 3 + (lane & 3) : 0, 639), lg = l / 160, lk = l - 160 * lg;
      pf = __builtin_amdgcn_raw_buffer_load_b32(c.rs, lane < 8 ? OFF_FLAGS + (b * 6 + min(lane, 5)) * 128 : OFF_WC + (4 * lg + 3) * 143360 + (82 + 3 * (lk >> 3)) * 1024 + (lk & 7) * 128, 0, SC1);
      pg = *(const GAS unsigned*)(cpc + min((sx * 16 + n16) * 128u, clen));
      LGKM0();
    }
    __builtin_amdgcn_s_barrier();
    CFENCE();
    STAMP(30);
  }
  if (__builtin_expect(w >= 4, 0)) {
    __builtin_amdgcn_s_setprio(3);
    const int hw = w - 6;
    STL(62);
    const bool fast = (0xa5 >> (pj & 7)) & 1;
    if (hw == 0) {
      const int u = pi * 8 + (pj >> 1) + (fast ? 0 : 128), wi = min(lane >> 2, 4), e = lane & 3;
      const bool k2 = u < 108;
      int cb, s, A, B = 4, C = 8;
      if (k2) {
        cb = u >> 2; s = 5 * (u & 3) + wi;
        A = 160 * (u & 3) + 32 * wi - (wi > 2 ? 16 : 0);
        if (wi == 2) { B = 8; C = 80; }
      } else {
        const int k = 5 * (u - 108) + wi;
        cb = k / 54; s = k - 54 * cb;
        if (s < 48) A = 16 * (s < 32 ? gam0(s >> 1) + 2 * (s & 1) : gam0(s - 32) + 4);
        else { const int l = 2 * (s - 48), a0 = gam0(l + l / 3), a1 = gam0(l + 1 + (l + 1) / 3); A = 16 * a0 + 96; B = 8; C = 16 * (a1 - a0); }
      }
      const int pitch = k2 ? 3456 : 1280;
      int so = (A + B * (e & 1) + C * (e >> 1)) * pitch + cb * 128;
      const int gam = 4 * cb + e, grp = gam / 27, rr = gam - 27 * grp, jj = min(rr / 7, 3), t = rr - 7 * jj, c5 = gam / 5;
      int db = k2 ? OFF_WC + (4 * grp + jj) * 143360 + (t < 4 ? 4 * s + t : 76 + 3 * s + t) * 1024
                  : OFF_WD + c5 * 276480 + ((s < 16 ? s * 10 : s < 32 ? (s - 16) * 10 + 5 : s < 43 ? (s - 16) * 10 : (s - 27) * 10 + 5) + gam - 5 * c5) * 1024;
      PIN(so); PIN(db); PIN(B);
      const rsrc_t r0 = __builtin_amdgcn_make_buffer_rsrc((void*)(k2 ? w1 : w2), 0, 2211840, 0x00020000);
      unsigned t0 = L_SCR + g * 1024 + ((lane >> 2) & 3) * 128 + 8 * e;
      PIN(t0);
      int va[5], dd[5][4];
#pragma unroll
      for (int i = 0; i < 5; ++i) {
        va[i] = ((lane >> 3) + (lane >= 32 && __builtin_amdgcn_readlane(B, 4 * i) == 4 ? 12 : 0)) * pitch + (lane & 7) * 16;
        PIN(va[i]);
#pragma unroll
        for (int e2 = 0; e2 < 4; ++e2) { dd[i][e2] = lane * 16 + __builtin_amdgcn_readlane(db, 4 * i + e2); PIN(dd[i][e2]); }
      }
      const int g0 = gam0(qj), c0 = (80 * cc) >> 6;
      int dep = lane < 12 ? min(4 * (g0 >> 2) + lane, 3 + 4 * ((g0 + 5) >> 2) + ((qj & 3) != 3 && ((g0 + 6) & 3) == 0 ? 4 : 0)) : min(108 + (54 * c0) / 5 + lane - 12, 108 + (54 * c0 + 107) / 5);
      dep += FLR;
      PIN(dep);
      STAMP(36);
      epoch();
      asm volatile("" :: "s"(c.ep));
      ctl_addc<C_ARR1>();
      v4u v[5][4];
      const auto fetch = [&]() AI {
#pragma unroll
        for (int i = 0; i < 20; ++i)
          asm volatile("s_mov_b32 m0, %0\n\tv_readlane_b32 s70, %3, %4\n\ts_nop 4\n\tbuffer_load_dwordx4 %1, %2, s70 offen lds"
                       :: "i"(L_SCR + i * 1024), "v"(va[i >> 2]), "s"(r0), "v"(so), "i"(i) : "memory", "s70");
      };
      const auto turn = [&]() AI {
        DRAIN();
#pragma unroll
        for (int i = 0; i < 5; ++i)
#pragma unroll
          for (int e2 = 0; e2 < 4; ++e2) v[i][e2] = tr2(t0, i * 4096 + 32 * e2, 512);
      };
      const auto put = [&]() AI {
#pragma unroll
        for (int i = 0; i < 5; ++i)
#pragma unroll
          for (int e2 = 0; e2 < 4; ++e2) st128(c, v[i][e2], dd[i][e2], 0);
      };
      if (k2) land_wait(c, RPK_F);
      STAMP(21);
      if (k2) {
        fetch();
        turn();
        put();
        float cs[4];
#pragma unroll
        for (int e2 = 0; e2 < 4; ++e2) {
          cs[e2] = 0.f;
#pragma unroll
          for (int i = 0; i < 5; ++i) cs[e2] = dot2(v[i][e2].x, ONE2, dot2(v[i][e2].y, ONE2, dot2(v[i][e2].z, ONE2, dot2(v[i][e2].w, ONE2, cs[e2]))));
        }
        v2u q0 = swap32(asu(cs[0]), asu(cs[2])), q1 = swap32(asu(cs[1]), asu(cs[3]));
        q0 = swap16(asu(asf(q0.x) + asf(q0.y)), asu(asf(q1.x) + asf(q1.y)));
        st32(c, asu(asf(q0.x) + asf(q0.y)), lane * 4, OFF_CS + ((u & 3) * 1792 + 64 * (u >> 2)) * 4);
      }
      STAMP(32);
      const bool late = !k2 && u < UMAX;
      if (!late) { ctl_addc<C_XB0>(); ctl_addc<C_XB1>(); }
      if (k2) { DRAIN(); flag_set(c, FLR + u); }
      STAMP(39);
      if ((TCH & 4) && !k2) touch(w2, 2211840, (5632 + 79 * (u - 108)) * 128, 79, 128);
      STAMP(37);
      ctl_wait<1>(c, C_PUB, LATE_L);
      if (late) fetch();
      flag_wait(c, dep, 12);
      if (lane == 0) *LP(volatile unsigned, L_CTL + 4 * C_CAP) = CH_D;
      STAMP(40);
      if (late) { turn(); ctl_addc<C_XB0>(); ctl_addc<C_XB1>(); }
      STAMP(41);
      done_wait(c, LATE_S);
      {
        f32x4 cs[4];
#pragma unroll
        for (int rb = 0; rb < 4; ++rb) cs[rb] = __builtin_bit_cast(f32x4, ld128(c, min(lane, 27) * 16, OFF_CS + (rb * 1792 + 16 * g0) * 4));
        SCHED();
        if (late) put();
        if (lane < 28) *LP(f32x4, L_CSUM + lane * 16) = add4(add4(cs[0], cs[1]), add4(cs[2], cs[3]));
      }
      ctl_addc<C_CS>();
      DRAIN();
      if (!k2) flag_set(c, FLR + u);
      STAMP(22);
      ctl_wait<2>(c, C_XB0, 3);
      ctl_wait<2>(c, C_XB1, 3);
      __builtin_amdgcn_s_sleep(8);
      STAMP(20);
      flag_wait(c, dep, 36);
      if (lane == 0) *LP(volatile unsigned, L_CTL + 4 * C_CAP) = 127;
      STAMP(34);
      DUMPL(20, 21, 22, 32, 34, 36, 37, 39, 40, 41);
      ENDPGM();
    }
    STL(62);
    unsigned ra[4], gd[4];
#pragma unroll
    for (int i = 0; i < 4; ++i) {
      {
        const int gr = min(lane + 32 * (i & 2), 119), hd = i & 1, z = gr >= 100, d4 = z ? gr % 5 : gr / 20;
        const int d4g = 5 * hd + d4, e8 = z ? 0 : 20 * hd + gr - 20 * d4 + 8;
        ra[i] = KVSLOT * 10240 + ((d4g >> 2) * 3 + (e8 >> 4)) * 1024 + ((d4g & 3) * 16 + (e8 & 15)) * 16;
      }
      {
        const int gr = 32 * i + 16 * (g >> 1) + n16, hd = g & 1, z = gr >= 100, d4 = z ? gr % 5 : gr / 20;
        const int e = gr - 20 * d4, gi = e / 5, r5 = e - 5 * gi;
        const int R = z || r5 == 4, row = z ? 4 * ((gr - 100) / 5) + 1 : r5 == 4 ? 4 * gi : 4 * gi + r5;
        const int dd = d4 + hd;
        gd[i] = gr < 120 ? L_RPK + (hd * 2 + R) * 1024 + ((dd & 3) * 16 + row) * 16 + (dd >> 2) * 8 : L_CK;
      }
      PIN(ra[i]); PIN(gd[i]);
    }
    int gs = g * KVP_STR + n16 * 16, gs7 = g * KVP_STR + (n16 & 7) * 16, kvo = OFF_KVP + (pj * 16 + pi) * KVP_STR + lane * 16;
    v4u sel[2];
#pragma unroll
    for (int i = 0; i < 4; ++i) {
      const unsigned o = ((n16 >> 1) & 3) == i ? (n16 & 1 ? 0x3f800000u : 0x3f80u) : 0u;
      sel[0][i] = n16 < 8 ? o : 0u; sel[1][i] = n16 < 8 ? 0u : o;
    }
    PIN(gs); PIN(gs7); PIN(kvo); PIN(sel[0]); PIN(sel[1]);
    if (TCH & 2) touch(wo, 819200, b * 3200, 25, 128);
    if (TCH & 4) touch(w2, 2211840, b * 2816, 22, 128);
    STAMP(38);
    epoch();
    TOUCH(pf); TOUCH(pg);
    ctl_wait<1>(c, C_ARR1, 5);
    STAMP(26);
    {
      f32x4 p[4][4];
#pragma unroll
      for (int i = 0; i < 4; ++i)
#pragma unroll
        for (int j = 0; j < 4; ++j) p[i][j] = *LP(f32x4, ra[i] + j * 10240);
      if (!CAPL) { CFENCE(); if (lane == 0) *LP(volatile unsigned, L_CTL + 4 * C_CAP) = CH_C1; }
#pragma unroll
      for (int h = 0; h < 2; ++h) {
        const f32x4 u = add4(add4(p[2 * h][0], p[2 * h][1]), add4(p[2 * h][2], p[2 * h][3])), v = add4(add4(p[2 * h + 1][0], p[2 * h + 1][1]), add4(p[2 * h + 1][2], p[2 * h + 1][3]));
        if (h == 0 || lane < 56) st128(c, v4u{pk2(u[0], u[1]), pk2(u[2], u[3]), pk2(v[0], v[1]), pk2(v[2], v[3])}, kvo, h * 1024);
      }
    }
    if (CAPL) { CFENCE(); if (lane == 0) *LP(volatile unsigned, L_CTL + 4 * C_CAP) = CH_C1; }
    STAMP(27);
    DRAIN();
    flag_set(c, FL1 + pj * 16 + pi);
    ctl_addc<C_PUB>();
    STAMP(23);
    flag_wait(c, FL1 + pq_ * 16 + n16, 16);
    STAMP(24);
    {
      v4u p[4][8];
#pragma unroll
      for (int t = 0; t < 4; ++t)
#pragma unroll
        for (int i = 0; i < 8; ++i) p[t][i] = ld128(c, (i == 7 ? gs7 : gs) + i * 256, OFF_KVP + (pq_ * 16 + 4 * t) * KVP_STR);
      ctl_addc<C_PUB>();
      SCHED(); STAMP(35); SCHED();
      f32x4 u[4];
#pragma unroll
      for (int i = 0; i < 4; ++i) u[i] = f32x4{0.f, 0.f, 0.f, 0.f};
#pragma unroll
      for (int t = 0; t < 4; ++t)
#pragma unroll
        for (int i = 0; i < 8; ++i) u[i >> 1] = mfma(sel[i & 1], p[t][i], u[i >> 1]);
#pragma unroll
      for (int i = 0; i < 4; ++i) *LP(v2u, gd[i]) = v2u{pk2(u[i][0], u[i][1]), pk2(u[i][2], u[i][3])};
    }
    ctl_addc<C_GO1>();
    LGKM0();
    STAMP(25);
    STL(25);
    ctl_wait<2>(c, C_XB0, 3);
    if (b == 0 && lane == 0) __hip_atomic_store((GAS unsigned*)(c.ws + OFF_EPOCH), c.ep, RLX_AGENT);
    ctl_wait<1>(c, C_ARR4A, 4);
    flag_set(c, FL4A + qi * 16 + qj);
    STAMP(42);
    flag_wait(c, FL4A + (hp >> 1) * 16 + n16, 16);
    ctl_addc<C_GO4A>();
    STAMP(28);
    ctl_wait<1>(c, C_ARR4B, 4);
    flag_set(c, FL4B + qi * 16 + qj);
    STAMP(44);
    flag_wait(c, FL4B + (hp >> 1) * 16 + n16, 16);
    ctl_addc<C_GO4B>();
    STAMP(29);
    DUMPL(23, 24, 25, 26, 27, 28, 29, 35, 38, 42, 44);
    ENDPGM();
  }

  int xvoff;
  STL(30);
  const unsigned dn = L_LAND + 128 + lane * 4;
  c.dm = L_DONE + w * 256;
  const int kb = w & 1;
  const int mtB = 4 * hp + w;
  v2u xr[5];

  {
    v4u xa[2][20];
    const unsigned hv = n16 < 8 ? ONE2 : 0u;
    v4u ones = {hv, hv, hv, hv}, oneh = {hv ^ ONE2, hv ^ ONE2, hv ^ ONE2, hv ^ ONE2};
    PIN(ones); PIN(oneh);
    unsigned rawlane = (4 * g + ((lane >> 2) & 3)) * 160 + 8 * (lane & 3);
    const int oc = 32 * w, o4 = 128 + 5120 * (w & 1);
    {
      f32x4 acc[2][5];
      {
        unsigned br0 = w * 4096 + (n16 >> 3) * 1024 + (n16 & 7) * 128 + ((g ^ (n16 >> 1)) * 16), br1 = br0 ^ 64, wb = rawlane + 16384;
        PIN(br0); PIN(br1); PIN(wb);
        f32x4 sm, sq[2], ca;
        v4u wf[4], cf;
        v2u clo;
        unsigned la, cad;
        float q2;
        const auto addr = [&](int o) AI { unsigned r; asm volatile("v_add_u32 %0, %1, %2" : "=v"(r) : "s"(o), "v"(wb)); return r; };
        const auto stat = [&](auto qc, auto bc) AI {
          constexpr int q = decltype(qc)::v, m = q & 1, e = 2 * decltype(bc)::v + (q >> 2);
          if constexpr ((q & 2) == 0) mm<1, e == 0>(sq[m], xa[m][e], xa[m][e]);
          else mm<1, e == 0 && m == 0>(sm, m ? oneh : ones, xa[m][e]);
          SCHED();
        };
        epoch();
        ring_wait<0>(c);
        STAMP(31);
        SCHED();
        static_for<-4, 100>([&](auto ic) AI {
          constexpr int f = decltype(ic)::v, fn = f + 3;
          constexpr int s = f < 0 ? 0 : f / 5, t = f < 0 ? 0 : f % 5;
          constexpr int cb = f < 0 ? -1 : f / 10, i = (f + 10) % 10, nb = cb + 1;
          constexpr int off = fn < 0 ? 0 : bin(fn / 10) + ((fn % 10) / 5) * 5120 + 32 * (fn % 5);
          constexpr bool R = fn >= 0 && fn < 100, more = cb < 9, go = f >= 0, rel = true;
          v2u lo;
          if constexpr (go) { mm<0, s == 0>(acc[0][t], wf[f % 4], xa[0][s]); SCHED(); }
          if constexpr (R) { lo = tr1(wb, off); SCHED(); }
          if constexpr (i == 4) { cad = addr(o4); SCHED(); }
          if constexpr (i == 8 && more) { cad = addr(oc); SCHED(); }
          if constexpr (i == 1 && more) bump<breg(nb) - breg(cb)>(br0);
          if constexpr (i == 2 && more) bump<breg(nb) - breg(cb)>(br1);
          if constexpr (i == 9 && go && rel) { release<3 * cb + 3>(c); SCHED(); }
          if constexpr (i == 6 && go && more) { ring_cmp<3 * nb>(la); SCHED(); }
          if constexpr (go) { mm<0, s == 0>(acc[1][t], wf[f % 4], xa[1][s]); SCHED(); }
          if constexpr (R) { const v2u hi = tr1(wb, off + 2560); wf[fn % 4] = v4u{lo.x, lo.y, hi.x, hi.y}; SCHED(); }
          if constexpr (i == 6 && go && more) { ring_br<3 * nb>(c); SCHED(); }
          if constexpr (go) {
            if constexpr (i == 1 || i == 4) { mm<1, f == 1>(ca, ones, cf); SCHED(); }
            else if constexpr (i == 7) { mm<1, false>(ca, oneh, cf); SCHED(); }
            else if constexpr (more) stat(IC<i - (i + 1) / 3>{}, IC<cb>{});
            else if constexpr (i < 4) stat(IC<i == 0 ? 0 : i == 2 ? 4 : 2>{}, IC<cb>{});
          }
          if constexpr (i == 0) xa[1][2 * cb + 1] = *LP(v4u, br1 + bin(cb) + 2048);
          if constexpr (i == 1) clo = tr1(cad, bin(cb) + 5120);
          if constexpr (i == 2) { const v2u hi = tr1(cad, bin(cb) + 7680); cf = v4u{clo.x, clo.y, hi.x, hi.y}; }
          if constexpr (i == 3 && more) la = *LP(unsigned, dn);
          if constexpr (i == 4) clo = tr1(cad, bin(cb));
          if constexpr (i == 5) { const v2u hi = tr1(cad, bin(cb) + 2560); cf = v4u{clo.x, clo.y, hi.x, hi.y}; }
          if constexpr (i == 6 && more) { xa[0][2 * nb] = *LP(v4u, br0 + bin(nb)); SCHED(); if constexpr (go) bump<breg(nb) - breg(cb)>(wb); }
          if constexpr (i == 7 && more) xa[1][2 * nb] = *LP(v4u, br0 + bin(nb) + 2048);
          if constexpr (i == 8 && more) clo = tr1(cad, bin(nb));
          if constexpr (i == 9 && more) { const v2u hi = tr1(cad, bin(nb) + 2560); cf = v4u{clo.x, clo.y, hi.x, hi.y}; }
          SCHED();
          if constexpr (i == 9 && go && more) stat(IC<7>{}, IC<cb>{});
          if constexpr (i == 9 && more) { xa[0][2 * nb + 1] = *LP(v4u, br1 + bin(nb)); SCHED(); }
          if constexpr (!more && i < 5) stat(IC<i == 0 ? 1 : i == 1 ? 3 : i == 2 ? 5 : i + 3>{}, IC<cb>{});
          if constexpr (f == 94) {
            const unsigned bw = 4 * BLK + w * 4096 + opaque(lane) * 16;
            *LP(f32x4, bw) = sq[0];
            *LP(f32x4, bw + 1024) = sq[1];
            CFENCE();
            SCHED();
          }
          if constexpr (f == 95) {
            const int l2 = opaque(lane), n2 = l2 & 15;
            q2 = *LP(float, 4 * BLK + w * 4096 + (l2 >> 5) * 1024 + ((n2 >> 2) * 16 + n2) * 16 + (n2 & 3) * 4);
            SCHED();
          }
          if constexpr (f == 97) {
            const int l2 = opaque(lane);
            PIN(sm);
            const float mean = sm[0] * (1.f / 640.f);
            const float a = __builtin_amdgcn_rsqf(fmaxf(q2 * (1.f / 640.f) - mean * mean, 0.f) + 1e-5f);
            *LP(float, L_STA + w * 256 + l2 * 4) = (l2 & 16) ? -mean * a : a;
            CFENCE();
            SCHED();
          }
          if constexpr (f == 98) {
            const unsigned a = L_CK + w * 256 + opaque(lane) * 4;
            asm volatile("s_nop 1" : "+v"(ca));
            *LP(float, a) = ca[0];
            ctl_addc<C_CSK>();
            SCHED();
          }
          if constexpr (f + 1 >= 0 && f + 1 < 100) {
            if constexpr (i == 5 && more) TOUCH(la);
            else if constexpr (i == 9) TOUCH(xa[1][2 * nb]);
            else TOUCH(wf[(f + 1) % 4]);
            SCHED();
          }
        });
        rawlane = wb - (16384 + breg(9));
        PIN(rawlane);
      }
      lane = opaque(lane); c.lane = lane; n16 = lane & 15; g = lane >> 4; xvoff = lane * 16;
      STAMP(4);
      f32x4 a4[2], b4[2];
#pragma unroll
      for (int m = 0; m < 2; ++m) { a4[m] = *LP(f32x4, L_STA + w * 256 + m * 128 + g * 16); b4[m] = *LP(f32x4, L_STA + w * 256 + m * 128 + 64 + g * 16); }
      SCHED();
      asm volatile("s_nop 3");
      SCHED();
      const float ks = n16 < 8 ? LOG2E : 1.f, kd = n16 < 8 ? PHID : 0.f;
#pragma unroll
      for (int m = 0; m < 2; ++m) {
        const f32x4 al = a4[m] * LOG2E;
        acc[m][0] = acc[m][0] * al + PHID; acc[m][1] = acc[m][1] * al + PHID;
        acc[m][2] = acc[m][2] * (a4[m] * ks) + kd;
        acc[m][3] = acc[m][3] * a4[m]; acc[m][4] = acc[m][4] * a4[m];
#pragma unroll
        for (int t = 0; t < 5; ++t) PIN(acc[m][t]);
      }
      ctl_wait(c, C_CSK, 4);
      STAMP(48);
      float cs[5];
      {
        const unsigned a = L_CK + n16 * 4;
#pragma unroll
        for (int t = 0; t < 4; ++t) cs[t] = *LP(float, a + t * 256);
        cs[4] = *LP(float, a + 128) + *LP(float, a + 384);
        cs[0] *= LOG2E; cs[1] *= LOG2E; cs[2] *= ks;
      }
      v4u pk[3], vv[3];
#pragma unroll
      for (int t = 4; t >= 0; --t) {
        const f32x4 t0 = b4[0] * cs[t] + acc[0][t], t1 = b4[1] * cs[t] + acc[1][t];
        if (t >= 2) vv[t - 2] = v4u{pk2(t0[0], t0[1]), pk2(t0[2], t0[3]), pk2(t1[0], t1[1]), pk2(t1[2], t1[3])};
        if (t <= 2) { const v2u p0 = phi4(t0), p1 = phi4(t1); pk[t] = v4u{p0.x, p0.y, p1.x, p1.y}; }
      }
      if (n16 < 8) vv[0] = ones;
      const unsigned kp = (KVSLOT + w) * 10240 + opaque(lane) * 16;
      const f32x4 z4 = {0.f, 0.f, 0.f, 0.f};
#pragma unroll
      for (int ta = 2; ta >= 0; --ta)
#pragma unroll
        for (int tb = 0; tb < 3; ++tb)
          if (ta != 0 || tb != 2) *LP(f32x4, kp + (ta * 3 + tb) * 1024) = mfma(pk[ta], vv[tb], z4);
      ctl_addc<C_ARR1>();
    }
    STAMP(3);
    v2u pq[2][3];
    {
      static_assert(region(CH_Q) == 0 && region(CH_B - 1) == 0, "q chunks");
      f32x4 aq[2][3], ca;
      v4u wf[4], cf[2];
      v2u clo;
      const unsigned wb = rawlane;
      unsigned c0 = rawlane + oc, c4 = rawlane + o4;
      PIN(c0); PIN(c4);
      float sa[2], sb[2];
      f32x4 cs[3];
      ring_wait<CH_B - 1>(c);
      STAMP(43);
      SCHED();
      constexpr auto qoff = [](int f) { return ((f % 20) / 2) * 5120 + (f & 1) * 80 + 32 * (f / 20); };
      constexpr auto coff = [](int j) { return (j / 3) * 10240 + (j % 3 == 1 ? 5120 : 0); };
      static_for<-3, 60>([&](auto ic) AI {
        constexpr int f = decltype(ic)::v, fn = f + 3, s = f < 0 ? 0 : f % 20, t = f < 0 ? 0 : f / 20, jn = f + 2;
        v2u lo;
        if constexpr (f >= 0) { mm<1, s == 0>(aq[0][t], wf[f % 4], xa[0][s]); SCHED(); }
        if constexpr (fn < 60) { lo = tr1(wb, qoff(fn)); SCHED(); }
        if constexpr (f >= 0) { mm<1, s == 0>(aq[1][t], wf[f % 4], xa[1][s]); SCHED(); }
        if constexpr (fn < 60) { const v2u hi = tr1(wb, qoff(fn) + 2560); wf[fn % 4] = v4u{lo.x, lo.y, hi.x, hi.y}; SCHED(); }
        if constexpr (f >= 0 && f < 15) { mm<1, f == 0>(ca, f % 3 == 2 ? oneh : ones, cf[f & 1]); SCHED(); }
        if constexpr (jn >= 0 && jn < 15) { cf[jn & 1] = tr2(jn % 3 == 2 ? c4 : c0, coff(jn), 2560); SCHED(); }
        if constexpr (f == 16) {
          const int l2 = opaque(lane);
          PIN(ca);
          if ((l2 & 16) == 0) *LP(float, L_CQ + (l2 & 15) * 4 + w * 64 + (l2 >> 5) * 256) = ca[0];
          ctl_addc<C_CSQ>();
#pragma unroll
          for (int m = 0; m < 2; ++m) { sa[m] = *LP(float, L_STA + w * 256 + m * 128 + n16 * 4); sb[m] = *LP(float, L_STA + w * 256 + m * 128 + 64 + n16 * 4); }
          SCHED();
        }
      });
      STAMP(46);
      release<32>(c);
      {
        const char* xb0 = x + (size_t)(16 * mtB * 1280 + cc * 160);
        const unsigned rowoff = n16 * 1280 + g * 8;
#pragma unroll
        for (int t = 0; t < 5; ++t) xr[t] = *(const GAS v2u*)(xb0 + rowoff + 32 * t);
        SCHED();
      }
      ctl_wait(c, C_CSQ, 4);
      STAMP(47);
      {
        const unsigned a = L_CQ + g * 16;
        cs[0] = *LP(f32x4, a) + *LP(f32x4, a + 160);
        const f32x4 p1 = *LP(f32x4, a + 288);
        cs[1] = *LP(f32x4, a + 64) + *LP(f32x4, a + 224) + (g < 2 ? f32x4{0.f, 0.f, 0.f, 0.f} : p1);
        cs[2] = *LP(f32x4, a + 128) + p1 + *LP(f32x4, a + 352);
      }
#pragma unroll
      for (int m = 0; m < 2; ++m) { sa[m] *= LOG2E; sb[m] *= LOG2E; }
#pragma unroll
      for (int t = 0; t < 3; ++t) {
#pragma unroll
        for (int m = 0; m < 2; ++m) {
          pq[m][t] = phi4(aq[m][t] * sa[m] + (cs[t] * sb[m] + PHID));
          if (t == 2 && g >= 2) pq[m][t] = v2u{0u, 0u};
        }
      }
    }
    v4u qb[2][2];
#pragma unroll
    for (int m = 0; m < 2; ++m)
#pragma unroll
      for (int hh = 0; hh < 2; ++hh) { qb[m][hh] = v4u{pq[m][hh].x, pq[m][hh].y, pq[m][hh + 1].x, pq[m][hh + 1].y}; PIN(qb[m][hh]); }
    const int mt0 = 8 * pi + 2 * w;
    int oa = OFF_O + (mt0 * 20 + pq_) * 1024 + xvoff;
    int ob = OFF_O + (mt0 * 20 + 16 + (pq_ >> 2)) * 1024 + (pq_ & 3) * 256 + g * 20480 + n16 * 16;
    unsigned fa = L_RPK + xvoff;
    PIN(oa); PIN(ob); PIN(fa);
#pragma unroll
    for (int t = 0; t < 5; ++t) PIN(xr[t]);
    STAMP(5);
    ctl_wait(c, C_GO1, 1);
    if (!QUIET) release<CH_B>(c);
    STAMP(7);
    v4u ka_[4];
#pragma unroll
    for (int i = 0; i < 4; ++i) ka_[i] = *LP(v4u, fa + (i ^ 1) * 1024);
    f32x4 on[2][2][2];
    const f32x4 z4 = {0.f, 0.f, 0.f, 0.f};
#pragma unroll
    for (int i = 0; i < 4; ++i)
#pragma unroll
      for (int m = 0; m < 2; ++m) on[m][i >> 1][(i & 1) ^ 1] = mfma(ka_[i], qb[m][i >> 1], z4);
    unsigned tl[2];
#pragma unroll
    for (int m = 0; m < 2; ++m) {
      const float r0 = __builtin_amdgcn_rcpf(on[m][0][1][1] + 1e-6f), r1 = __builtin_amdgcn_rcpf(on[m][1][1][1] + 1e-6f);
      const v4u o = {pk2(on[m][0][0][0] * r0, on[m][0][0][1] * r0), pk2(on[m][0][0][2] * r0, on[m][0][0][3] * r0),
                     pk2(on[m][0][1][0] * r0, on[m][1][0][0] * r1), pk2(on[m][1][0][1] * r1, on[m][1][0][2] * r1)};
      tl[m] = pk2(on[m][1][0][3] * r1, on[m][1][1][0] * r1);
      st128(c, o, oa, m * 20480);
    }
    const v2u t0 = swap16(tl[0], tl[1]);
    const v2u u0 = swap32(t0.x, t0.x), u1 = swap32(t0.y, t0.y);
    if (lane < 32) st128(c, v4u{u0.x, u1.x, u0.y, u1.y}, ob, 0);
  }
  STAMP(8);
  unsigned hq[10];

  {
    f32x4 acc[2][5];
    {
#pragma unroll
      for (int t = 0; t < 5; ++t) { acc[0][t] = unpk4(xr[t].x, xr[t].y); PIN(acc[0][t]); }
    }
    const int sb = 5 * (cc >> 1) + 3 * (cc & 1);
    int ha = OFF_H + (mtB * 20 + sb) * 1024 + xvoff;
    int hb = OFF_H + (mtB * 20 + 5 * (cc >> 1) + 2) * 1024 + (cc & 1) * 512 + g * 128 + n16 * 16;
    unsigned wraw = (4 * g + ((lane >> 2) & 3)) * 160 + 8 * (lane & 3) + kb * 5120;
    PIN(ha); PIN(hb); PIN(wraw);
    DRAIN();
    const bool last = arrive<C_ARR2>(c, FL2 + pi * 16 + pq_);
    STAMP(6);
    if (PW & 1) ctl_wait(c, C_ARR2, 4);
    if (!(PW & 4)) flag_wait(c, FL2 + (hp >> 1) * 16 + n16, kb ? 16 : 10);
    else if (last) { flag_wait(c, FL2 + (hp >> 1) * 16 + n16, 16); ctl_addc<C_ARR2>(); }
    else ctl_wait(c, C_ARR2, 5);
    STAMP(9);
    v4u xb[2][10];
    const int xs = OFF_O + mtB * 20480 + kb * 10240, xm = kb ? -20480 : 20480;
    xload<10, 0, 0, PRE_B, 10>(c, xb, xvoff, xs, xm, rotB);
    if (QUIET) release<CH_B>(c);
    gemm_pass<5, 10, CH_B, 5, 10, 2, true>(c, dn, wraw, acc, xb, [&](auto ic) AI {
      constexpr int f = decltype(ic)::v;
      if constexpr (f >= 0 && f % 5 == 1 && f / 5 + PRE_B < 10) xload<10, 0, f / 5 + PRE_B, 1, 10>(c, xb, xvoff, xs, xm, rotB);
    });
    STAMP(1);
    ctl_wait(c, C_XB0 + (w >> 1), 1);
    pair_reduce(c, w, C_XB0, 1, acc, hq);
    STAMP(49);
    st128(c, v4u{hq[0], hq[1], hq[2], hq[3]}, ha, 0);
    st128(c, v4u{hq[4], hq[5], hq[6], hq[7]}, ha, 1024);
    const v2u ta = swap16(hq[8], hq[8]), tb = swap16(hq[9], hq[9]);
    if ((g & 1) == 0) st128(c, v4u{ta.x, tb.x, ta.y, tb.y}, hb, 0);
    float s1 = 0.f, s2 = 0.f;
#pragma unroll
    for (int i = 0; i < 10; ++i) { s1 = dot2(hq[i], ONE2, s1); s2 = dot2(hq[i], hq[i], s2); }
    s1 = sum_g(s1); s2 = sum_g(s2);
    if (g == 0) st64(c, v2u{asu(s1), asu(s2)}, n16 * 8, OFF_HS + mtB * 1024 + cc * 128);
  }
  if (!QUIET3) release<CH_C1>(c);
  STAMP(10);
  if (QUIET3) DRAIN(); else VMWAIT(1);
  const bool last3 = arrive<C_ARR3>(c, FL3 + hp * 8 + cc);
  if (QUIET3 == 2) release<Q3N>(c);
  ctl_wait(c, C_CS, 1);
  STAMP(33);
  if (PW & 2) ctl_wait(c, C_ARR3, 4);

  {
    const int mt0 = 8 * qi + 2 * w;
    f32x4 acc[2][4];
#pragma unroll
    for (int t = 0; t < 4; ++t) { acc[0][t] = *LP(f32x4, L_CSUM + t * 64 + g * 16); PIN(acc[0][t]); }
    if (!(PW & 8)) flag_wait(c, FL3 + qi * 16 + (w >> 1) * 8 + n16, 8);
    else if (last3) { flag_wait(c, FL3 + qi * 16 + n16, 16); ctl_addc<C_ARR3>(); }
    else ctl_wait(c, C_ARR3, 5);
    if (QUIET3 == 1 || (QUIET3 == 2 && Q3N < 45)) release<CH_C1>(c);
    STAMP(11);
    v2u st[2][2];
#pragma unroll
    for (int m = 0; m < 2; ++m)
#pragma unroll
      for (int i = 0; i < 2; ++i) st[m][i] = ld64(c, lane * 8, OFF_HS + (mt0 + m) * 1024 + i * 512);
    v4u xh[2][20];
    xload<20, 0, 0, PRE_C, 20>(c, xh, xvoff, OFF_H + mt0 * 20480, 20480, rotC);
    float mneg[2], s2[2];
#pragma unroll
    for (int m = 0; m < 2; ++m) {
      mneg[m] = sum_g(asf(st[m][0].x) + asf(st[m][1].x)) * (-1.f / 640.f);
      s2[m] = asf(st[m][0].y) + asf(st[m][1].y);
    }
#pragma unroll
    for (int t = 0; t < 4; ++t) { acc[1][t] = acc[0][t] * mneg[1]; acc[0][t] = acc[0][t] * mneg[0]; PIN(acc[0][t]); PIN(acc[1][t]); }
    float sd[2], c2[2];
    gemm_pass<4, 20, CH_C1, 10, 20, 0, false>(c, dn, xvoff, acc, xh, [&](auto ic) AI {
      constexpr int f = decltype(ic)::v;
      if constexpr (f >= 0 && f % 4 == 1 && f / 4 + PRE_C < 20) xload<20, 0, f / 4 + PRE_C, 1, 20>(c, xh, xvoff, OFF_H + mt0 * 20480, 20480, rotC);
      if constexpr (f == 2 || f == 6) {
        constexpr int m = f == 6;
        const float var = fmaxf(sum_g(s2[m]) * (1.f / 640.f) - mneg[m] * mneg[m], 0.f) + 1e-5f;
        const float rs = __builtin_amdgcn_rsqf(var);
        sd[m] = var * rs;
        c2[m] = -1.4426950408889634f * rs;
        PIN(sd[m]); PIN(c2[m]);
      }
    });
    STAMP(12);
    const auto silu2 = [&](int m, const f32x4& a) AI {
      float y[4];
#pragma unroll
      for (int i = 0; i < 4; ++i) {
        const float e = __builtin_amdgcn_exp2f(a[i] * c2[m]);
        y[i] = a[i] * __builtin_amdgcn_rcpf(fmaf(e, sd[m], sd[m]));
      }
      return v2u{pk2(y[0], y[1]), pk2(y[2], y[3])};
    };
    const int fb = OFF_F + mt0 * 55296;
    f32x4 ac2[2][3];
#pragma unroll
    for (int t = 0; t < 3; ++t) ac2[0][t] = *LP(f32x4, L_CSUM + (4 + t) * 64 + g * 16);
#pragma unroll
    for (int m = 0; m < 2; ++m)
#pragma unroll
      for (int v = 0; v < 2; ++v) {
        const v2u a = silu2(m, acc[m][2 * v]), d = silu2(m, acc[m][2 * v + 1]);
        st128(c, v4u{a.x, a.y, d.x, d.y}, xvoff, fb + m * 55296 + (2 * qj + v) * 1024);
      }
#pragma unroll
    for (int t = 0; t < 3; ++t) { ac2[1][t] = ac2[0][t] * mneg[1]; ac2[0][t] = ac2[0][t] * mneg[0]; PIN(ac2[0][t]); PIN(ac2[1][t]); }
    STAMP(2);
    gemm_pass<3, 20, CH_C2, 10, 20, 0, false>(c, dn, xvoff, ac2, xh, [&](auto ic) AI {
      if constexpr (decltype(ic)::v == F4A) { asm volatile("s_waitcnt vmcnt(%0)" :: "i"((F4A + 1) / 10) : "memory"); ctl_addc<C_ARR4A>(); }
    });
    STAMP(13);
#pragma unroll
    for (int m = 0; m < 2; ++m) {
      const v2u a = silu2(m, ac2[m][0]), d = silu2(m, ac2[m][1]);
      st128(c, v4u{a.x, a.y, d.x, d.y}, xvoff, fb + m * 55296 + (32 + qj) * 1024);
    }
    if ((qj & 3) != 3) {
      const v2u a = silu2(0, ac2[0][2]), d = silu2(1, ac2[1][2]);
      const v2u ta = swap16(a.x, d.x), tb = swap16(a.y, d.y);
      const int l = 3 * (qj >> 2) + (qj & 3);
      st128(c, v4u{ta.x, tb.x, ta.y, tb.y}, (g & 1) * 55296 + (g >> 1) * 256 + n16 * 16, fb + (48 + (l >> 1)) * 1024 + (l & 1) * 512);
    }
  }
  STAMP(14);

  {
    const int xs = OFF_F + mtB * 55296, xm = kb ? -55296 : 55296;
    const bool early = ctl_read(C_GO4A) != 0u;
    CFENCE();
    if (!early) { DRAIN(); ctl_addc<C_ARR4B>(); ctl_wait(c, C_GO4A, 1); }
    v4u xf[2][20];
    xload<20, 0, 0, PRE_D, 16>(c, xf, xvoff, xs + kb * 16384, xm, rotD1);
    f32x4 acc[2][5];
#pragma unroll
    for (int t = 0; t < 5; ++t) { acc[0][t] = unpk4(hq[2 * t], hq[2 * t + 1]); PIN(acc[0][t]); }
    if (early) { asm volatile("s_waitcnt vmcnt(%0)" :: "i"(2 * PRE_D) : "memory"); ctl_addc<C_ARR4B>(); }
    STAMP(16);
    gemm_pass<5, 27, CH_D, 5, 20, 2, false>(c, dn, xvoff + kb * 5120, acc, xf, [&](auto ic) AI {
      constexpr int f = decltype(ic)::v, sn = f / 5 + PRE_D;
      if constexpr (f >= 0 && f % 5 == 1 && sn < 27) {
        if constexpr (sn == 16) {
          STAMP(17);
          ctl_wait(c, C_GO4B, 1);
          STAMP(18);
        }
        if constexpr (sn < 16) xload<20, 0, sn, 1, 16>(c, xf, xvoff, xs + kb * 16384, xm, rotD1);
        else xload<20, 16, sn - 16, 1, 11>(c, xf, xvoff, xs + 32768 + kb * 11264, xm, rotD2);
      }
    });
    STAMP(19);
    unsigned y[10];
    pair_reduce(c, w, C_XD0, 0, acc, y);
    const int l2 = opaque(lane);
    char* ob = out + (size_t)(16 * mtB * 1280 + cc * 160);
    const unsigned ro = (l2 & 15) * 1280 + (l2 & 16) * 2 + (l2 & 32) / 2;
#pragma unroll
    for (int u = 0; u < 2; ++u) {
      const v2u a = swap16(y[4 * u], y[4 * u + 2]), d = swap16(y[4 * u + 1], y[4 * u + 3]);
      *(GAS v4u*)(ob + ro + 64 * u) = v4u{a.x, d.x, a.y, d.y};
    }
    *(GAS v2u*)(ob + ((l2 & 15) * 1280 + (l2 >> 4) * 8 + 128)) = v2u{y[8], y[9]};
  }
  STAMP(15);
}

extern "C" void kernel_launch(void* const* d_in, const int* in_sizes, int n_in, void* d_out, int out_size, void* d_ws, size_t ws_size,
                              hipStream_t stream) {
  (void)hipFuncSetAttribute((const void*)mk_kernel, hipFuncAttributeMaxDynamicSharedMemorySize, LDS_BYTES);
  mk_kernel<<<256, 512, LDS_BYTES, stream>>>((const char*)d_in[0], (const char*)d_in[1], (const char*)d_in[2], (const char*)d_in[4],
                                             (const char*)d_in[5], (char*)d_out, (char*)d_ws);
}
```

```cpp
#include <hip/hip_runtime.h>
#include <stdint.h>

typedef unsigned v4u __attribute__((ext_vector_type(4)));
typedef unsigned v2u __attribute__((ext_vector_type(2)));
typedef short v4s __attribute__((ext_vector_type(4)));
typedef float f32x4 __attribute__((ext_vector_type(4)));
typedef float f32x2 __attribute__((ext_vector_type(2)));
typedef __bf16 bf16x2 __attribute__((ext_vector_type(2)));
typedef __bf16 bf16x8 __attribute__((ext_vector_type(8)));
typedef unsigned long long u64;
typedef __amdgpu_buffer_rsrc_t rsrc_t;
#define LAS __attribute__((address_space(3)))
#define GAS __attribute__((address_space(1)))
#define DI __device__ __forceinline__
#define AI __attribute__((always_inline))
#define RLX_AGENT __ATOMIC_RELAXED, __HIP_MEMORY_SCOPE_AGENT
#define SCHED() __builtin_amdgcn_sched_barrier(0)
#define CFENCE() asm volatile("" ::: "memory")
#define DRAIN() asm volatile("s_waitcnt vmcnt(0)" ::: "memory")
#define VMWAIT(n) asm volatile("s_waitcnt vmcnt(" #n ")" ::: "memory")
#define LGKM0() asm volatile("s_waitcnt lgkmcnt(0)" ::: "memory")
#define ENDPGM() asm volatile("s_endpgm" ::: "memory")
#define PIN(v) asm volatile("" : "+v"(v))
#define LP(T, a) ((LAS T*)(uintptr_t)(unsigned)(a))
constexpr int SC1 = 16;
constexpr unsigned ONE2 = 0x3f803f80u;

#define DEP_A 12
#define DEP_P 10
#define LPRIO 0
#define REL_B 5
#define REL_C 3
#define F4A 18
#define RPK_F 24
#define RPK_S 35
#define RPK_PUB 2
#define PQX 0
#define CMAP 1
#define UMAX 216
#define CAPL 1
#define TCH 166
#define TN 91
#define TCH_T 0
#define LATE_L 1
#define LATE_S 0
#define QUIET3 2
#define WGF 1
#define Q3N 45
#define BMAP 0
#define ROTS 0
#define PW 12
#define ROTT 0x10876532u
#define PADT 1
#define REL_H 0
#define MROT 1
#define DEP_B 12
#define RPK_P 30
#define CS_T 32
#define FLR_T 1
#define XS 5
#define HOLD 0
#define QUIET 1
#define PSL 0
#define PD 0
#define XPRE 1
#define ROTA 1
#define STR_(x) #x
#define STR(x) STR_(x)

#define ROT 1
#define PRE_B 6
#define PRE_C 6
#define NSC 6
#define Q4 1
#define SREL 1
#define RD1 0
#define E4F 134
#define G4F 21
#define PRE_D 6

constexpr int OFF_EPOCH = 0;
constexpr int OFF_FLAGS = 4096;
constexpr int OFF_CS = 655360;
constexpr int OFF_HS = 819200;
constexpr int OFF_KVP = 1 << 20;
constexpr int KVP_STR = 1920;
constexpr int OFF_WB = 2 << 20;
constexpr int OFF_WC = OFF_WB + 819200;
constexpr int OFF_WD = OFF_WC + 2293760;
constexpr int OFF_O = OFF_WD + 2211840;
constexpr int OFF_H = OFF_O + 2621440;
constexpr int OFF_F = OFF_H + 2621440;
constexpr int WS_BYTES = OFF_F + 7077888;
constexpr int OFF_PROF = 32 << 20;
enum { FLR = 0, FL1 = 256, FL2 = 512, FL3 = 768, FL4A = 1024, FL4B = 1280, FL_N = 1536 };
constexpr unsigned EPOCH_STEP = 0x9E3779B6u;

constexpr int RCH = 13;
constexpr int L_SCR = RCH * 10240;
constexpr int L_RPK = L_SCR + 20480;
constexpr int L_CSUM = L_RPK + 4096;
constexpr int L_DONE = L_CSUM + 512;
constexpr int L_LAND = L_DONE + 1024;
constexpr int L_FLG = L_LAND + 512;
constexpr int L_STA = L_FLG + 1024;
constexpr int L_CK = L_STA + 1024;
constexpr int L_CQ = L_CK + 1024;
constexpr int L_CTL = L_CQ + 512;
constexpr int LDS_BYTES = L_CTL + 128;
static_assert(LDS_BYTES <= 163840, "lds");
enum { C_CAP = 0, C_CSK, C_CSQ, C_PUB, C_CS, C_ARR1, C_GO1, C_XB0, C_XB1, C_XD0, C_XD1, C_ARR2, C_ARR3, C_ARR4A, C_GO4A, C_ARR4B, C_GO4B, C_ISS4 };

constexpr int CH_Q = 30, CH_B = 35, CH_C1 = 45, CH_C2 = 53, CH_D = 59, CH_END = 86;
constexpr int BLK = 26624;
static_assert(5 * BLK == RCH * 10240, "ring");
constexpr int needp(int ch) { return ch + 1; }
constexpr int slot(int ch) { return (ch - CH_Q) % RCH; }
constexpr int KVSLOT = 9;

struct Ctx {
  rsrc_t rs, rt;
  unsigned dm;
  GAS char* ws;
  unsigned ep;
  int lane;
  unsigned budget;
};

DI f32x4 mfma(v4u a, v4u b, f32x4 c) {
  return __builtin_amdgcn_mfma_f32_16x16x32_bf16(__builtin_bit_cast(bf16x8, a), __builtin_bit_cast(bf16x8, b), c, 0, 0, 0);
}
DI float asf(unsigned u) { return __builtin_bit_cast(float, u); }
DI unsigned asu(float f) { return __builtin_bit_cast(unsigned, f); }
DI unsigned pk2(float a, float b) { f32x2 f = {a, b}; return __builtin_bit_cast(unsigned, __builtin_convertvector(f, bf16x2)); }
DI float bflo(unsigned u) { return asf(u << 16); }
DI float bfhi(unsigned u) { return asf(u & 0xffff0000u); }
DI f32x4 unpk4(unsigned a, unsigned b) { return f32x4{bflo(a), bfhi(a), bflo(b), bfhi(b)}; }
DI float dot2(unsigned a, unsigned b, float c) {
  return __builtin_amdgcn_fdot2_f32_bf16(__builtin_bit_cast(bf16x2, a), __builtin_bit_cast(bf16x2, b), c, false);
}
DI f32x4 add4(f32x4 a, f32x4 b) {
  f32x4 r;
#pragma unroll
  for (int i = 0; i < 4; ++i) asm("v_add_f32 %0, %1, %2" : "=v"(r[i]) : "v"(a[i]), "v"(b[i]));
  return r;
}
DI v4u ld128(const Ctx& c, int voff, int soff) { return __builtin_amdgcn_raw_buffer_load_b128(c.rs, voff, soff, SC1); }
DI v2u ld64(const Ctx& c, int voff, int soff) { return __builtin_amdgcn_raw_buffer_load_b64(c.rs, voff, soff, SC1); }
DI void st128(const Ctx& c, v4u v, int voff, int soff) { __builtin_amdgcn_raw_buffer_store_b128(v, c.rs, voff + soff, 0, SC1); }
DI void st64(const Ctx& c, v2u v, int voff, int soff) { __builtin_amdgcn_raw_buffer_store_b64(v, c.rs, voff, soff, SC1); }
DI void st32(const Ctx& c, unsigned v, int voff, int soff) { __builtin_amdgcn_raw_buffer_store_b32(v, c.rs, voff, soff, SC1); }
DI u64 now() { return __builtin_amdgcn_s_memrealtime(); }

DI void ctl_add(int i) {
  asm volatile("v_mov_b32 v254, %0\n\tv_mov_b32 v255, %1\n\ts_mov_b64 exec, 1\n\tds_add_u32 v254, v255\n\ts_mov_b64 exec, -1"
               :: "s"(L_CTL + 4 * i), "i"(L_CTL) : "memory", "v254", "v255");
}
template <int I> DI void ctl_addc() {
  asm volatile("v_mov_b32 v254, %0\n\ts_mov_b64 exec, 1\n\tds_add_u32 v254, v254 offset:%1\n\ts_mov_b64 exec, -1"
               :: "i"(L_CTL), "i"(4 * I) : "memory", "v254");
}
template <int SLP = 0> DI void ctl_wait(Ctx& c, int i, int n) {
  unsigned t;
  asm volatile(
      "v_mov_b32 v254, %2\n"
      ".Lw%=:\n\t"
      ".if %4\n\ts_sleep %4\n\t.endif\n\t"
      "ds_read_b32 v255, v254\n\t"
      "s_sub_u32 %0, %0, 1\n\t"
      "s_max_i32 %0, %0, 0\n\t"
      "s_waitcnt lgkmcnt(0)\n\t"
      "v_readfirstlane_b32 %1, v255\n\t"
      "s_cmp_lt_u32 %1, %3\n\t"
      "s_cselect_b32 %1, %0, 0\n\t"
      "s_cmp_lg_u32 %1, 0\n\t"
      "s_cbranch_scc1 .Lw%="
      : "+{s71}"(c.budget), "=&s"(t) : "s"(L_CTL + 4 * i), "s"(n * L_CTL), "i"(SLP) : "memory", "scc", "v254", "v255");
}
DI unsigned ctl_read(int i) { return __builtin_amdgcn_readfirstlane(*LP(volatile unsigned, L_CTL + 4 * i)); }
DI void flag_set(const Ctx& c, int idx) {
  asm volatile("v_mov_b32 v255, %1\n\ts_mov_b64 exec, 1\n\tbuffer_store_dword v255, off, %0, %2 sc1\n\ts_mov_b64 exec, -1"
               :: "s"(c.rs), "s"(c.ep), "s"(OFF_FLAGS + idx * 128) : "memory", "v255");
}
template <int I> DI bool arrive(const Ctx& c, int idx) {
  unsigned n;
  asm volatile("v_mov_b32 v254, %1\n\ts_mov_b64 exec, 1\n\tds_add_rtn_u32 v255, v254, v254 offset:%2\n\ts_mov_b64 exec, -1\n\ts_waitcnt lgkmcnt(0)\n\tv_readfirstlane_b32 %0, v255"
               : "=s"(n) : "i"(L_CTL), "i"(4 * I) : "memory", "v254", "v255");
  if (n == 3 * L_CTL) flag_set(c, idx);
  return n == 3 * L_CTL;
}
DI bool flag_test(const Ctx& c, int idx, int nl) {
  unsigned v = c.ep;
  if (c.lane < nl) v = __hip_atomic_load((GAS unsigned*)(c.ws + OFF_FLAGS + idx * 128), RLX_AGENT);
  const bool ok = !__any(v != c.ep);
  CFENCE();
  return ok;
}
DI void flag_wait(const Ctx& c, int off, int nl) {
  unsigned n = 1u << 17;
  while (!flag_test(c, off, nl) && --n != 0) {}
}
DI void flag_copy(Ctx& c, int row, int idx0, u64 mask) {
  asm volatile(
      "s_mov_b32 m0, %3\n\t"
      "v_lshl_add_u32 v254, %5, 2, %3\n\t"
      "s_mov_b64 exec, %6\n"
      ".Lf%=:\n\t"
      ".if " STR(PSL) "\n\ts_sleep " STR(PSL) "\n\t.endif\n\t"
      "buffer_load_dword %7, %1, %4 offen sc1 lds\n\t"
      "s_waitcnt vmcnt(" STR(PD) ")\n\t"
      "ds_read_b32 v255, v254\n\t"
      "s_sub_u32 %0, %0, 64\n\t"
      "s_cbranch_scc1 .Lg%=\n\t"
      "s_waitcnt lgkmcnt(0)\n\t"
      "v_cmp_ne_u32 vcc, %2, v255\n\t"
      "s_and_b64 exec, exec, vcc\n\t"
      "s_cbranch_scc1 .Lf%=\n\t"
      "s_branch .Le%=\n"
      ".Lg%=:\n\t"
      "s_mov_b32 %0, 0\n"
      ".Le%=:\n\t"
      "s_mov_b64 exec, -1\n\t"
      "s_waitcnt vmcnt(0) lgkmcnt(0)"
      : "+s"(c.budget) : "s"(c.rs), "s"(c.ep), "s"(L_FLG + row * 256), "s"(OFF_FLAGS + idx0 * 128), "v"(c.lane), "s"(mask), "v"(c.lane * 128)
      : "memory", "scc", "vcc", "v254", "v255");
}
template <int ROW> DI void flag_seen(Ctx& c, unsigned dn, u64 mask) {
  asm volatile(
      ".Lf%=:\n\t"
      "ds_read_b32 v255, %1 offset:%4\n\t"
      "s_sub_u32 %0, %0, 1\n\t"
      "s_cbranch_scc1 .Lg%=\n\t"
      "s_waitcnt lgkmcnt(0)\n\t"
      "v_cmp_ne_u32 vcc, %2, v255\n\t"
      "s_and_b64 vcc, vcc, %3\n\t"
      "s_cbranch_scc1 .Lf%=\n\t"
      ".subsection 1\n"
      ".Lg%=:\n\t"
      "s_mov_b32 %0, 0\n\t"
      "s_waitcnt lgkmcnt(0)\n\t"
      "s_branch .Le%=\n\t"
      ".subsection 0\n"
      ".Le%=:"
      : "+{s71}"(c.budget) : "v"(dn), "s"(c.ep), "s"(mask), "i"(L_FLG + ROW * 256 - L_LAND - 128) : "memory", "scc", "vcc", "v255");
}
DI void done_wait(Ctx& c, unsigned n) {
  unsigned k = 0;
  while (__any(*LP(volatile unsigned, L_DONE + c.lane * 16) < n) && ++k < (1u << 18)) __builtin_amdgcn_s_sleep(2);
  CFENCE();
}
DI void touch(const char* p, int bytes, int off, int n, int pitch) {
  const rsrc_t r = __builtin_amdgcn_make_buffer_rsrc((void*)p, 0, bytes, 0x00020000);
  asm volatile(".Lt%=:\n\t"
               "s_buffer_load_dwordx2 s[98:99], %2, %0 offset:60\n\t"
               "s_add_u32 %0, %0, %3\n\t"
               "s_sub_u32 %1, %1, 1\n\t"
               "s_cmp_gt_i32 %1, 0\n\t"
               "s_cbranch_scc1 .Lt%=\n\t"
               "s_waitcnt lgkmcnt(0)"
               : "+s"(off), "+s"(n) : "s"(r), "s"(pitch) : "memory", "scc", "s98", "s99");
}
DI void land_wait(const Ctx& c, unsigned n) {
  unsigned k = 0;
  while (__any(*LP(volatile unsigned, L_LAND + 128 + c.lane * 4) < n) && ++k < (1u << 16)) __builtin_amdgcn_s_sleep(4);
  CFENCE();
}
#define STAMP(i) do {} while (0)
#define STL(i) do {} while (0)
#define DUMPL(...) do {} while (0)

DI v2u swap16(unsigned a, unsigned b) { return __builtin_amdgcn_permlane16_swap(a, b, false, false); }
DI v2u swap32(unsigned a, unsigned b) { return __builtin_amdgcn_permlane32_swap(a, b, false, false); }
DI float sum_g(float v) {
  v2u r = swap16(asu(v), asu(v));
  v = asf(r.x) + asf(r.y);
  r = swap32(asu(v), asu(v));
  return asf(r.x) + asf(r.y);
}
DI v2u tr1(unsigned a, int off) { return __builtin_bit_cast(v2u, __builtin_amdgcn_ds_read_tr16_b64_v4i16(LP(v4s, a + off))); }
DI v4u tr2(unsigned a, int off, int hioff) {
  const v2u lo = tr1(a, off), hi = tr1(a, off + hioff);
  return v4u{lo.x, lo.y, hi.x, hi.y};
}

DI int gam0(int j) { return 27 * (j >> 2) + 7 * (j & 3); }

template <int CH> DI void ring_wait(Ctx& c) {
  asm volatile("s_movk_i32 s70, %1\n\ts_call_b64 s[72:73], .Lmk_ring"
               : "+{s71}"(c.budget) : "i"(needp(CH)) : "memory", "scc", "vcc", "s70", "s72", "s73", "v254", "v255");
}
template <int CH> DI void ring_cmp(unsigned la) { asm volatile("v_cmp_gt_u32 vcc, %1, %0" :: "v"(la), "i"(needp(CH)) : "vcc"); }
template <int CH> DI void ring_br(Ctx& c) {
  asm volatile("s_cbranch_vccnz .Lrs%=\n\t.subsection 1\n.Lrs%=:\n\ts_movk_i32 s70, %1\n\ts_call_b64 s[72:73], .Lmk_ring\n\ts_branch .Lrb%=\n\t.subsection 0\n.Lrb%=:"
               : "+{s71}"(c.budget) : "i"(needp(CH)) : "memory", "scc", "vcc", "s70", "s72", "s73", "v254", "v255");
}
#define TOUCH(x) asm volatile("" :: "v"(x))
template <int N> DI void release(const Ctx& c) {
  asm volatile("s_mov_b32 m0, %1\n\ts_movk_i32 s70, %2\n\tbuffer_load_dword off, %0, s70 lds" :: "s"(c.rt), "s"(c.dm), "i"(4 * N) : "memory", "s70");
}
constexpr int upto(int ch) {
  if (ch == CH_Q) return CH_B - 1;
  if (ch == CH_B) return CH_C1 - 1;
  if (ch == CH_C1) return CH_C1 + 6;
  if (ch > CH_C1 + 6 && ch < CH_D - 1) return (ch - CH_C1) % 2 ? ch + 1 : -1;
  if (ch == CH_D - 1) return ch;
  if (ch == CH_D) return CH_D + 6;
  if (ch == CH_D + 7) return CH_D + 12;
  if (ch >= CH_D + 13 && (ch - CH_D - 13) % 2 == 0) return ch + 1;
  return -1;
}
constexpr bool released(int n) {
  if (n + RCH > CH_END + 1) return false;
  if (n <= CH_C1) return n == CH_B + REL_B;
  if (n <= CH_D) return n >= CH_C1 + REL_C;
  return n > CH_D + NSC && ((n - CH_B) % 2 == 0 || n <= CH_D + RD1) && n < CH_END + 1 - RCH;
}
template <int I> struct IC { static constexpr int v = I; };
template <int B, int E, class Fn> DI void static_for(Fn&& fn) {
  if constexpr (B < E) { fn(IC<B>{}); static_for<B + 1, E>(fn); }
}
template <int D> DI void bump(unsigned& b) { if constexpr (D != 0) asm volatile("v_add_u32 %0, %1, %0" : "+v"(b) : "i"(D)); }
constexpr int breg(int b) { return ((b % 5) / 2) * 2 * BLK; }
constexpr int bin(int b) { return ((b % 5) % 2) * BLK; }
constexpr int region(int ch) { return (slot(ch) / 6) * 61440; }
constexpr int inreg(int ch) { return (slot(ch) % 6) * 10240; }

template <int TL, bool Z> DI void mm(f32x4& acc, const v4u& wf, const v4u& x) {
  if constexpr (Z && TL) asm volatile("v_mfma_f32_16x16x32_bf16 %0, %1, %2, 0" : "=&v"(acc) : "v"(wf), "v"(x));
  else if constexpr (Z) asm volatile("v_mfma_f32_16x16x32_bf16 %0, %2, %1, 0" : "=&v"(acc) : "v"(wf), "v"(x));
  else if constexpr (TL) asm volatile("v_mfma_f32_16x16x32_bf16 %0, %1, %2, %0" : "+v"(acc) : "v"(wf), "v"(x));
  else asm volatile("v_mfma_f32_16x16x32_bf16 %0, %2, %1, %0" : "+v"(acc) : "v"(wf), "v"(x));
}
constexpr int ord_s(int f) { return f < 70 ? f / 7 : f < 110 ? 10 + ((f - 70) % 20) / 2 : 10 + (f - 110) / 3; }
constexpr int ord_t(int f) { return f < 70 ? f % 7 : f < 110 ? 2 * ((f - 70) / 20) + (f & 1) : 4 + (f - 110) % 3; }
struct NoMid { template <class T> DI void operator()(T) const {} };
template <int NT, int NKS, int CH0, int FPC, int XW, int ZM, bool RAW, class Hook, class Mid = NoMid>
DI void gemm_pass(Ctx& c, unsigned dn, unsigned wb, f32x4 (&acc)[2][NT], v4u (&x)[2][XW], const v4u* sw, Hook&& hook, Mid&& mid = Mid{}) {
  constexpr int LA = 3, WBN = LA + 1, NF = NKS * NT, NS = CH0 == CH_D ? NSC * FPC : 0;
  v4u wf[WBN];
  unsigned la;
  bump<region(CH0)>(wb);
  SCHED();
  static_for<-LA, NF>([&](auto ic) AI {
    constexpr int f = decltype(ic)::v, fn = f + LA;
    constexpr int s = f < 0 ? 0 : NT == 7 ? ord_s(f) : f / NT, t = f < 0 ? 0 : NT == 7 ? ord_t(f) : f % NT;
    constexpr int ch = CH0 + fn / FPC, off = inreg(ch) + (fn % FPC) * (RAW ? 32 : 1024);
    v2u lo;
    constexpr auto chk = [](int i) { return i > 0 && i < NF && i % FPC == 0 && upto(CH0 + i / FPC) >= 0; };
    constexpr bool rel = f >= 0 && (f + 1) % FPC == 0 && released(CH0 + (f + 1) / FPC);
    if constexpr (f >= 0) { mm<1, (ZM & 1) && s == 0>(acc[0][t], f < NS ? sw[f] : wf[f % WBN], x[0][s % XW]); SCHED(); }
    if constexpr (fn < NF) {
      if constexpr (fn == 0 && NS == 0) ring_wait<upto(ch)>(c);
      if constexpr (chk(fn)) ring_br<upto(ch)>(c);
      if constexpr (RAW) lo = tr1(wb, off);
      else if constexpr (fn >= NS) wf[fn % WBN] = *LP(v4u, wb + off);
      SCHED();
    }
    mid(ic);
    SCHED();
    if constexpr (f >= 0) { mm<1, (ZM & 2) && s == 0>(acc[1][t], f < NS ? sw[f] : wf[f % WBN], x[1][s % XW]); SCHED(); }
    if constexpr (RAW && fn < NF) { const v2u hi = tr1(wb, off + 2560); wf[fn % WBN] = v4u{lo.x, lo.y, hi.x, hi.y}; SCHED(); }
    if constexpr (fn + 1 < NF && (fn + 1) % FPC == 0) bump<region(ch + 1) - region(ch)>(wb);
    if constexpr (rel) release<CH0 + (f + 1) / FPC>(c);
    if constexpr (chk(fn + 4)) la = *LP(unsigned, dn);
    hook(ic);
    SCHED();
    if constexpr (chk(fn + 1)) { ring_cmp<upto(CH0 + (fn + 1) / FPC)>(la); SCHED(); }
  });
  asm volatile("s_nop 7");
  SCHED();
}
template <int XW, int S0, int S1, int N, int LEN> DI void xload(const Ctx& c, v4u (&x)[2][XW], int xvoff, int xsoff, int xmt, int rot) {
#pragma unroll
  for (int s = S1; s < S1 + N; ++s) {
    const int o = xsoff + (rot + s >= LEN ? rot + s - LEN : rot + s) * 1024;
    x[0][(S0 + s) % XW] = ld128(c, xvoff, o);
    x[1][(S0 + s) % XW] = ld128(c, xvoff, o + xmt);
  }
  SCHED();
}
DI int opaque(int v) { asm volatile("" : "+v"(v)); return v; }
constexpr float LOG2E = 1.4426950408889634f, PHID = 1.4426950408889634e-6f;
DI v2u phi4(f32x4 u) {
  f32x4 e, m;
#pragma unroll
  for (int i = 0; i < 4; ++i) { e[i] = __builtin_amdgcn_fmed3f(__builtin_amdgcn_exp2f(u[i]), 0.f, 1.f); m[i] = fmaxf(u[i], PHID); }
  const f32x4 y = m * 0.6931471805599453f + e;
  return v2u{pk2(y[0], y[1]), pk2(y[2], y[3])};
}

DI void pair_reduce(Ctx& c, int w, int cx, int n0, f32x4 (&acc)[2][5], unsigned (&y)[10]) {
  const unsigned lo = opaque(c.lane) * 16 + L_SCR;
  LAS f32x4* mine = LP(f32x4, lo + w * 5120);
  LAS f32x4* other = LP(f32x4, lo + (w ^ 1) * 5120);
#pragma unroll
  for (int t = 0; t < 5; ++t) mine[t * 64] = acc[1][t];
  ctl_add(cx + (w >> 1));
  ctl_wait(c, cx + (w >> 1), n0 + 2);
#pragma unroll
  for (int t = 0; t < 5; ++t) {
    const f32x4 o = acc[0][t] + other[t * 64];
    y[2 * t] = pk2(o[0], o[1]); y[2 * t + 1] = pk2(o[2], o[3]);
  }
}
DI int mod10(int v) { return v >= 20 ? v - 20 : v >= 10 ? v - 10 : v; }

__global__ void __launch_bounds__(512, 2)
mk_kernel(const char* __restrict__ x, const char* __restrict__ wqkv, const char* __restrict__ wo, const char* __restrict__ w1,
          const char* __restrict__ w2, char* __restrict__ out, char* ws) {
  u64 cpc; unsigned clen, toff;
  asm volatile(
      "s_getpc_b64 %0\n"
      ".Lpc%=:\n\t"
      "s_mov_b32 %1, .Lend%=-.Lpc%=-4\n\t"
      "s_mov_b32 %2, .Lmk_tab-.Lpc%=\n\t"
      ".subsection 1\n"
      ".Lmk_ring:\n\t"
      "v_mov_b32 v254, %3\n\t"
      "ds_read2_b32 v[254:255], v254 offset1:64\n\t"
      "s_sub_u32 s71, s71, 1\n\t"
      "s_cbranch_scc1 .Lmk_give\n\t"
      "s_waitcnt lgkmcnt(0)\n\t"
      "v_min_u32 v254, v254, v255\n\t"
      "v_cmp_gt_u32 vcc, s70, v254\n\t"
      "s_cbranch_vccnz .Lmk_ring\n\t"
      "s_setpc_b64 s[72:73]\n"
      ".Lmk_give:\n\t"
      "s_waitcnt lgkmcnt(0)\n\t"
      "s_mov_b32 s71, 0\n\t"
      "s_setpc_b64 s[72:73]\n\t"
      ".subsection 2\n"
      ".Lend%=:\n\t"
      ".fill 128, 4, 0xBF800000\n\t"
      ".subsection 0"
      : "=s"(cpc), "=s"(clen), "=s"(toff) : "i"(L_LAND));
  asm volatile("" :: "s"(x), "s"(wqkv), "s"(wo), "s"(w1), "s"(w2), "s"(out), "s"(ws));
  const int tid = threadIdx.x, w = __builtin_amdgcn_readfirstlane(tid >> 6);
  int lane = tid & 63;
  const int b = blockIdx.x, xc = b & 7, sx = b >> 3;
  const int pi = 4 * (xc >> 1) + (sx >> 3), pj = (xc & 1) * 8 + (sx & 7);
  const int pq_ = pj ^ PQX;
  const int hp = BMAP ? 8 * (xc >> 1) + (sx >> 2) : 4 * xc + (sx >> 3), cc = BMAP ? 4 * (xc & 1) + (sx & 3) : sx & 7;
  const int qi = CMAP ? pi : 2 * xc + (sx >> 4), qj = CMAP ? pj : sx & 15;
  const int r8 = BMAP ? sx >> 2 : sx & 7, r4 = BMAP ? sx & 3 : sx >> 3;
  const int rotB = mod10(((5 * r8) >> 2) + (BMAP || (ROTS & 1) ? (5 * r4) >> 1 : 0)), rotC = 5 * (((qj >> 1) + ((ROTS & 2) ? sx >> 3 : 0)) & 1), rotD1 = (r8 + ((5 * r4) >> 1)) & 7;
  const int rd2 = ((11 * r8) >> 3) + 3 * r4, rotD2 = rd2 >= 11 ? rd2 - 11 : rd2;
  int n16 = lane & 15, g = lane >> 4;
  Ctx c;
  c.lane = lane;
  c.rt = __builtin_amdgcn_make_buffer_rsrc((void*)(cpc + toff), 0, 512, 0x00020000);
  if (__builtin_expect((w >> 1) == 2, 1)) {
    __builtin_amdgcn_s_setprio(LPRIO);
    const int rot = ROTA == 0 ? 0 : ROTA == 2 ? 5 * (sx & 1) : mod10(((ROTT >> (4 * (sx & 7))) & 15) + ((5 * (sx >> 3)) >> 1) + (xc >> 1));
    const int Lw = w - 4;
    const int ln = opaque(lane), xr = ln >> 3;
    int xv[4], ka[5], qa[5];
    bool hi_[5];
    xv[0] = xr * 1280 + (((ln & 7) ^ (xr >> 1)) * 16) + 3072; xv[1] = (xv[0] ^ 64) + 10240 - 1024; xv[2] = xv[0] - 2048; xv[3] = xv[1] - 2048;
    const auto raw_addr = [&](int (&a)[5], int lo, int hi) AI {
#pragma unroll
      for (int i = 0; i < 5; ++i) {
        const unsigned m = ln * 205 + 64 * 205 * i, rho = m >> 11;
        const int ch16 = ln * 16 + 1024 * i - 160 * rho;
        const int k = ((m >> 10) & 24) | ((m >> 13) & 4) | (rho & 3);
        hi_[i] = ch16 >= 80;
        a[i] = k * 3840 + ch16 + 3072 - (i & 3) * 1024 + (ch16 < 80 ? lo : hi);
        PIN(a[i]);
      }
    };
#pragma unroll
    for (int i = 0; i < 4; ++i) PIN(xv[i]);
    unsigned lim = RCH, bud = 1u << 20, key = 0;
    const unsigned lw = Lw * 5120;
    int sxr = (128 * pi + 64 * Lw) * 1280, sr = rot * 128, sl = Lw * 8192;
    asm volatile("" : "+s"(sxr), "+s"(sr), "+s"(sl));
    SCHED();
    const rsrc_t rx = __builtin_amdgcn_make_buffer_rsrc((void*)(x - 3072), 0, 2048 * 1280 + 3072, 0x00020000);
#define MK_ST "+{s70}"(lim), "+{s71}"(bud), "+{s84}"(key), "+{s80}"(sr), "+{s85}"(sl)
#define MK_IN "{s81}"(lw), "{s76}"(L_LAND + Lw * 256), [rt] "s"(c.rt)
#define MK_CL "memory", "scc", "s72", "s73", "s74", "s75", "s77", "s78", "s79", "s82", "s83", "s86", "s87", "s88", "s90", "s91", "v248", "v252", "v253", "v254", "v255"
    asm volatile(
        ".macro MK_LD dep, ops:vararg\n\t"
        ".if \\dep\n\ts_waitcnt vmcnt(\\dep)\n\t.endif\n\t"
        "buffer_load_dwordx4 \\ops lds\n\t"
        ".endm\n\t"
        ".macro MK_FLAG rt, add\n\t"
        "s_mov_b32 m0, s76\n\t"
        "s_lshl2_add_u32 s83, s84, 4*\\add\n\t"
        "buffer_load_dword off, \\rt, s83 lds\n\t"
        ".endm\n\t"
        ".macro MK_HEAD key\n\t"
        "s_cmp_gt_i32 s70, \\key\n\t"
        "s_cbranch_scc1 1f\n\t"
        "s_mov_b32 s74, \\key\n\t"
        "s_call_b64 s[72:73], .Lmk_lslow\n"
        "1:\n\t"
        ".endm\n\t"
        ".macro MK_RAW dep, a0, a1, a2, a3, a4, rs, step\n\t"
        "MK_LD \\dep, \\a0, \\rs, s82 offen\n\t"
        "s_add_u32 s78, s82, \\step\n\t"
        "MK_LD \\dep, \\a1, \\rs, s82 offen offset:1024\n\t"
        "s_sub_u32 s79, s78, 2457600\n\t"
        "MK_LD \\dep, \\a2, \\rs, s82 offen offset:2048\n\t"
        "MK_LD \\dep, \\a3, \\rs, s82 offen offset:3072\n\t"
        "s_add_u32 m0, m0, 4096\n\t"
        "s_min_u32 s78, s78, s79\n\t"
        "MK_LD \\dep, \\a4, \\rs, s82 offen\n\t"
        "s_mov_b32 s82, s78\n\t"
        ".endm\n\t"
        ".macro MK_X4 dep, a0, a1, a2, a3, rs\n\t"
        "MK_LD \\dep, \\a0, \\rs, s77 offen\n\t"
        "s_add_u32 s78, s77, 20480\n\t"
        "MK_LD \\dep, \\a1, \\rs, s77 offen offset:1024\n\t"
        "MK_LD \\dep, \\a2, \\rs, s78 offen offset:2048\n\t"
        "s_add_u32 s77, s77, 40960\n\t"
        "MK_LD \\dep, \\a3, \\rs, s78 offen offset:3072\n\t"
        ".endm\n\t"
        ".macro MK_X dep, a0, a1, a2, a3, rs, sx\n\t"
        "s_mov_b32 m0, s85\n\t"
        "s_add_u32 s77, s80, \\sx\n\t"
        "MK_X4 \\dep, \\a0, \\a1, \\a2, \\a3, \\rs\n\t"
        "s_add_u32 m0, s85, 4096\n\t"
        "s_add_u32 s80, s80, 128\n\t"
        "MK_X4 \\dep, \\a0, \\a1, \\a2, \\a3, \\rs\n\t"
        "s_sub_u32 s79, s80, 1280\n\t"
        "s_min_u32 s80, s80, s79\n\t"
        ".endm\n\t"
        ".subsection 1\n"
        ".Lmk_lsleep:\n\t"
        "s_sleep 1\n"
        ".Lmk_lslow:\n\t"
        "v_mov_b32 v248, %[dn]\n\t"
        "ds_read2st64_b32 v[252:253], v248 offset1:1\n\t"
        "ds_read2st64_b32 v[254:255], v248 offset0:2 offset1:3\n\t"
        "ds_read_b32 v248, v248 offset:%[cap]\n\t"
        "s_sub_u32 s71, s71, 1\n\t"
        "s_cbranch_scc1 .Lmk_lgive\n\t"
        "s_waitcnt lgkmcnt(0)\n\t"
        "v_min3_u32 v252, v252, v253, v254\n\t"
        "v_min_u32 v252, v252, v255\n\t"
        "v_readfirstlane_b32 s75, v248\n\t"
        "v_readfirstlane_b32 s70, v252\n\t"
        "s_add_u32 s70, s70, 13\n\t"
        "s_min_u32 s70, s70, s75\n\t"
        "s_cmp_gt_i32 s70, s74\n\t"
        "s_cbranch_scc0 .Lmk_lsleep\n\t"
        "s_setpc_b64 s[72:73]\n"
        ".Lmk_lgive:\n\t"
        "s_endpgm\n\t"
        ".subsection 0\n\t"
        "MK_X 0, %[x0], %[x1], %[x2], %[x3], %[rx], %[sx]"
        : MK_ST
        : MK_IN, [x0] "v"(xv[0]), [x1] "v"(xv[1]), [x2] "v"(xv[2]), [x3] "v"(xv[3]), [rx] "s"(rx), [sx] "s"(sxr),
          [dn] "i"(L_DONE), [cap] "i"(L_CTL + 4 * C_CAP - L_DONE)
        : MK_CL);
    SCHED();
    *LP(unsigned, L_LAND + Lw * 256 + ln * 4) = 0u;
    raw_addr(ka, 1280, 2480);
    const int ok = Lw * 122880 + pj * 80 - 3072;
    const rsrc_t rk = __builtin_amdgcn_make_buffer_rsrc((void*)(wqkv + ok), 0, 640 * 3840 - ok, 0x00020000);
    asm volatile(
        "s_mov_b32 s82, %[sk]\n\t"
        "s_waitcnt lgkmcnt(0)\n"
        ".Lmk_blk:\n\t"
        "s_add_u32 m0, s85, %[kl]\n\t"
        "s_add_u32 s85, s85, 26624\n\t"
        "MK_RAW " STR(DEP_A) ", %[k0], %[k1], %[k2], %[k3], %[k4], %[rk], 245760\n\t"
        "MK_FLAG %[rt], 3\n\t"
        "s_sub_u32 s79, s85, 133120\n\t"
        "s_min_u32 s85, s85, s79\n\t"
        "s_add_u32 s84, s84, 3\n\t"
        "s_cmpk_eq_u32 s84, 30\n\t"
        "s_cbranch_scc1 .Lmk_blkend\n\t"
        "s_cmpk_lg_u32 s84, 3*" STR(XPRE) "\n\t"
        "s_cbranch_scc1 .Lmk_nobar\n\t"
        "s_barrier\n"
        ".Lmk_nobar:\n\t"
        "MK_HEAD s84\n\t"
        "MK_X " STR(DEP_A) ", %[x0], %[x1], %[x2], %[x3], %[rx], %[sx]\n\t"
        "s_branch .Lmk_blk\n"
        ".Lmk_blkend:\n\t"
        ".purgem MK_X4\n\t.purgem MK_X"
        : MK_ST
        : MK_IN, [x0] "v"(xv[0]), [x1] "v"(xv[1]), [x2] "v"(xv[2]), [x3] "v"(xv[3]), [rx] "s"(rx), [sx] "s"(sxr),
          [kl] "s"(16384 - Lw * 3072), [k0] "v"(ka[0]), [k1] "v"(ka[1]), [k2] "v"(ka[2]), [k3] "v"(ka[3]), [k4] "v"(ka[4]), [rk] "s"(rk), [sk] "s"(rot * 245760)
        : MK_CL);
    SCHED();
#pragma unroll
    for (int i = 0; i < 5; ++i) { qa[i] = ka[i] - 1280 + (hi_[i] ? 32 * 3840 - 80 - 1200 : 0); PIN(qa[i]); }
    const int oq = pq_ * 80 - 3072;
    const rsrc_t rw = __builtin_amdgcn_make_buffer_rsrc((void*)(wqkv + oq), 0, 640 * 3840 - oq, 0x00020000);
    char* ws2 = ws;
    asm volatile("" : "+s"(ws2));
    const rsrc_t rs = __builtin_amdgcn_make_buffer_rsrc((void*)ws2, 0, WS_BYTES, 0x00020000);
    asm volatile(
        "s_mov_b32 s82, %[sq]\n\t"
        "s_mov_b32 s85, s81\n\t"
        "s_movk_i32 s86, 30\n"
        ".Lmk_q:\n\t"
        "MK_HEAD s86\n\t"
        "s_mov_b32 m0, s85\n\t"
        "s_add_u32 s85, s85, 10240\n\t"
        "MK_RAW " STR(DEP_A) ", %[q0], %[q1], %[q2], %[q3], %[q4], %[rw], 491520\n\t"
        "MK_FLAG %[rt], 1\n\t"
        "s_add_u32 s84, s84, 1\n\t"
        "s_cmpk_ge_u32 s84, 32\n\t"
        "s_cselect_b32 s86, 33, 30\n\t"
        "s_cmpk_lg_u32 s84, 35\n\t"
        "s_cbranch_scc1 .Lmk_q"
        : MK_ST
        : MK_IN, [q0] "v"(qa[0]), [q1] "v"(qa[1]), [q2] "v"(qa[2]), [q3] "v"(qa[3]), [q4] "v"(qa[4]), [rw] "s"(rw), [sq] "s"(mod10(rot + Lw) * 245760)
        : MK_CL);
    SCHED();
#pragma unroll
    for (int i = 0; i < 5; ++i) {
      const unsigned rho = (ln * 205 + 64 * 205 * i) >> 11;
      const int o = ln * 16 + 1024 * i - 160 * rho + 3072 - (i & 3) * 1024, gg = (rho >> 2) & 3, j = ((rho >> 2) & 4) | (rho & 3);
      ka[i] = (5 * gg + (j >= 5 ? 15 + j : j)) * 1280 + o;
      qa[i] = (40 * gg + 23 + 5 * (j >> 1) + (j & 1)) * 1280 + o;
      PIN(ka[i]); PIN(qa[i]);
    }
    const rsrc_t ro = __builtin_amdgcn_make_buffer_rsrc((void*)(wo + (cc * 160 - 3072)), 0, 819200 - (cc * 160 - 3072), 0x00020000);
    asm volatile(
        "s_mov_b32 s80, %[rb]\n"
        ".Lmk_b:\n\t"
        "s_max_u32 s86, s84, 40\n\t"
        "s_cmp_lt_u32 s84, 39\n\t"
        "s_cselect_b32 s86, 39, s86\n\t"
        "s_cmp_lt_u32 s84, 37\n\t"
        "s_cselect_b32 s86, 36, s86\n\t"
        "MK_HEAD s86\n\t"
        "s_mov_b32 m0, s85\n\t"
        "s_add_u32 s85, s85, 10240\n\t"
        "s_cmp_ge_u32 s80, 16\n\t"
        "s_cbranch_scc1 .Lmk_b1\n\t"
        "s_mul_i32 s82, s80, 51200\n\t"
        "MK_RAW " STR(DEP_B) ", %[k0], %[k1], %[k2], %[k3], %[k4], %[ro], 0\n\t"
        "s_branch .Lmk_b2\n"
        ".Lmk_b1:\n\t"
        "s_mul_i32 s82, s80, 204800\n\t"
        "s_sub_u32 s82, s82, 3276800\n\t"
        "MK_RAW " STR(DEP_B) ", %[q0], %[q1], %[q2], %[q3], %[q4], %[ro], 0\n"
        ".Lmk_b2:\n\t"
        "MK_FLAG %[rt], 1\n\t"
        "s_sub_u32 s79, s85, 133120\n\t"
        "s_min_u32 s85, s85, s79\n\t"
        "s_add_u32 s80, s80, 1\n\t"
        "s_cmp_eq_u32 s80, %[be]\n\t"
        "s_cselect_b32 s79, 10, 0\n\t"
        "s_sub_u32 s80, s80, s79\n\t"
        "s_add_u32 s84, s84, 1\n\t"
        "s_cmpk_lg_u32 s84, 45\n\t"
        "s_cbranch_scc1 .Lmk_b\n\t"
        ".macro MK_SEG rot, base, len, end\n\t"
        "s_mov_b32 s80, \\rot\n\t"
        "s_mov_b32 s87, \\base\n\t"
        "s_mov_b32 s88, \\len\n\t"
        "s_movk_i32 s86, \\end\n\t"
        "s_call_b64 s[90:91], .Lmk_pk\n\t"
        ".endm\n\t"
        "MK_SEG %[rc1], %[sc1], 71680, 52\n\t"
        "s_add_u32 s87, s87, 71680\n\t"
        "MK_SEG %[rc2], s87, 20480, 54\n\t"
        "s_add_u32 s87, s87, 20480\n\t"
        "MK_SEG %[rc2], s87, 20480, 56\n\t"
        "s_add_u32 s87, s87, 20480\n\t"
        "MK_SEG %[rc3], s87, 30720, 59\n\t"
        "MK_SEG %[rd1], %[sd1], 81920, 67\n\t"
        "s_add_u32 s87, s87, 81920\n\t"
        "MK_SEG %[rd1], s87, 81920, 75\n\t"
        "s_add_u32 s87, s87, 81920\n\t"
        "MK_SEG %[rd2], s87, 112640, 86\n\t"
        "s_waitcnt vmcnt(0)\n\t"
        "s_endpgm\n"
        ".Lmk_pk:\n\t"
        "MK_HEAD s84\n\t"
        "s_mov_b32 m0, s85\n\t"
        "s_add_u32 s77, s80, s87\n\t"
        "MK_LD " STR(DEP_P) ", %[l0], %[rs], s77 offen sc1\n\t"
        "s_add_u32 s80, s80, 10240\n\t"
        "MK_LD " STR(DEP_P) ", %[l0], %[rs], s77 offen offset:1024 sc1\n\t"
        "s_sub_u32 s79, s80, s88\n\t"
        "MK_LD " STR(DEP_P) ", %[l0], %[rs], s77 offen offset:2048 sc1\n\t"
        "s_min_u32 s80, s80, s79\n\t"
        "MK_LD " STR(DEP_P) ", %[l0], %[rs], s77 offen offset:3072 sc1\n\t"
        "s_add_u32 m0, m0, 4096\n\t"
        "s_add_u32 s85, s85, 10240\n\t"
        "MK_LD " STR(DEP_P) ", %[l1], %[rs], s77 offen sc1\n\t"
        "MK_FLAG %[rt], 1\n\t"
        "s_sub_u32 s79, s85, 133120\n\t"
        "s_min_u32 s85, s85, s79\n\t"
        "s_add_u32 s84, s84, 1\n\t"
        "s_cmp_lg_u32 s84, s86\n\t"
        "s_cbranch_scc1 .Lmk_pk\n\t"
        "s_setpc_b64 s[90:91]\n"
        ".Lmk_tab:\n\t"
        ".set mk_i, 0\n\t.rept 128\n\t.long mk_i\n\t.set mk_i, mk_i + 1\n\t.endr\n\t"
        ".purgem MK_FLAG\n\t.purgem MK_HEAD\n\t.purgem MK_RAW\n\t.purgem MK_SEG\n\t.purgem MK_LD"
        : MK_ST
        : MK_IN, [q0] "v"(qa[0]), [q1] "v"(qa[1]), [q2] "v"(qa[2]), [q3] "v"(qa[3]), [q4] "v"(qa[4]),
          [k0] "v"(ka[0]), [k1] "v"(ka[1]), [k2] "v"(ka[2]), [k3] "v"(ka[3]), [k4] "v"(ka[4]), [ro] "s"(ro),
          [l0] "v"(ln * 16), [l1] "v"(ln * 16 + 4096), [rs] "s"(rs),
          [rb] "s"(rotB + 10 * Lw), [be] "s"(10 + 10 * Lw),
          [sc1] "s"(OFF_WC + qj * 143360), [rc1] "s"(rotC * 7168 + lw),
          [rc2] "s"(rotC * 2048 + lw), [rc3] "s"(rotC * 3072 + lw),
          [sd1] "s"(OFF_WD + cc * 276480), [rd1] "s"(rotD1 * 10240 + lw),
          [rd2] "s"(rotD2 * 10240 + lw)
        : MK_CL);
  }
  STAMP(0);
  c.ws = (GAS char*)ws;
  c.rs = __builtin_amdgcn_make_buffer_rsrc((void*)ws, 0, WS_BYTES, 0x00020000);
  c.budget = 1u << 22;
  unsigned e0 = __hip_atomic_load((GAS unsigned*)(c.ws + OFF_EPOCH), RLX_AGENT);
  const auto epoch = [&]() AI { SCHED(); PIN(e0); c.ep = (__builtin_amdgcn_readfirstlane(e0) + EPOCH_STEP) | 1u; };
  unsigned pf = 0u, pg = 0u;
  {
    if (w == 7) {
      const v4u z = {0u, 0u, 0u, 0u};
      *LP(v4u, L_DONE + lane * 16) = z;
      *LP(v4u, L_FLG + lane * 16) = z;
      *LP(unsigned, L_CTL + (lane & 31) * 4) = (lane & 31) == C_CAP ? 40 : 0u;
#pragma unroll
      for (int i = 0; i < 4; ++i) *LP(v4u, L_RPK + i * 1024 + lane * 16) = z;
      const int l = min(PADT ? b * 3 + (lane & 3) : 0, 639), lg = l / 160, lk = l - 160 * lg;
      pf = __builtin_amdgcn_raw_buffer_load_b32(c.rs, lane < 8 ? OFF_FLAGS + (b * 6 + min(lane, 5)) * 128 : OFF_WC + (4 * lg + 3) * 143360 + (lk < 80 ? 6 + 7 * (lk >> 3) : 82 + 3 * (lk >> 3)) * 1024 + (lk & 7) * 128, 0, SC1);
      pg = *(const GAS unsigned*)(cpc + min((sx * 16 + n16) * 128u, clen));
      LGKM0();
    }
    __builtin_amdgcn_s_barrier();
    CFENCE();
    STAMP(30);
  }
  if (__builtin_expect(w >= 4, 0)) {
    __builtin_amdgcn_s_setprio(3);
    const int hw = w - 6;
    STL(62);
    const bool fast = (0xa5 >> (pj & 7)) & 1;
    if (hw == 0) {
      const int g0 = gam0(qj), c0 = (80 * cc) >> 6;
      int dep = lane < 12 ? min(4 * (g0 >> 2) + lane, 3 + 4 * ((g0 + 5) >> 2) + ((qj & 3) != 3 && ((g0 + 6) & 3) == 0 ? 4 : 0)) : min(108 + (54 * c0) / 5 + lane - 12, 108 + (54 * c0 + 107) / 5);
      dep = FLR + (dep & 127);
      PIN(dep);
      const int p = pi * 8 + (pj >> 1);
      if ((TCH & 32) && !fast) {
        if (TCH_T) land_wait(c, TCH_T);
        if (TCH & 128) touch(w2, 2211840, 720896 + p * TN * 128, TN, 128);
        else touch(w1, 2211840, p * TN * 128, TN, 128);
      }
      if (fast) {
        const int u = p + 4 * (lane & 32), wi = min((lane & 31) >> 2, 4), e = lane & 3;
        const bool k2 = u < 108;
        int cb, s, A, B = 4, C = 8;
        if (k2) {
          cb = u >> 2; s = 5 * (u & 3) + wi;
          A = 160 * (u & 3) + 32 * wi - (wi > 2 ? 16 : 0);
          if (wi == 2) { B = 8; C = 80; }
        } else {
          const int k = 5 * (u - 108) + wi;
          cb = k / 54; s = k - 54 * cb;
          if (s < 48) A = 16 * (s < 32 ? gam0(((s >> 1) & 8) + (s & 7)) + ((s >> 2) & 2) : gam0(s - 32) + 4);
          else { const int l = 2 * (s - 48), a0 = gam0(l + l / 3), a1 = gam0(l + 1 + (l + 1) / 3); A = 16 * a0 + 96; B = 8; C = 16 * (a1 - a0); }
        }
        int so = (A + B * (e & 1) + C * (e >> 1)) * (k2 ? 3456 : 1280) + cb * 128;
        const int gam = 4 * cb + e, grp = gam / 27, rr = gam - 27 * grp, jj = min(rr / 7, 3), t = rr - 7 * jj, c5 = gam / 5;
        const int sp = mod10(s), cf = (s >= 10) == (jj & 1) ? 7 * sp + t : t < 4 ? 70 + 10 * (t & 2) + 2 * sp + (t & 1) : 106 + 3 * sp + t;
        int db = k2 ? OFF_WC + (4 * grp + jj) * 143360 + cf * 1024
                    : OFF_WD + c5 * 276480 + ((s < 16 ? s * 10 : s < 32 ? (s - 16) * 10 + 5 : s < 43 ? (s - 16) * 10 : (s - 27) * 10 + 5) + gam - 5 * c5) * 1024;
        PIN(so); PIN(db); PIN(B);
        const rsrc_t r0 = __builtin_amdgcn_make_buffer_rsrc((void*)(p < 108 ? w1 : w2), 0, 2211840, 0x00020000);
        const rsrc_t r1 = __builtin_amdgcn_make_buffer_rsrc((void*)w2, 0, 2211840, 0x00020000);
        unsigned t0 = L_SCR + g * 1024 + ((lane >> 2) & 3) * 128 + 8 * e;
        PIN(t0);
        int va[2][5], dd[2][5][4];
#pragma unroll
        for (int h = 0; h < 2; ++h)
#pragma unroll
          for (int i = 0; i < 5; ++i) {
            va[h][i] = ((lane >> 3) + (lane >= 32 && __builtin_amdgcn_readlane(B, 32 * h + 4 * i) == 4 ? 12 : 0)) * (h == 0 && p < 108 ? 3456 : 1280) + (lane & 7) * 16;
            PIN(va[h][i]);
#pragma unroll
            for (int e2 = 0; e2 < 4; ++e2) { dd[h][i][e2] = lane * 16 + __builtin_amdgcn_readlane(db, 32 * h + 4 * i + e2); PIN(dd[h][i][e2]); }
          }
        v4u v[2][5][4];
        const auto fetch = [&](int h) AI {
#pragma unroll
          for (int i = 0; i < 20; ++i)
            asm volatile("s_mov_b32 m0, %0\n\tv_readlane_b32 s70, %3, %4\n\ts_nop 4\n\tbuffer_load_dwordx4 %1, %2, s70 offen lds"
                         :: "i"(L_SCR + i * 1024), "v"(va[h][i >> 2]), "s"(h ? r1 : r0), "v"(so), "i"(32 * h + i) : "memory", "s70");
        };
        const auto turn = [&](int h) AI {
#pragma unroll
          for (int i = 0; i < 5; ++i)
#pragma unroll
            for (int e2 = 0; e2 < 4; ++e2) v[h][i][e2] = tr2(t0, i * 4096 + 32 * e2, 512);
        };
        const auto put = [&](int h) AI {
#pragma unroll
          for (int i = 0; i < 5; ++i)
#pragma unroll
            for (int e2 = 0; e2 < 4; ++e2) st128(c, v[h][i][e2], dd[h][i][e2], 0);
        };
        epoch();
        asm volatile("" :: "s"(c.ep));
        ctl_addc<C_ARR1>();
        land_wait(c, RPK_F);
        STAMP(21);
        fetch(0);
        DRAIN();
        turn(0);
        LGKM0();
        if (p < 88) {
          fetch(1);
          if (RPK_P) land_wait(c, RPK_P);
          put(0);
          VMWAIT(20);
          turn(1);
          put(1);
        } else {
          if (RPK_P) land_wait(c, RPK_P);
          put(0);
        }
        ctl_addc<C_XB0>(); ctl_addc<C_XB1>();
        STAMP(32);
        if (p < 108) {
          done_wait(c, CS_T);
          float cs[4];
#pragma unroll
          for (int e2 = 0; e2 < 4; ++e2) {
            cs[e2] = 0.f;
#pragma unroll
            for (int i = 0; i < 5; ++i) cs[e2] = dot2(v[0][i][e2].x, ONE2, dot2(v[0][i][e2].y, ONE2, dot2(v[0][i][e2].z, ONE2, dot2(v[0][i][e2].w, ONE2, cs[e2]))));
          }
          v2u q0 = swap32(asu(cs[0]), asu(cs[2])), q1 = swap32(asu(cs[1]), asu(cs[3]));
          q0 = swap16(asu(asf(q0.x) + asf(q0.y)), asu(asf(q1.x) + asf(q1.y)));
          st32(c, asu(asf(q0.x) + asf(q0.y)), lane * 4, OFF_CS + ((p & 3) * 1792 + 64 * (p >> 2)) * 4);
        }
        DRAIN();
        flag_set(c, FLR + p);
        STAMP(22);
      } else {
        epoch();
        asm volatile("" :: "s"(c.ep));
        ctl_addc<C_ARR1>();
        ctl_addc<C_XB0>(); ctl_addc<C_XB1>();
      }
      STL(22);
      if (FLR_T == 0) ctl_wait<1>(c, C_PUB, 2); else ctl_wait<1>(c, C_ARR2, 4);
      STAMP(20);
      flag_wait(c, dep, 36);
      if (lane == 0) *LP(volatile unsigned, L_CTL + 4 * C_CAP) = 127;
      STAMP(34);
      done_wait(c, CH_B);
      {
        f32x4 cs[4];
#pragma unroll
        for (int rb = 0; rb < 4; ++rb) cs[rb] = __builtin_bit_cast(f32x4, ld128(c, min(lane, 27) * 16, OFF_CS + (rb * 1792 + 16 * g0) * 4));
        if (lane < 28) *LP(f32x4, L_CSUM + lane * 16) = add4(add4(cs[0], cs[1]), add4(cs[2], cs[3]));
      }
      ctl_addc<C_CS>();
      DUMPL(20, 21, 22, 32, 34);
      ENDPGM();
    }
    STL(62);
    unsigned ra[4], gd[4];
#pragma unroll
    for (int i = 0; i < 4; ++i) {
      {
        const int gr = min(lane + 32 * (i & 2), 119), hd = i & 1, z = gr >= 100, d4 = z ? gr % 5 : gr / 20;
        const int d4g = 5 * hd + d4, e8 = z ? 0 : 20 * hd + gr - 20 * d4 + 8;
        ra[i] = KVSLOT * 10240 + ((d4g >> 2) * 3 + (e8 >> 4)) * 1024 + ((d4g & 3) * 16 + (e8 & 15)) * 16;
      }
      {
        const int gr = 32 * i + 16 * (g >> 1) + n16, hd = g & 1, z = gr >= 100, d4 = z ? gr % 5 : gr / 20;
        const int e = gr - 20 * d4, gi = e / 5, r5 = e - 5 * gi;
        const int R = z || r5 == 4, row = z ? 4 * ((gr - 100) / 5) + 1 : r5 == 4 ? 4 * gi : 4 * gi + r5;
        const int dd = d4 + hd;
        gd[i] = gr < 120 ? L_RPK + (hd * 2 + R) * 1024 + ((dd & 3) * 16 + row) * 16 + (dd >> 2) * 8 : L_CK;
      }
      PIN(ra[i]); PIN(gd[i]);
    }
    int gs = g * KVP_STR + n16 * 16, gs7 = g * KVP_STR + (n16 & 7) * 16, kvo = OFF_KVP + (pj * 16 + pi) * KVP_STR + lane * 16;
    v4u sel[2];
#pragma unroll
    for (int i = 0; i < 4; ++i) {
      const unsigned o = ((n16 >> 1) & 3) == i ? (n16 & 1 ? 0x3f800000u : 0x3f80u) : 0u;
      sel[0][i] = n16 < 8 ? o : 0u; sel[1][i] = n16 < 8 ? 0u : o;
    }
    PIN(gs); PIN(gs7); PIN(kvo); PIN(sel[0]); PIN(sel[1]);
    if (TCH & 16) touch(w1, 2211840, b * 8704, 68, 128);
    if (TCH & 8) touch(w2, 2211840, b * 8704, 68, 128);
    if (TCH & 64) touch(wqkv, 2457600, (b / 10) * 99840 + (b % 10) * 128, 26, 3840);
    if (TCH & 2) touch(wo, 819200, b * 3200, 25, 128);
    if (TCH & 4) touch(w2, 2211840, b * 2816, 22, 128);
    STAMP(38);
    epoch();
    TOUCH(pf); TOUCH(pg);
    ctl_wait<1>(c, C_ARR1, 5);
    STAMP(26);
    {
      f32x4 p[4][4];
#pragma unroll
      for (int i = 0; i < 4; ++i)
#pragma unroll
        for (int j = 0; j < 4; ++j) p[i][j] = *LP(f32x4, ra[i] + j * 10240);
      if (!CAPL) { CFENCE(); if (lane == 0) *LP(volatile unsigned, L_CTL + 4 * C_CAP) = CH_C1; }
#pragma unroll
      for (int h = 0; h < 2; ++h) {
        const f32x4 u = add4(add4(p[2 * h][0], p[2 * h][1]), add4(p[2 * h][2], p[2 * h][3])), v = add4(add4(p[2 * h + 1][0], p[2 * h + 1][1]), add4(p[2 * h + 1][2], p[2 * h + 1][3]));
        if (h == 0 || lane < 56) st128(c, v4u{pk2(u[0], u[1]), pk2(u[2], u[3]), pk2(v[0], v[1]), pk2(v[2], v[3])}, kvo, h * 1024);
      }
    }
    if (CAPL == 1) { CFENCE(); if (lane == 0) *LP(volatile unsigned, L_CTL + 4 * C_CAP) = CH_C1; }
    STAMP(27);
    DRAIN();
    flag_set(c, FL1 + pj * 16 + pi);
    if (CAPL == 2 && lane == 0) *LP(volatile unsigned, L_CTL + 4 * C_CAP) = CH_C1;
    ctl_addc<C_PUB>();
    STAMP(23);
    flag_wait(c, FL1 + pq_ * 16 + n16, 16);
    STAMP(24);
    {
      v4u p[4][8];
#pragma unroll
      for (int t = 0; t < 4; ++t)
#pragma unroll
        for (int i = 0; i < 8; ++i) p[t][i] = ld128(c, (i == 7 ? gs7 : gs) + i * 256, OFF_KVP + (pq_ * 16 + 4 * t) * KVP_STR);
      ctl_addc<C_PUB>();
      SCHED(); STAMP(35); SCHED();
      f32x4 u[4];
#pragma unroll
      for (int i = 0; i < 4; ++i) u[i] = f32x4{0.f, 0.f, 0.f, 0.f};
#pragma unroll
      for (int t = 0; t < 4; ++t)
#pragma unroll
        for (int i = 0; i < 8; ++i) u[i >> 1] = mfma(sel[i & 1], p[t][i], u[i >> 1]);
#pragma unroll
      for (int i = 0; i < 4; ++i) *LP(v2u, gd[i]) = v2u{pk2(u[i][0], u[i][1]), pk2(u[i][2], u[i][3])};
    }
    ctl_addc<C_GO1>();
    LGKM0();
    STAMP(25);
    STL(25);
    ctl_wait<2>(c, C_XB0, 3);
    if (b == 0 && lane == 0) __hip_atomic_store((GAS unsigned*)(c.ws + OFF_EPOCH), c.ep, RLX_AGENT);
    ctl_wait<1>(c, C_ARR4A, 4);
    flag_set(c, FL4A + qi * 16 + qj);
    if (Q4 == 1) ctl_wait(c, C_ARR4B, 4);
    if (Q4 >= 2) ctl_wait(c, C_ISS4, 4);
    STAMP(42);
    {
      const GAS unsigned* fp = (const GAS unsigned*)(c.ws + OFF_FLAGS + ((lane & 16 ? FL4B : FL4A) + (hp >> 1) * 16 + n16) * 128);
      unsigned n = 1u << 17;
      u64 miss;
      do { unsigned v = c.ep; if (lane < 32) v = __hip_atomic_load(fp, RLX_AGENT); miss = __builtin_amdgcn_ballot_w64(v != c.ep); } while ((miss & 0xffff) && --n != 0);
      CFENCE();
      ctl_addc<C_GO4A>();
      STAMP(28);
      if (Q4 == 3) ctl_wait(c, C_ARR4B, 4);
      if (miss) flag_wait(c, FL4B + (hp >> 1) * 16 + n16, 16);
      ctl_addc<C_GO4B>();
      STAMP(29);
    }
    DUMPL(23, 24, 25, 26, 27, 28, 29, 35, 38, 42);
    ENDPGM();
  }

  int xvoff;
  STL(30);
  const unsigned dn = L_LAND + 128 + lane * 4;
  c.dm = L_DONE + w * 256;
  const int kb = w & 1;
  const int mtB = 4 * hp + w;
  v2u xr[5];

  {
    v4u xa[2][20];
    const unsigned hv = n16 < 8 ? ONE2 : 0u;
    v4u ones = {hv, hv, hv, hv}, oneh = {hv ^ ONE2, hv ^ ONE2, hv ^ ONE2, hv ^ ONE2};
    PIN(ones); PIN(oneh);
    unsigned rawlane = (4 * g + ((lane >> 2) & 3)) * 160 + 8 * (lane & 3);
    const int oc = 32 * w, o4 = 128 + 5120 * (w & 1);
    {
      f32x4 acc[2][5];
      {
        unsigned br0 = w * 4096 + (n16 >> 3) * 1024 + (n16 & 7) * 128 + ((g ^ (n16 >> 1)) * 16), br1 = br0 ^ 64, wb = rawlane + 16384;
        PIN(br0); PIN(br1); PIN(wb);
        f32x4 sm, sq[2], ca;
        v4u wf[4], cf;
        v2u clo;
        unsigned la, cad;
        float q2;
        const auto addr = [&](int o) AI { unsigned r; asm volatile("v_add_u32 %0, %1, %2" : "=v"(r) : "s"(o), "v"(wb)); return r; };
        const auto stat = [&](auto qc, auto bc) AI {
          constexpr int q = decltype(qc)::v, m = q & 1, e = 2 * decltype(bc)::v + (q >> 2);
          if constexpr ((q & 2) == 0) mm<1, e == 0>(sq[m], xa[m][e], xa[m][e]);
          else mm<1, e == 0 && m == 0>(sm, m ? oneh : ones, xa[m][e]);
          SCHED();
        };
        epoch();
        ring_wait<0>(c);
        STAMP(31);
        SCHED();
        static_for<-4, 100>([&](auto ic) AI {
          constexpr int f = decltype(ic)::v, fn = f + 3;
          constexpr int s = f < 0 ? 0 : f / 5, t = f < 0 ? 0 : f % 5;
          constexpr int cb = f < 0 ? -1 : f / 10, i = (f + 10) % 10, nb = cb + 1;
          constexpr int off = fn < 0 ? 0 : bin(fn / 10) + ((fn % 10) / 5) * 5120 + 32 * (fn % 5);
          constexpr bool R = fn >= 0 && fn < 100, more = cb < 9, go = f >= 0, rel = true;
          v2u lo;
          if constexpr (go) { mm<0, s == 0>(acc[0][t], wf[f % 4], xa[0][s]); SCHED(); }
          if constexpr (R) { lo = tr1(wb, off); SCHED(); }
          if constexpr (i == 4) { cad = addr(o4); SCHED(); }
          if constexpr (i == 8 && more) { cad = addr(oc); SCHED(); }
          if constexpr (i == 1 && more) bump<breg(nb) - breg(cb)>(br0);
          if constexpr (i == 2 && more) bump<breg(nb) - breg(cb)>(br1);
          if constexpr (i == 9 && go && rel) { release<3 * cb + 3>(c); SCHED(); }
          if constexpr (i == 6 && go && more) { ring_cmp<3 * nb>(la); SCHED(); }
          if constexpr (go) { mm<0, s == 0>(acc[1][t], wf[f % 4], xa[1][s]); SCHED(); }
          if constexpr (R) { const v2u hi = tr1(wb, off + 2560); wf[fn % 4] = v4u{lo.x, lo.y, hi.x, hi.y}; SCHED(); }
          if constexpr (i == 6 && go && more) { ring_br<3 * nb>(c); SCHED(); }
          if constexpr (go) {
            if constexpr (i == 1 || i == 4) { mm<1, f == 1>(ca, ones, cf); SCHED(); }
            else if constexpr (i == 7) { mm<1, false>(ca, oneh, cf); SCHED(); }
            else if constexpr (more) stat(IC<i - (i + 1) / 3>{}, IC<cb>{});
            else if constexpr (i < 4) stat(IC<i == 0 ? 0 : i == 2 ? 4 : 2>{}, IC<cb>{});
          }
          if constexpr (i == 0) xa[1][2 * cb + 1] = *LP(v4u, br1 + bin(cb) + 2048);
          if constexpr (i == 1) clo = tr1(cad, bin(cb) + 5120);
          if constexpr (i == 2) { const v2u hi = tr1(cad, bin(cb) + 7680); cf = v4u{clo.x, clo.y, hi.x, hi.y}; }
          if constexpr (i == 3 && more) la = *LP(unsigned, dn);
          if constexpr (i == 4) clo = tr1(cad, bin(cb));
          if constexpr (i == 5) { const v2u hi = tr1(cad, bin(cb) + 2560); cf = v4u{clo.x, clo.y, hi.x, hi.y}; }
          if constexpr (i == 6 && more) { xa[0][2 * nb] = *LP(v4u, br0 + bin(nb)); SCHED(); if constexpr (go) bump<breg(nb) - breg(cb)>(wb); }
          if constexpr (i == 7 && more) xa[1][2 * nb] = *LP(v4u, br0 + bin(nb) + 2048);
          if constexpr (i == 8 && more) clo = tr1(cad, bin(nb));
          if constexpr (i == 9 && more) { const v2u hi = tr1(cad, bin(nb) + 2560); cf = v4u{clo.x, clo.y, hi.x, hi.y}; }
          SCHED();
          if constexpr (i == 9 && go && more) stat(IC<7>{}, IC<cb>{});
          if constexpr (i == 9 && more) { xa[0][2 * nb + 1] = *LP(v4u, br1 + bin(nb)); SCHED(); }
          if constexpr (!more && i < 5) stat(IC<i == 0 ? 1 : i == 1 ? 3 : i == 2 ? 5 : i + 3>{}, IC<cb>{});
          if constexpr (f == 94) {
            const unsigned bw = 4 * BLK + w * 4096 + opaque(lane) * 16;
            *LP(f32x4, bw) = sq[0];
            *LP(f32x4, bw + 1024) = sq[1];
            CFENCE();
            SCHED();
          }
          if constexpr (f == 95) {
            const int l2 = opaque(lane), n2 = l2 & 15;
            q2 = *LP(float, 4 * BLK + w * 4096 + (l2 >> 5) * 1024 + ((n2 >> 2) * 16 + n2) * 16 + (n2 & 3) * 4);
            SCHED();
          }
          if constexpr (f == 97) {
            const int l2 = opaque(lane);
            PIN(sm);
            const float mean = sm[0] * (1.f / 640.f);
            const float a = __builtin_amdgcn_rsqf(fmaxf(q2 * (1.f / 640.f) - mean * mean, 0.f) + 1e-5f);
            *LP(float, L_STA + w * 256 + l2 * 4) = (l2 & 16) ? -mean * a : a;
            CFENCE();
            SCHED();
          }
          if constexpr (f == 98) {
            const unsigned a = L_CK + w * 256 + opaque(lane) * 4;
            asm volatile("s_nop 1" : "+v"(ca));
            *LP(float, a) = ca[0];
            ctl_addc<C_CSK>();
            SCHED();
          }
          if constexpr (f + 1 >= 0 && f + 1 < 100) {
            if constexpr (i == 5 && more) TOUCH(la);
            else if constexpr (i == 9) TOUCH(xa[1][2 * nb]);
            else TOUCH(wf[(f + 1) % 4]);
            SCHED();
          }
        });
        rawlane = wb - (16384 + breg(9));
        PIN(rawlane);
      }
      lane = opaque(lane); c.lane = lane; n16 = lane & 15; g = lane >> 4; xvoff = lane * 16;
      STAMP(4);
      f32x4 a4[2], b4[2];
#pragma unroll
      for (int m = 0; m < 2; ++m) { a4[m] = *LP(f32x4, L_STA + w * 256 + m * 128 + g * 16); b4[m] = *LP(f32x4, L_STA + w * 256 + m * 128 + 64 + g * 16); }
      SCHED();
      asm volatile("s_nop 3");
      SCHED();
      const float ks = n16 < 8 ? LOG2E : 1.f, kd = n16 < 8 ? PHID : 0.f;
#pragma unroll
      for (int m = 0; m < 2; ++m) {
        const f32x4 al = a4[m] * LOG2E;
        acc[m][0] = acc[m][0] * al + PHID; acc[m][1] = acc[m][1] * al + PHID;
        acc[m][2] = acc[m][2] * (a4[m] * ks) + kd;
        acc[m][3] = acc[m][3] * a4[m]; acc[m][4] = acc[m][4] * a4[m];
#pragma unroll
        for (int t = 0; t < 5; ++t) PIN(acc[m][t]);
      }
      ctl_wait(c, C_CSK, 4);
      STAMP(48);
      float cs[5];
      {
        const unsigned a = L_CK + n16 * 4;
#pragma unroll
        for (int t = 0; t < 4; ++t) cs[t] = *LP(float, a + t * 256);
        cs[4] = *LP(float, a + 128) + *LP(float, a + 384);
        cs[0] *= LOG2E; cs[1] *= LOG2E; cs[2] *= ks;
      }
      v4u pk[3], vv[3];
#pragma unroll
      for (int t = 4; t >= 0; --t) {
        const f32x4 t0 = b4[0] * cs[t] + acc[0][t], t1 = b4[1] * cs[t] + acc[1][t];
        if (t >= 2) vv[t - 2] = v4u{pk2(t0[0], t0[1]), pk2(t0[2], t0[3]), pk2(t1[0], t1[1]), pk2(t1[2], t1[3])};
        if (t <= 2) { const v2u p0 = phi4(t0), p1 = phi4(t1); pk[t] = v4u{p0.x, p0.y, p1.x, p1.y}; }
      }
      if (n16 < 8) vv[0] = ones;
      const unsigned kp = (KVSLOT + w) * 10240 + opaque(lane) * 16;
      const f32x4 z4 = {0.f, 0.f, 0.f, 0.f};
#pragma unroll
      for (int ta = 2; ta >= 0; --ta)
#pragma unroll
        for (int tb = 0; tb < 3; ++tb)
          if (ta != 0 || tb != 2) *LP(f32x4, kp + (ta * 3 + tb) * 1024) = mfma(pk[ta], vv[tb], z4);
      ctl_addc<C_ARR1>();
    }
    STAMP(3);
    v2u pq[2][3];
    {
      static_assert(region(CH_Q) == 0 && region(CH_B - 1) == 0, "q chunks");
      f32x4 aq[2][3], ca;
      v4u wf[4], cf[2];
      v2u clo;
      const unsigned wb = rawlane;
      unsigned c0 = rawlane + oc, c4 = rawlane + o4;
      PIN(c0); PIN(c4);
      float sa[2], sb[2];
      f32x4 cs[3];
      ring_wait<CH_B - 1>(c);
      STAMP(43);
      SCHED();
      constexpr auto qoff = [](int f) { return ((f % 20) / 2) * 5120 + (f & 1) * 80 + 32 * (f / 20); };
      constexpr auto coff = [](int j) { return (j / 3) * 10240 + (j % 3 == 1 ? 5120 : 0); };
      static_for<-3, 60>([&](auto ic) AI {
        constexpr int f = decltype(ic)::v, fn = f + 3, s = f < 0 ? 0 : f % 20, t = f < 0 ? 0 : f / 20, jn = f + 2;
        v2u lo;
        if constexpr (f >= 0) { mm<1, s == 0>(aq[0][t], wf[f % 4], xa[0][s]); SCHED(); }
        if constexpr (fn < 60) { lo = tr1(wb, qoff(fn)); SCHED(); }
        if constexpr (f >= 0) { mm<1, s == 0>(aq[1][t], wf[f % 4], xa[1][s]); SCHED(); }
        if constexpr (fn < 60) { const v2u hi = tr1(wb, qoff(fn) + 2560); wf[fn % 4] = v4u{lo.x, lo.y, hi.x, hi.y}; SCHED(); }
        if constexpr (f >= 0 && f < 15) { mm<1, f == 0>(ca, f % 3 == 2 ? oneh : ones, cf[f & 1]); SCHED(); }
        if constexpr (jn >= 0 && jn < 15) { cf[jn & 1] = tr2(jn % 3 == 2 ? c4 : c0, coff(jn), 2560); SCHED(); }
        if constexpr (f == 16) {
          const int l2 = opaque(lane);
          PIN(ca);
          if ((l2 & 16) == 0) *LP(float, L_CQ + (l2 & 15) * 4 + w * 64 + (l2 >> 5) * 256) = ca[0];
          ctl_addc<C_CSQ>();
#pragma unroll
          for (int m = 0; m < 2; ++m) { sa[m] = *LP(float, L_STA + w * 256 + m * 128 + n16 * 4); sb[m] = *LP(float, L_STA + w * 256 + m * 128 + 64 + n16 * 4); }
          SCHED();
        }
      });
      STAMP(46);
      release<32>(c);
      {
        const char* xb0 = x + (size_t)(16 * mtB * 1280 + cc * 160);
        const unsigned rowoff = n16 * 1280 + g * 8;
#pragma unroll
        for (int t = 0; t < 5; ++t) xr[t] = *(const GAS v2u*)(xb0 + rowoff + 32 * t);
        SCHED();
      }
      ctl_wait(c, C_CSQ, 4);
      STAMP(47);
      {
        const unsigned a = L_CQ + g * 16;
        cs[0] = *LP(f32x4, a) + *LP(f32x4, a + 160);
        const f32x4 p1 = *LP(f32x4, a + 288);
        cs[1] = *LP(f32x4, a + 64) + *LP(f32x4, a + 224) + (g < 2 ? f32x4{0.f, 0.f, 0.f, 0.f} : p1);
        cs[2] = *LP(f32x4, a + 128) + p1 + *LP(f32x4, a + 352);
      }
#pragma unroll
      for (int m = 0; m < 2; ++m) { sa[m] *= LOG2E; sb[m] *= LOG2E; }
#pragma unroll
      for (int t = 0; t < 3; ++t) {
#pragma unroll
        for (int m = 0; m < 2; ++m) {
          pq[m][t] = phi4(aq[m][t] * sa[m] + (cs[t] * sb[m] + PHID));
          if (t == 2 && g >= 2) pq[m][t] = v2u{0u, 0u};
        }
      }
    }
    v4u qb[2][2];
#pragma unroll
    for (int m = 0; m < 2; ++m)
#pragma unroll
      for (int hh = 0; hh < 2; ++hh) { qb[m][hh] = v4u{pq[m][hh].x, pq[m][hh].y, pq[m][hh + 1].x, pq[m][hh + 1].y}; PIN(qb[m][hh]); }
    const int mt0 = 8 * pi + 2 * w;
    int oa = OFF_O + (mt0 * 20 + pq_) * 1024 + xvoff;
    int ob = OFF_O + (mt0 * 20 + 16 + (pq_ >> 2)) * 1024 + (pq_ & 3) * 256 + g * 20480 + n16 * 16;
    unsigned fa = L_RPK + xvoff;
    PIN(oa); PIN(ob); PIN(fa);
#pragma unroll
    for (int t = 0; t < 5; ++t) PIN(xr[t]);
    STAMP(5);
    ctl_wait(c, C_GO1, 1);
    if (!QUIET) release<CH_B>(c);
    STAMP(7);
    v4u ka_[4];
#pragma unroll
    for (int i = 0; i < 4; ++i) ka_[i] = *LP(v4u, fa + (i ^ 1) * 1024);
    f32x4 on[2][2][2];
    const f32x4 z4 = {0.f, 0.f, 0.f, 0.f};
#pragma unroll
    for (int i = 0; i < 4; ++i)
#pragma unroll
      for (int m = 0; m < 2; ++m) on[m][i >> 1][(i & 1) ^ 1] = mfma(ka_[i], qb[m][i >> 1], z4);
    unsigned tl[2];
#pragma unroll
    for (int m = 0; m < 2; ++m) {
      const float r0 = __builtin_amdgcn_rcpf(on[m][0][1][1] + 1e-6f), r1 = __builtin_amdgcn_rcpf(on[m][1][1][1] + 1e-6f);
      const v4u o = {pk2(on[m][0][0][0] * r0, on[m][0][0][1] * r0), pk2(on[m][0][0][2] * r0, on[m][0][0][3] * r0),
                     pk2(on[m][0][1][0] * r0, on[m][1][0][0] * r1), pk2(on[m][1][0][1] * r1, on[m][1][0][2] * r1)};
      tl[m] = pk2(on[m][1][0][3] * r1, on[m][1][1][0] * r1);
      st128(c, o, oa, m * 20480);
    }
    const v2u t0 = swap16(tl[0], tl[1]);
    const v2u u0 = swap32(t0.x, t0.x), u1 = swap32(t0.y, t0.y);
    if (lane < 32) st128(c, v4u{u0.x, u1.x, u0.y, u1.y}, ob, 0);
  }
  STAMP(8);
  unsigned hq[10];

  {
    f32x4 acc[2][5];
    {
#pragma unroll
      for (int t = 0; t < 5; ++t) { acc[0][t] = unpk4(xr[t].x, xr[t].y); PIN(acc[0][t]); }
    }
    const int sb = 5 * (cc >> 1) + 3 * (cc & 1);
    int ha = OFF_H + (mtB * 20 + sb) * 1024 + xvoff;
    int hb = OFF_H + (mtB * 20 + 5 * (cc >> 1) + 2) * 1024 + (cc & 1) * 512 + g * 128 + n16 * 16;
    unsigned wraw = (4 * g + ((lane >> 2) & 3)) * 160 + 8 * (lane & 3) + kb * 5120;
    PIN(ha); PIN(hb); PIN(wraw);
    DRAIN();
    const bool last = arrive<C_ARR2>(c, FL2 + pi * 16 + pq_);
    STAMP(6);
    if (PW & 1) ctl_wait(c, C_ARR2, 4);
    if (!(PW & 4)) flag_wait(c, FL2 + (hp >> 1) * 16 + n16, kb ? 16 : 10);
    else if (last) { flag_wait(c, FL2 + (hp >> 1) * 16 + n16, 16); ctl_addc<C_ARR2>(); }
    else ctl_wait(c, C_ARR2, 5);
    STAMP(9);
    v4u xb[2][10];
    const int xs = OFF_O + mtB * 20480 + kb * 10240, xm = kb ? -20480 : 20480;
    xload<10, 0, 0, PRE_B, 10>(c, xb, xvoff, xs, xm, rotB);
    if (QUIET) release<CH_B>(c);
    gemm_pass<5, 10, CH_B, 5, 10, 2, true>(c, dn, wraw, acc, xb, nullptr, [&](auto ic) AI {
      constexpr int f = decltype(ic)::v;
      if constexpr (f >= 0 && f % 5 == 1 && f / 5 + PRE_B < 10) xload<10, 0, f / 5 + PRE_B, 1, 10>(c, xb, xvoff, xs, xm, rotB);
    });
    STAMP(1);
    ctl_wait(c, C_XB0 + (w >> 1), 1);
    pair_reduce(c, w, C_XB0, 1, acc, hq);
    STAMP(49);
    st128(c, v4u{hq[0], hq[1], hq[2], hq[3]}, ha, 0);
    st128(c, v4u{hq[4], hq[5], hq[6], hq[7]}, ha, 1024);
    const v2u ta = swap16(hq[8], hq[8]), tb = swap16(hq[9], hq[9]);
    if ((g & 1) == 0) st128(c, v4u{ta.x, tb.x, ta.y, tb.y}, hb, 0);
    float s1 = 0.f, s2 = 0.f;
#pragma unroll
    for (int i = 0; i < 10; ++i) { s1 = dot2(hq[i], ONE2, s1); s2 = dot2(hq[i], hq[i], s2); }
    s1 = sum_g(s1); s2 = sum_g(s2);
    if (g == 0) st64(c, v2u{asu(s1), asu(s2)}, n16 * 8, OFF_HS + mtB * 1024 + cc * 128);
  }
  if (!QUIET3) release<CH_C1>(c);
  STAMP(10);
  if (QUIET3) DRAIN(); else VMWAIT(1);
  const bool last3 = arrive<C_ARR3>(c, FL3 + hp * 8 + cc);
  if (QUIET3 == 2) release<Q3N>(c);
  ctl_wait(c, C_CS, 1);
  STAMP(33);
  if (PW & 2) ctl_wait(c, C_ARR3, 4);

  {
    const int mt0 = 8 * qi + 2 * w;
    f32x4 acc[2][7];
#pragma unroll
    for (int t = 0; t < 7; ++t) { acc[0][t] = *LP(f32x4, L_CSUM + t * 64 + g * 16); PIN(acc[0][t]); }
    const int xe = OFF_H + mt0 * 20480 + (qj & 1) * 10240, xl = OFF_H + mt0 * 20480 + 10240 - (qj & 1) * 10240;
    const int fb = OFF_F + mt0 * 55296;
    int fa[2] = {fb + (qj + (qj & 8)) * 1024 + xvoff, fb + 55296 + (qj + (qj & 8)) * 1024 + xvoff};
    PIN(fa[0]); PIN(fa[1]);
    if (last3) { flag_wait(c, FL3 + qi * 16 + n16, 16); ctl_addc<C_ARR3>(); }
    else ctl_wait(c, C_ARR3, 5);
    if (QUIET3 == 1 || (QUIET3 == 2 && Q3N < 45)) release<CH_C1>(c);
    STAMP(11);
    v2u st[2][2];
#pragma unroll
    for (int m = 0; m < 2; ++m)
#pragma unroll
      for (int i = 0; i < 2; ++i) st[m][i] = ld64(c, lane * 8, OFF_HS + (mt0 + m) * 1024 + i * 512);
    v4u xh[2][16];
    xload<16, 0, 0, PRE_C, 10>(c, xh, xvoff, xe, 20480, rotC);
    float mneg[2], s2[2];
#pragma unroll
    for (int m = 0; m < 2; ++m) {
      mneg[m] = sum_g(asf(st[m][0].x) + asf(st[m][1].x)) * (-1.f / 640.f);
      s2[m] = asf(st[m][0].y) + asf(st[m][1].y);
    }
#pragma unroll
    for (int t = 0; t < 7; ++t) { acc[1][t] = acc[0][t] * mneg[1]; acc[0][t] = acc[0][t] * mneg[0]; PIN(acc[0][t]); PIN(acc[1][t]); }
    float sd[2], c2[2], z[32];
    v4u fo;
    const auto gap = [&](auto fc, auto hc) AI {
      constexpr int G = 2 * (decltype(fc)::v - 90) + decltype(hc)::v, k = G / 3;
      if constexpr (G >= 0) {
        const auto a = [&](int u) AI { return acc[(u >> 3) & 1][2 * (u >> 4) + ((u >> 2) & 1)][u & 3]; };
        if constexpr (G % 3 == 0) {
          if constexpr (k >= 1 && k <= 32) asm volatile("v_fma_f32 %0, %0, %1, %1" : "+v"(z[k - 1]) : "v"(sd[((k - 1) >> 3) & 1]));
          if constexpr (k == 0) asm volatile("v_mul_f32 %0, %1, %2" : "=v"(z[0]) : "v"(a(0)), "v"(c2[0]));
          if constexpr (k < 31) asm volatile("v_mul_f32 %0, %1, %2" : "=v"(z[k + 1]) : "v"(a(k + 1)), "v"(c2[((k + 1) >> 3) & 1]));
          if constexpr (k >= 3 && k < 35) asm volatile("v_mul_f32 %0, %0, %1" : "+v"(z[k - 3]) : "v"(a(k - 3)));
        } else if constexpr (G % 3 == 1) {
          if constexpr (k < 32) asm volatile("v_exp_f32 %0, %0" : "+v"(z[k]));
          if constexpr (k >= 5 && (k & 1)) { unsigned o; asm volatile("v_cvt_pk_bf16_f32 %0, %1, %2" : "=v"(o) : "v"(z[k - 5]), "v"(z[k - 4])); fo[((k - 5) >> 1) & 3] = o; }
        } else {
          if constexpr (k >= 1 && k <= 32) asm volatile("v_rcp_f32 %0, %0" : "+v"(z[k - 1]));
          if constexpr (k >= 11 && k % 8 == 3) st128(c, fo, fa[((k - 4) >> 3) & 1], ((k - 4) >> 4) * 8192);
        }
      }
    };
    gemm_pass<7, 20, CH_C1, 10, 16, 0, false>(c, dn, xvoff, acc, xh, nullptr, [&](auto ic) AI {
      constexpr int f = decltype(ic)::v, p = f / XS + PRE_C;
      if constexpr (f >= 0 && f % XS == 1 && p < 10) xload<16, 0, p, 1, 10>(c, xh, xvoff, xe, 20480, rotC);
      if constexpr (f >= 0 && f % XS == 1 && p >= 10 && p < 20) xload<16, 10, p - 10, 1, 10>(c, xh, xvoff, xl, 20480, rotC);
      if constexpr (f == 80 || f == 84) {
        constexpr int m = f == 84;
        const float var = fmaxf(sum_g(s2[m]) * (1.f / 640.f) - mneg[m] * mneg[m], 0.f) + 1e-5f;
        const float rs = __builtin_amdgcn_rsqf(var);
        sd[m] = var * rs;
        c2[m] = -1.4426950408889634f * rs;
        PIN(sd[m]); PIN(c2[m]);
      }
      if constexpr (f == 69) STAMP(12);
      if constexpr (f == 109) STAMP(2);
      gap(ic, IC<1>{});
      static_assert(E4F > 119 && E4F < 140 && released(CH_C1 + 13) && released(CH_C1 + 14) && 13 * XS + 1 + (6 - PRE_C) * XS < 119, "count");
      if constexpr (f == E4F) {
        SCHED();
        if constexpr (f < 129) VMWAIT(0); else if constexpr (f < 131) VMWAIT(1); else if constexpr (f < 139) VMWAIT(2); else VMWAIT(3);
        ctl_addc<C_ARR4A>();
      }
    }, [&](auto ic) AI { gap(ic, IC<0>{}); });
    static_for<0, 8>([&](auto hc) AI { gap(IC<140>{}, hc); });
    SCHED();
    STAMP(13);
    const auto silu2 = [&](int m, const f32x4& a) AI {
      float y[4];
#pragma unroll
      for (int i = 0; i < 4; ++i) {
        const float e = __builtin_amdgcn_exp2f(a[i] * c2[m]);
        y[i] = a[i] * __builtin_amdgcn_rcpf(fmaf(e, sd[m], sd[m]));
      }
      return v2u{pk2(y[0], y[1]), pk2(y[2], y[3])};
    };
#pragma unroll
    for (int m = 0; m < 2; ++m) {
      const v2u a = silu2(m, acc[m][4]), d = silu2(m, acc[m][5]);
      st128(c, v4u{a.x, a.y, d.x, d.y}, xvoff, fb + m * 55296 + (32 + qj) * 1024);
    }
    if ((qj & 3) != 3) {
      const v2u a = silu2(0, acc[0][6]), d = silu2(1, acc[1][6]);
      const v2u ta = swap16(a.x, d.x), tb = swap16(a.y, d.y);
      const int l = 3 * (qj >> 2) + (qj & 3);
      st128(c, v4u{ta.x, tb.x, ta.y, tb.y}, (g & 1) * 55296 + (g >> 1) * 256 + n16 * 16, fb + (48 + (l >> 1)) * 1024 + (l & 1) * 512);
    }
  }
  if (Q4 >= 2) ctl_addc<C_ISS4>();
  STAMP(14);

  {
    const int xs = OFF_F + mtB * 55296, xm = kb ? -55296 : 55296;
    static_assert(NSC >= 1 && NSC <= 7, "covered by the first landing check");
    v4u sw[5 * NSC];
    ring_wait<upto(CH_D)>(c);
    static_for<0, 5 * NSC>([&](auto ic) AI {
      constexpr int f = decltype(ic)::v;
      sw[f] = *LP(v4u, xvoff + kb * 5120 + slot(CH_D + f / 5) * 10240 + (f % 5) * 1024);
    });
#pragma unroll
    for (int f = 0; f < 5 * NSC; ++f) PIN(sw[f]);
    if (SREL == 0) { release<CH_D + NSC>(c); VMWAIT(1); } else DRAIN();
    arrive<C_ARR4B>(c, FL4B + qi * 16 + qj);
    if (SREL == 1) release<CH_D + NSC>(c);
    STAMP(44);
    ctl_wait(c, C_GO4A, 1);
    v4u xf[2][20];
    xload<20, 0, 0, PRE_D, 8>(c, xf, xvoff, xs + kb * 16384, xm, rotD1);
    f32x4 acc[2][5];
#pragma unroll
    for (int t = 0; t < 5; ++t) { acc[0][t] = unpk4(hq[2 * t], hq[2 * t + 1]); PIN(acc[0][t]); }
    STAMP(16);
    gemm_pass<5, 27, CH_D, 5, 20, 2, false>(c, dn, xvoff + kb * 5120, acc, xf, sw, [&](auto ic) AI {
      constexpr int f = decltype(ic)::v, sn = f / 5 + PRE_D;
      static_assert(PRE_D <= 8 && G4F % 5 == 1 && G4F / 5 + PRE_D >= 8 && G4F < 36 && G4F / 5 + PRE_D < 16, "order");
      if constexpr (f >= 0 && f % 5 == 1 && sn < 27) {
        if constexpr (sn < 8) xload<20, 0, sn, 1, 8>(c, xf, xvoff, xs + kb * 16384, xm, rotD1);
        else if constexpr (f == G4F) {
          STAMP(17);
          ctl_wait(c, C_GO4B, 1);
          STAMP(18);
          xload<20, 8, 0, sn - 7, 8>(c, xf, xvoff, xs + kb * 16384 + 8192, xm, rotD1);
        } else if constexpr (f > G4F) {
          if constexpr (sn < 16) xload<20, 8, sn - 8, 1, 8>(c, xf, xvoff, xs + kb * 16384 + 8192, xm, rotD1);
          else xload<20, 16, sn - 16, 1, 11>(c, xf, xvoff, xs + 32768 + kb * 11264, xm, rotD2);
        }
      }
    });
    STAMP(19);
    unsigned y[10];
    pair_reduce(c, w, C_XD0, 0, acc, y);
    const int l2 = opaque(lane);
    char* ob = out + (size_t)(16 * mtB * 1280 + cc * 160);
    const unsigned ro = (l2 & 15) * 1280 + (l2 & 16) * 2 + (l2 & 32) / 2;
#pragma unroll
    for (int u = 0; u < 2; ++u) {
      const v2u a = swap16(y[4 * u], y[4 * u + 2]), d = swap16(y[4 * u + 1], y[4 * u + 3]);
      *(GAS v4u*)(ob + ro + 64 * u) = v4u{a.x, d.x, a.y, d.y};
    }
    *(GAS v2u*)(ob + ((l2 & 15) * 1280 + (l2 >> 4) * 8 + 128)) = v2u{y[8], y[9]};
  }
  STAMP(15);
}

extern "C" void kernel_launch(void* const* d_in, const int* in_sizes, int n_in, void* d_out, int out_size, void* d_ws, size_t ws_size,
                              hipStream_t stream) {
  (void)hipFuncSetAttribute((const void*)mk_kernel, hipFuncAttributeMaxDynamicSharedMemorySize, LDS_BYTES);
  mk_kernel<<<256, 512, LDS_BYTES, stream>>>((const char*)d_in[0], (const char*)d_in[1], (const char*)d_in[2], (const char*)d_in[4],
                                             (const char*)d_in[5], (char*)d_out, (char*)d_ws);
}
```
